```python
import torch

def _softmax(x, axis=-1):
    return torch.softmax(x, dim=axis)
def _gelu(x):
    return torch.nn.functional.gelu(x, approximate="tanh")
def _silu(x):
    return torch.nn.functional.silu(x)
def _sigmoid(x):
    return torch.sigmoid(x)
def _layernorm(x, axis=-1, eps=1e-5):
    m = x.mean(dim=axis, keepdim=True)
    v = x.var(dim=axis, keepdim=True, unbiased=False)
    return (x - m) / torch.sqrt(v + eps)
def _scatter_add(tgt, idx, src, axis=0):
    return tgt.clone().index_add_(axis, idx, src)
def _segment_sum(src, seg, n):
    out = torch.zeros((n,) + src.shape[1:], dtype=src.dtype)
    return out.index_add_(0, seg, src)
def _pad(x, pw):
    flat = tuple(v for pair in reversed(pw) for v in pair)
    return torch.nn.functional.pad(x, flat)

kernel_name = 'standalone__hybrid_graph_tf_exphormer__graph'

def setup_inputs():

    _w = (lambda a: (lambda t: t.to(torch.bfloat16) if t.is_floating_point() else t)(torch.as_tensor(a)))
    N, deg, D = 10000, 16, 256
    E = N * deg
    exp_deg = 4
    Ee = N * exp_deg
    src = (lambda lo, hi, *s: torch.randint(lo, hi, s))(0, N, E).to(torch.int64)
    dst = (lambda lo, hi, *s: torch.randint(lo, hi, s))(0, N, E).to(torch.int64)
    esrc = (lambda lo, hi, *s: torch.randint(lo, hi, s))(0, N, Ee).to(torch.int64)
    edst = torch.repeat_interleave(torch.arange(N), exp_deg).to(torch.int64)
    deg_cnt = torch.bincount(dst, minlength=N).to(float) + 1.0
    return {
        "x": _w((lambda *s: torch.randn(*s, dtype=torch.bfloat16) * 0.05)(N, D)),
        "src": _w(src),
        "dst": _w(dst),
        "esrc": _w(esrc),
        "edst": _w(edst),
        "deg_inv_sqrt": _w(1.0 / (lambda _x: torch.sqrt(torch.as_tensor(_x)))(deg_cnt)),
        "w_l": _w((lambda *s: torch.randn(*s, dtype=torch.bfloat16) * 0.05)(D, D)),
        "wq": _w((lambda *s: torch.randn(*s, dtype=torch.bfloat16) * 0.05)(D, D)),
        "wk": _w((lambda *s: torch.randn(*s, dtype=torch.bfloat16) * 0.05)(D, D)),
        "wv": _w((lambda *s: torch.randn(*s, dtype=torch.bfloat16) * 0.05)(D, D)),
    }

def reference(x, src, dst, esrc, edst, deg_inv_sqrt, w_l, wq, wk, wv):
    N, D = x.shape
    h = x @ w_l
    msg = h[src] * deg_inv_sqrt[src, None] * deg_inv_sqrt[dst, None]
    h_local = _segment_sum(msg, dst, N)
    q = x @ wq
    k = x @ wk
    v = x @ wv
    e = (q[edst] * k[esrc]).sum(-1) * (D ** -0.5)
    exp_e = torch.exp(e)
    denom = _segment_sum(exp_e[:, None], edst, N)[:, 0] + 1e-9
    alpha = exp_e / denom[edst]
    h_attn = _segment_sum(alpha[:, None] * v[esrc], edst, N)
    return h_local + h_attn
```

```cpp
#include <hip/hip_runtime.h>
#include <hip/hip_bf16.h>
#include <stdint.h>

using f32x4 = __attribute__((ext_vector_type(4))) float;
using f32x2 = __attribute__((ext_vector_type(2))) float;
using u32x2 = __attribute__((ext_vector_type(2))) unsigned;
using bf16x2 = __attribute__((ext_vector_type(2))) __bf16;

constexpr int D = 256;
constexpr int TN = 40;
constexpr int L_TOT = 160 * 1024;
constexpr size_t WS_BK = 466944;

__device__ __forceinline__ float bf2f(uint32_t h) { return __uint_as_float(h << 16); }
__device__ __forceinline__ uint32_t pack2(float lo, float hi) {
    return __builtin_bit_cast(uint32_t, __builtin_convertvector(f32x2{lo, hi}, bf16x2));
}
__device__ __forceinline__ uint16_t f2bf(float f) { return (uint16_t)pack2(f, 0.f); }
__device__ __forceinline__ f32x4 wide(const u32x2 r) {
    return f32x4{bf2f(r[0] & 0xffffu), __uint_as_float(r[0] & 0xffff0000u), bf2f(r[1] & 0xffffu), __uint_as_float(r[1] & 0xffff0000u)};
}

#define BUF(type, name) type* name, uint32_t name##_bytes, uint32_t name##_flags
#define ARG(type, name) type* name; uint32_t name##_bytes, name##_flags;
struct Args {
    ARG(const uint16_t, x) ARG(const uint16_t, dis) ARG(const uint16_t, wl) ARG(const uint16_t, wv)
    ARG(const uint16_t, wq) ARG(const uint16_t, wk) ARG(const int, src) ARG(const int, dst)
    ARG(const int, esrc) ARG(const int, edst) ARG(char, ws) ARG(uint16_t, out)
    int N, E, Ee, fast, T, spare;
    void* stamps;
};
__global__ __launch_bounds__(512, 2) void main_kernel(
    BUF(const uint16_t, x_), BUF(const uint16_t, dis_), BUF(const uint16_t, wl_), BUF(const uint16_t, wv_),
    BUF(const uint16_t, wq_), BUF(const uint16_t, wk_), BUF(const int, src_), BUF(const int, dst_),
    BUF(const int, esrc_), BUF(const int, edst_), BUF(char, ws_), BUF(uint16_t, out_),
    int N_, int E_, int Ee_, int fast_, int T_, int spare_, void* stamps_) {
    extern __shared__ __attribute__((aligned(16))) char smem[];
    int wave, tile = blockIdx.x;
    unsigned tx = threadIdx.x;
    auto kp = (const __attribute__((address_space(4))) Args*)__builtin_amdgcn_kernarg_segment_ptr();
    asm volatile(
        ".Lcode:\n"
        "s_load_dwordx16 s[4:19], s[0:1], 0x0\n"
        "s_load_dwordx16 s[20:35], s[0:1], 0x40\n"
        "s_load_dwordx16 s[36:51], s[0:1], 0x80\n"
        "s_load_dwordx8 s[52:59], s[0:1], 0xc0\n"
        "s_getpc_b64 s[60:61]\n"
        ".Lpc:\n"
        "s_sub_u32 s60, s60, .Lpc-.Lcode\n"
        "s_subb_u32 s61, s61, 0\n"
        "s_mov_b32 s62, .Lcode_end-.Lcode\n"
        "s_mov_b32 s63, 0x20000\n"
        "s_mov_b32 m0, 23808\n"
        "v_lshlrev_b32 v9, 7, v0\n"
        "buffer_load_dword v9, s[60:63], 0 offen lds\n"
        "v_lshrrev_b32 v2, 6, v0\n"
        "s_nop 0\n"
        "v_readfirstlane_b32 s3, v2\n"
        "v_and_b32 v1, 63, v0\n"
        "v_mov_b32 v3, 1\n"
        "v_lshlrev_b32 v4, 4, v1\n"
        "v_lshlrev_b32 v8, 2, v1\n"
        "v_add_u32 v2, 0x80, v8\n"
        "v_writelane_b32 v2, 0, 0\n"
        "s_and_b32 s72, s3, 3\n"
        "s_add_u32 s83, s2, 128\n"
        "s_sub_u32 s73, s2, 64\n"
        "s_cselect_b32 s83, s83, s73\n"
        "s_cmp_ge_u32 s2, 192\n"
        "s_cselect_b32 s83, 255, s83\n"
        "s_cmp_ge_u32 s2, 254\n"
        "s_cselect_b32 s73, 56, 58\n"
        "s_sub_u32 s73, s2, s73\n"
        "s_cmp_ge_u32 s2, 250\n"
        "s_cselect_b32 s83, s73, s83\n"
        "s_cmp_ge_u32 s3, 4\n"
        "s_cbranch_scc1 .Lpre_a\n"
        "v_mov_b32 v9, 0\n"
        "s_cmp_lg_u32 s3, 0\n"
        "s_cbranch_scc1 .Lpre_1\n"
        "ds_write_b32 v8, v9 offset:45440\n"
        ".Lpre_1:\n"
        "s_setprio 3\n"
        "s_cmp_ge_u32 s83, 128\n"
        "s_cbranch_scc1 .Lpre_m\n"
        "v_lshrrev_b32 v6, 2, v1\n"
        "v_lshl_add_u32 v6, v6, 2, s72\n"
        "v_and_b32 v7, 48, v4\n"
        "v_lshl_or_b32 v5, v6, 13, v7\n"
        "s_lshr_b32 s73, s83, 4\n"
        "s_lshl_b32 s73, s73, 10\n"
        "s_and_b32 s74, s83, 7\n"
        "s_lshl_b32 s74, s74, 7\n"
        "s_add_u32 s73, s73, s74\n"
        "s_bfe_u32 s74, s83, 0x10003\n"
        "s_lshl_b32 s74, s74, 6\n"
        "s_add_u32 s73, s73, s74\n"
        "s_add_u32 s74, s73, 0x80000\n"
        "s_waitcnt lgkmcnt(0)\n"
        "s_load_dwordx2 s[64:65], s[44:45], 0x0\n"
        "buffer_load_dwordx4 v[8:11], v5, s[32:35], s73 offen\n"
        "buffer_load_dwordx4 v[12:15], v5, s[28:31], s73 offen\n"
        "buffer_load_dwordx4 v[16:19], v5, s[32:35], s74 offen\n"
        "buffer_load_dwordx4 v[20:23], v5, s[28:31], s74 offen\n"
        "s_branch .Lpre_done\n"
        ".Lpre_m:\n"
        "s_cmp_ge_u32 s83, 192\n"
        "s_cbranch_scc1 .Lpre_w\n"
        "v_bfe_u32 v5, v0, 5, 3\n"
        "v_and_b32 v7, 31, v0\n"
        "v_xor_b32 v7, v7, v5\n"
        "v_lshlrev_b32 v5, 9, v5\n"
        "v_lshl_or_b32 v6, v7, 4, v5\n"
        "v_xor_b32 v7, 0x80, v6\n"
        "s_sub_u32 s73, s83, 128\n"
        "s_lshr_b32 s77, s73, 2\n"
        "s_lshl_b32 s77, s77, 13\n"
        "s_and_b32 s78, s73, 3\n"
        "s_lshl_b32 s78, s78, 15\n"
        "s_lshl_b32 s76, s72, 10\n"
        "s_add_u32 m0, s76, 0\n"
        "s_waitcnt lgkmcnt(0)\n"
        "s_load_dwordx2 s[64:65], s[44:45], 0x0\n"
        "buffer_load_dwordx4 v6, s[20:23], s77 offen lds\n"
        "s_add_u32 m0, m0, 4096\n"
        "s_add_u32 s77, s77, 0x1000\n"
        "buffer_load_dwordx4 v7, s[20:23], s77 offen lds\n"
        "s_add_u32 m0, s76, 58368\n"
        "s_nop 0\n"
        "buffer_load_dwordx4 v6, s[24:27], s78 offen lds\n"
        "s_add_u32 m0, m0, 4096\n"
        "s_add_u32 s78, s78, 0x1000\n"
        "buffer_load_dwordx4 v7, s[24:27], s78 offen lds\n"
        "s_add_u32 m0, m0, 16384\n"
        "s_add_u32 s78, s78, 0x1000\n"
        "buffer_load_dwordx4 v6, s[24:27], s78 offen lds\n"
        "s_add_u32 m0, m0, 4096\n"
        "s_add_u32 s78, s78, 0x1000\n"
        "buffer_load_dwordx4 v7, s[24:27], s78 offen lds\n"
        "s_add_u32 m0, m0, 16384\n"
        "s_add_u32 s78, s78, 0x1000\n"
        "buffer_load_dwordx4 v6, s[24:27], s78 offen lds\n"
        "s_add_u32 m0, m0, 4096\n"
        "s_add_u32 s78, s78, 0x1000\n"
        "buffer_load_dwordx4 v7, s[24:27], s78 offen lds\n"
        "s_add_u32 m0, m0, 16384\n"
        "s_add_u32 s78, s78, 0x1000\n"
        "buffer_load_dwordx4 v6, s[24:27], s78 offen lds\n"
        "s_add_u32 m0, m0, 4096\n"
        "s_add_u32 s78, s78, 0x1000\n"
        "buffer_load_dwordx4 v7, s[24:27], s78 offen lds\n"
        "s_branch .Lpre_done\n"
        ".Lpre_w:\n"
        "v_bfe_u32 v6, v1, 3, 1\n"
        "v_lshlrev_b32 v6, 14, v6\n"
        "v_lshrrev_b32 v7, 4, v1\n"
        "v_lshl_or_b32 v6, v7, 12, v6\n"
        "v_and_b32 v7, 1, v1\n"
        "v_lshl_or_b32 v6, v7, 6, v6\n"
        "v_and_b32 v7, 4, v1\n"
        "v_lshl_or_b32 v6, v7, 3, v6\n"
        "v_and_b32 v7, 2, v1\n"
        "v_lshl_or_b32 v6, v7, 3, v6\n"
        "s_lshl_b32 s73, s72, 7\n"
        "v_add_u32 v5, s73, v6\n"
        "s_and_b32 s77, s83, 3\n"
        "s_lshl_b32 s77, s77, 15\n"
        "s_waitcnt lgkmcnt(0)\n"
        "s_load_dwordx2 s[64:65], s[44:45], 0x0\n"
        "s_cmp_ge_u32 s83, 200\n"
        "s_cbranch_scc1 .Lpre_done\n"
        "s_bitcmp1_b32 s83, 2\n"
        "s_cselect_b64 s[12:13], s[16:17], s[12:13]\n"
        "buffer_load_dwordx4 v[76:79], v5, s[12:15], s77 offen offset:0\n"
        "buffer_load_dwordx4 v[80:83], v5, s[12:15], s77 offen offset:512\n"
        "buffer_load_dwordx4 v[84:87], v5, s[12:15], s77 offen offset:1024\n"
        "buffer_load_dwordx4 v[88:91], v5, s[12:15], s77 offen offset:1536\n"
        "buffer_load_dwordx4 v[92:95], v5, s[12:15], s77 offen offset:2048\n"
        "buffer_load_dwordx4 v[96:99], v5, s[12:15], s77 offen offset:2560\n"
        "buffer_load_dwordx4 v[100:103], v5, s[12:15], s77 offen offset:3072\n"
        "buffer_load_dwordx4 v[104:107], v5, s[12:15], s77 offen offset:3584\n"
        ".Lpre_done:\n"
        "s_barrier\n"
        "s_bitcmp0_b32 s55, 0\n"
        "s_cbranch_scc1 .Lend_l\n"
        "s_memrealtime s[62:63]\n"
        "s_cmp_ge_u32 s83, 128\n"
        "s_cbranch_scc1 .Lno_sort\n"
        "s_lshl_b32 s78, s53, 2\n"
        "s_sub_i32 s80, s78, s74\n"
        "s_sub_i32 s78, s78, s73\n"
        "v_cmp_gt_i32 s[78:79], s78, v5\n"
        "v_cmp_gt_i32 s[80:81], s80, v5\n"
        "s_lshl_b32 s82, s83, 2\n"
        "s_lshl_b32 s75, s83, 4\n"
        "s_xor_b32 s82, s82, s75\n"
        "s_or_b32 s82, s82, 48\n"
        "s_and_b32 s82, s82, 0xfc\n"
        "v_mov_b32 v52, s82\n"
        "v_mov_b32 v53, s82\n"
        "v_mov_b32 v54, s82\n"
        "v_mov_b32 v55, s82\n"
        "v_lshlrev_b32 v56, 4, v0\n"
        "v_add_u32 v56, 127744, v56\n"
        "ds_write_b128 v56, v[52:55] offset:0\n"
        "ds_write_b128 v56, v[52:55] offset:4096\n"
        "ds_write_b128 v56, v[52:55] offset:8192\n"
        "ds_write_b128 v56, v[52:55] offset:12288\n"
        "ds_write_b128 v56, v[52:55] offset:16384\n"
        "ds_write_b128 v56, v[52:55] offset:20480\n"
        "ds_write_b128 v56, v[52:55] offset:24576\n"
        "ds_write_b128 v56, v[52:55] offset:28672\n"
        "ds_write_b128 v56, v[52:55] offset:32768\n"
        "ds_add_u32 v2, v3 offset:45524\n"
        "s_waitcnt lgkmcnt(0)\n"
        "s_add_u32 s73, s64, 1\n"
        "s_addc_u32 s74, s65, 0\n"
        "s_mul_i32 s76, s73, 0x7f4a7c15\n"
        "s_mul_hi_u32 s77, s73, 0x7f4a7c15\n"
        "s_mul_i32 s75, s73, 0x9e3779b9\n"
        "s_add_u32 s77, s77, s75\n"
        "s_mul_i32 s75, s74, 0x7f4a7c15\n"
        "s_add_u32 s77, s77, s75\n"
        "s_xor_b32 s66, s76, 0xd192ed03\n"
        "s_or_b32 s66, s66, 3\n"
        "s_xor_b32 s67, s77, 0xd1b54a32\n"
        "s_xor_b32 s76, s76, 0x4f6cdd1d\n"
        "s_xor_b32 s77, s77, 0x2545f491\n"
        "s_and_b32 s73, s76, 3\n"
        "s_lshr_b32 s74, s76, 2\n"
        "s_lshl_b32 s74, s74, 23\n"
        "s_or_b32 s68, s73, s74\n"
        "s_lshr_b64 s[76:77], s[76:77], 11\n"
        "s_and_b32 s73, s76, 3\n"
        "s_lshr_b32 s74, s76, 2\n"
        "s_lshl_b32 s74, s74, 23\n"
        "s_or_b32 s69, s73, s74\n"
        "s_lshr_b64 s[76:77], s[76:77], 11\n"
        "s_and_b32 s73, s76, 3\n"
        "s_lshr_b32 s74, s76, 2\n"
        "s_lshl_b32 s74, s74, 23\n"
        "s_or_b32 s70, s73, s74\n"
        "s_lshr_b64 s[76:77], s[76:77], 11\n"
        "s_and_b32 s73, s76, 3\n"
        "s_lshr_b32 s74, s76, 2\n"
        "s_lshl_b32 s74, s74, 23\n"
        "s_or_b32 s71, s73, s74\n"
        "ds_add_u32 v2, v3 offset:45480\n"
        "v_mov_b32 v52, 0x200\n"
        "s_mov_b32 s73, 52429\n"
        "s_movk_i32 s74, 62936\n"
        "s_movk_i32 s75, 144\n"
        "v_mov_b32 v53, 127744\n"
        "s_movk_i32 s82, 0xfc\n"
        "s_waitcnt vmcnt(3)\n"
        "v_mul_u32_u24 v40, s73, v8\n"
        "v_lshrrev_b32 v40, 21, v40\n"
        "v_mad_u32_u24 v24, v40, s75, v53\n"
        "v_mul_u32_u24 v41, s73, v9\n"
        "v_lshrrev_b32 v41, 21, v41\n"
        "v_mad_u32_u24 v25, v41, s75, v53\n"
        "v_mul_u32_u24 v42, s73, v10\n"
        "v_lshrrev_b32 v42, 21, v42\n"
        "v_mad_u32_u24 v26, v42, s75, v53\n"
        "v_mul_u32_u24 v43, s73, v11\n"
        "v_lshrrev_b32 v43, 21, v43\n"
        "v_mad_u32_u24 v27, v43, s75, v53\n"
        "s_waitcnt vmcnt(1)\n"
        "v_mul_u32_u24 v44, s73, v16\n"
        "v_lshrrev_b32 v44, 21, v44\n"
        "v_mad_u32_u24 v28, v44, s75, v53\n"
        "v_mul_u32_u24 v45, s73, v17\n"
        "v_lshrrev_b32 v45, 21, v45\n"
        "v_mad_u32_u24 v29, v45, s75, v53\n"
        "v_mul_u32_u24 v46, s73, v18\n"
        "v_lshrrev_b32 v46, 21, v46\n"
        "v_mad_u32_u24 v30, v46, s75, v53\n"
        "v_mul_u32_u24 v47, s73, v19\n"
        "v_lshrrev_b32 v47, 21, v47\n"
        "v_mad_u32_u24 v31, v47, s75, v53\n"
        "s_add_u32 s61, s72, 4\n"
        "s_mov_b32 s77, 0x40000\n"
        ".Lt_1:\n"
        "ds_read_b32 v54, v2 offset:45524\n"
        "s_sub_u32 s77, s77, 1\n"
        "s_waitcnt lgkmcnt(0)\n"
        "s_cbranch_scc1 .Lte_2\n"
        "v_readfirstlane_b32 s76, v54\n"
        "s_cmp_lt_u32 s76, s61\n"
        "s_cbranch_scc1 .Lt_1\n"
        ".Lte_2:\n"
        "s_mov_b64 exec, s[78:79]\n"
        "ds_add_rtn_u32 v32, v24, v52\n"
        "ds_add_rtn_u32 v33, v25, v52\n"
        "ds_add_rtn_u32 v34, v26, v52\n"
        "ds_add_rtn_u32 v35, v27, v52\n"
        "s_mov_b64 exec, s[80:81]\n"
        "ds_add_rtn_u32 v36, v28, v52\n"
        "ds_add_rtn_u32 v37, v29, v52\n"
        "ds_add_rtn_u32 v38, v30, v52\n"
        "ds_add_rtn_u32 v39, v31, v52\n"
        "s_mov_b64 exec, -1\n"
        "ds_add_u32 v2, v3 offset:45524\n"
        "s_waitcnt vmcnt(0)\n"
        "v_lshl_add_u32 v8, v8, 6, v8\n"
        "v_mad_i32_i24 v40, v40, s74, v8\n"
        "v_lshlrev_b32 v12, 9, v12\n"
        "v_and_or_b32 v40, v40, s82, v12\n"
        "v_lshl_add_u32 v9, v9, 6, v9\n"
        "v_mad_i32_i24 v41, v41, s74, v9\n"
        "v_lshlrev_b32 v13, 9, v13\n"
        "v_and_or_b32 v41, v41, s82, v13\n"
        "v_lshl_add_u32 v10, v10, 6, v10\n"
        "v_mad_i32_i24 v42, v42, s74, v10\n"
        "v_lshlrev_b32 v14, 9, v14\n"
        "v_and_or_b32 v42, v42, s82, v14\n"
        "v_lshl_add_u32 v11, v11, 6, v11\n"
        "v_mad_i32_i24 v43, v43, s74, v11\n"
        "v_lshlrev_b32 v15, 9, v15\n"
        "v_and_or_b32 v43, v43, s82, v15\n"
        "v_lshl_add_u32 v16, v16, 6, v16\n"
        "v_mad_i32_i24 v44, v44, s74, v16\n"
        "v_lshlrev_b32 v20, 9, v20\n"
        "v_and_or_b32 v44, v44, s82, v20\n"
        "v_lshl_add_u32 v17, v17, 6, v17\n"
        "v_mad_i32_i24 v45, v45, s74, v17\n"
        "v_lshlrev_b32 v21, 9, v21\n"
        "v_and_or_b32 v45, v45, s82, v21\n"
        "v_lshl_add_u32 v18, v18, 6, v18\n"
        "v_mad_i32_i24 v46, v46, s74, v18\n"
        "v_lshlrev_b32 v22, 9, v22\n"
        "v_and_or_b32 v46, v46, s82, v22\n"
        "v_lshl_add_u32 v19, v19, 6, v19\n"
        "v_mad_i32_i24 v47, v47, s74, v19\n"
        "v_lshlrev_b32 v23, 9, v23\n"
        "v_and_or_b32 v47, v47, s82, v23\n"
        "s_mov_b64 exec, s[78:79]\n"
        "s_waitcnt lgkmcnt(8)\n"
        "v_lshrrev_b32 v32, 9, v32\n"
        "v_min_u32 v32, 31, v32\n"
        "v_lshl_add_u32 v24, v32, 2, v24\n"
        "ds_write_b32 v24, v40 offset:4\n"
        "s_waitcnt lgkmcnt(8)\n"
        "v_lshrrev_b32 v33, 9, v33\n"
        "v_min_u32 v33, 31, v33\n"
        "v_lshl_add_u32 v25, v33, 2, v25\n"
        "ds_write_b32 v25, v41 offset:4\n"
        "s_waitcnt lgkmcnt(8)\n"
        "v_lshrrev_b32 v34, 9, v34\n"
        "v_min_u32 v34, 31, v34\n"
        "v_lshl_add_u32 v26, v34, 2, v26\n"
        "ds_write_b32 v26, v42 offset:4\n"
        "s_waitcnt lgkmcnt(8)\n"
        "v_lshrrev_b32 v35, 9, v35\n"
        "v_min_u32 v35, 31, v35\n"
        "v_lshl_add_u32 v27, v35, 2, v27\n"
        "ds_write_b32 v27, v43 offset:4\n"
        "s_mov_b64 exec, s[80:81]\n"
        "s_waitcnt lgkmcnt(8)\n"
        "v_lshrrev_b32 v36, 9, v36\n"
        "v_min_u32 v36, 31, v36\n"
        "v_lshl_add_u32 v28, v36, 2, v28\n"
        "ds_write_b32 v28, v44 offset:4\n"
        "s_waitcnt lgkmcnt(8)\n"
        "v_lshrrev_b32 v37, 9, v37\n"
        "v_min_u32 v37, 31, v37\n"
        "v_lshl_add_u32 v29, v37, 2, v29\n"
        "ds_write_b32 v29, v45 offset:4\n"
        "s_waitcnt lgkmcnt(8)\n"
        "v_lshrrev_b32 v38, 9, v38\n"
        "v_min_u32 v38, 31, v38\n"
        "v_lshl_add_u32 v30, v38, 2, v30\n"
        "ds_write_b32 v30, v46 offset:4\n"
        "s_waitcnt lgkmcnt(8)\n"
        "v_lshrrev_b32 v39, 9, v39\n"
        "v_min_u32 v39, 31, v39\n"
        "v_lshl_add_u32 v31, v39, 2, v31\n"
        "ds_write_b32 v31, v47 offset:4\n"
        "s_mov_b64 exec, -1\n"
        "ds_add_u32 v2, v3 offset:45500\n"
        "v_lshrrev_b32 v8, 2, v0\n"
        "v_bfe_u32 v10, v8, 1, 3\n"
        "v_and_or_b32 v10, v8, 48, v10\n"
        "v_and_b32 v8, 1, v8\n"
        "v_lshl_or_b32 v8, v8, 3, v10\n"
        "v_and_b32 v9, 48, v4\n"
        "v_mul_u32_u24 v10, 144, v8\n"
        "v_add3_u32 v10, v10, v9, v53\n"
        "v_lshlrev_b32 v11, 4, v0\n"
        "v_cmp_eq_u32 s[78:79], 0, v9\n"
        "v_mov_b32 v13, 16424\n"
        "v_mov_b32 v12, 0x2000\n"
        "v_cndmask_b32 v13, -1, v13, s[78:79]\n"
        "v_cndmask_b32 v12, -1, v12, s[78:79]\n"
        "s_mul_i32 s75, s83, 32768\n"
        "s_add_u32 s75, s75, 466944\n"
        "s_mov_b64 s[78:79], 0\n"
        "ds_read_b32 v15, v2 offset:45500\n"
        "s_waitcnt lgkmcnt(0)\n"
        "v_readfirstlane_b32 s73, v15\n"
        "s_cmp_ge_u32 s73, 4\n"
        "s_cbranch_scc0 .Lw_3\n"
        ".Le_4:\n"
        "ds_read_b128 v[16:19], v10 offset:0\n"
        "ds_read_b128 v[20:23], v10 offset:9216\n"
        "ds_read_b128 v[24:27], v10 offset:18432\n"
        "ds_read_b128 v[28:31], v10 offset:27648\n"
        "s_waitcnt lgkmcnt(3)\n"
        "v_min_u32 v16, v16, v13\n"
        "v_cmp_ge_u32 vcc, v16, v12\n"
        "v_or_b32 v16, s68, v16\n"
        "v_or_b32 v17, s69, v17\n"
        "v_or_b32 v18, s70, v18\n"
        "v_or_b32 v19, s71, v19\n"
        "buffer_store_dwordx4 v[16:19], v11, s[44:47], s75 offen sc1\n"
        "s_or_b64 s[78:79], s[78:79], vcc\n"
        "s_add_u32 s75, s75, 4096\n"
        "s_waitcnt lgkmcnt(2)\n"
        "v_min_u32 v20, v20, v13\n"
        "v_cmp_ge_u32 vcc, v20, v12\n"
        "v_or_b32 v20, s68, v20\n"
        "v_or_b32 v21, s69, v21\n"
        "v_or_b32 v22, s70, v22\n"
        "v_or_b32 v23, s71, v23\n"
        "buffer_store_dwordx4 v[20:23], v11, s[44:47], s75 offen sc1\n"
        "s_or_b64 s[78:79], s[78:79], vcc\n"
        "s_add_u32 s75, s75, 4096\n"
        "s_waitcnt lgkmcnt(1)\n"
        "v_min_u32 v24, v24, v13\n"
        "v_cmp_ge_u32 vcc, v24, v12\n"
        "v_or_b32 v24, s68, v24\n"
        "v_or_b32 v25, s69, v25\n"
        "v_or_b32 v26, s70, v26\n"
        "v_or_b32 v27, s71, v27\n"
        "buffer_store_dwordx4 v[24:27], v11, s[44:47], s75 offen sc1\n"
        "s_or_b64 s[78:79], s[78:79], vcc\n"
        "s_add_u32 s75, s75, 4096\n"
        "s_waitcnt lgkmcnt(0)\n"
        "v_min_u32 v28, v28, v13\n"
        "v_cmp_ge_u32 vcc, v28, v12\n"
        "v_or_b32 v28, s68, v28\n"
        "v_or_b32 v29, s69, v29\n"
        "v_or_b32 v30, s70, v30\n"
        "v_or_b32 v31, s71, v31\n"
        "buffer_store_dwordx4 v[28:31], v11, s[44:47], s75 offen sc1\n"
        "s_or_b64 s[78:79], s[78:79], vcc\n"
        "s_add_u32 s75, s75, 4096\n"
        "s_cmp_lg_u64 s[78:79], 0\n"
        "s_cbranch_scc1 .Lpub_more\n"
        ".Lpub_done:\n"
        "s_branch .Lspecial_done\n"
        ".Lno_sort:\n"
        "s_cmp_ge_u32 s83, 192\n"
        "s_cbranch_scc1 .Lno_m\n"
        "v_and_b32 v112, 15, v1\n"
        "v_lshrrev_b32 v113, 4, v1\n"
        "v_xor_b32 v113, v113, v112\n"
        "v_and_b32 v113, 3, v113\n"
        "v_and_or_b32 v113, v1, 12, v113\n"
        "v_lshlrev_b32 v112, 9, v112\n"
        "v_lshl_add_u32 v108, v113, 4, v112\n"
        "v_add_u32 v108, 0, v108\n"
        "v_xor_b32 v109, 64, v108\n"
        "v_xor_b32 v110, 128, v108\n"
        "v_xor_b32 v111, 192, v108\n"
        "s_mul_i32 s76, s3, 20480\n"
        "s_add_u32 s76, s76, 58368\n"
        "v_add_u32 v124, s76, v108\n"
        "v_add_u32 v125, s76, v109\n"
        "v_add_u32 v126, s76, v110\n"
        "v_add_u32 v127, s76, v111\n"
        "v_bfe_u32 v112, v1, 2, 1\n"
        "v_lshlrev_b32 v112, 13, v112\n"
        "v_bfe_u32 v113, v1, 5, 1\n"
        "v_lshl_or_b32 v112, v113, 8, v112\n"
        "v_bfe_u32 v113, v1, 3, 1\n"
        "v_lshl_or_b32 v112, v113, 6, v112\n"
        "v_and_b32 v113, 3, v1\n"
        "v_lshl_or_b32 v112, v113, 4, v112\n"
        "v_bfe_u32 v113, v1, 4, 1\n"
        "v_lshl_or_b32 v112, v113, 3, v112\n"
        "s_sub_u32 s80, s83, 128\n"
        "s_lshr_b32 s81, s80, 2\n"
        "s_and_b32 s80, s80, 3\n"
        "s_lshl_b32 s80, s80, 2\n"
        "s_add_u32 s80, s80, s3\n"
        "s_lshr_b32 s79, s80, 1\n"
        "s_lshl_b32 s79, s79, 14\n"
        "s_lshl_b32 s78, s81, 9\n"
        "s_add_u32 s79, s79, s78\n"
        "s_and_b32 s78, s80, 1\n"
        "s_lshl_b32 s78, s78, 7\n"
        "s_add_u32 s79, s79, s78\n"
        "s_add_u32 s79, s79, 8192\n"
        "s_lshl_b32 s80, s80, 7\n"
        "s_lshl_b32 s81, s81, 3\n"
        "s_add_u32 s78, s80, s81\n"
        "s_add_u32 s78, s78, 2304\n"
        "s_waitcnt lgkmcnt(0)\n"
        "s_add_u32 s73, s64, 1\n"
        "s_addc_u32 s74, s65, 0\n"
        "s_mul_i32 s76, s73, 0x7f4a7c15\n"
        "s_mul_hi_u32 s77, s73, 0x7f4a7c15\n"
        "s_mul_i32 s75, s73, 0x9e3779b9\n"
        "s_add_u32 s77, s77, s75\n"
        "s_mul_i32 s75, s74, 0x7f4a7c15\n"
        "s_add_u32 s77, s77, s75\n"
        "s_xor_b32 s66, s76, 0xd192ed03\n"
        "s_or_b32 s66, s66, 3\n"
        "s_xor_b32 s67, s77, 0xd1b54a32\n"
        "s_xor_b32 s76, s76, 0x4f6cdd1d\n"
        "s_xor_b32 s77, s77, 0x2545f491\n"
        "s_and_b32 s73, s76, 3\n"
        "s_lshr_b32 s74, s76, 2\n"
        "s_lshl_b32 s74, s74, 23\n"
        "s_or_b32 s68, s73, s74\n"
        "s_lshr_b64 s[76:77], s[76:77], 11\n"
        "s_and_b32 s73, s76, 3\n"
        "s_lshr_b32 s74, s76, 2\n"
        "s_lshl_b32 s74, s74, 23\n"
        "s_or_b32 s69, s73, s74\n"
        "s_lshr_b64 s[76:77], s[76:77], 11\n"
        "s_and_b32 s73, s76, 3\n"
        "s_lshr_b32 s74, s76, 2\n"
        "s_lshl_b32 s74, s74, 23\n"
        "s_or_b32 s70, s73, s74\n"
        "s_lshr_b64 s[76:77], s[76:77], 11\n"
        "s_and_b32 s73, s76, 3\n"
        "s_lshr_b32 s74, s76, 2\n"
        "s_lshl_b32 s74, s74, 23\n"
        "s_or_b32 s71, s73, s74\n"
        "ds_add_u32 v2, v3 offset:45480\n"
        "s_waitcnt vmcnt(0)\n"
        "ds_add_u32 v2, v3 offset:45500\n"
        "ds_read_b32 v113, v2 offset:45500\n"
        "s_waitcnt lgkmcnt(0)\n"
        "v_readfirstlane_b32 s73, v113\n"
        "s_cmp_ge_u32 s73, 4\n"
        "s_cbranch_scc0 .Lw_5\n"
        ".Le_6:\n"
        "ds_read_b128 v[8:11], v108 offset:0\n"
        "ds_read_b128 v[12:15], v124 offset:0\n"
        "ds_read_b128 v[16:19], v109 offset:0\n"
        "ds_read_b128 v[20:23], v125 offset:0\n"
        "ds_read_b128 v[24:27], v110 offset:0\n"
        "ds_read_b128 v[28:31], v126 offset:0\n"
        "ds_read_b128 v[32:35], v111 offset:0\n"
        "ds_read_b128 v[36:39], v127 offset:0\n"
        "ds_read_b128 v[40:43], v108 offset:256\n"
        "ds_read_b128 v[44:47], v124 offset:256\n"
        "ds_read_b128 v[48:51], v109 offset:256\n"
        "ds_read_b128 v[52:55], v125 offset:256\n"
        "ds_read_b128 v[56:59], v110 offset:256\n"
        "ds_read_b128 v[60:63], v126 offset:256\n"
        "ds_read_b128 v[64:67], v111 offset:256\n"
        "ds_read_b128 v[68:71], v127 offset:256\n"
        "s_waitcnt lgkmcnt(14)\n"
        "v_mfma_f32_16x16x32_bf16 v[116:119], v[8:11], v[12:15], 0\n"
        "s_waitcnt lgkmcnt(12)\n"
        "v_mfma_f32_16x16x32_bf16 v[120:123], v[16:19], v[20:23], 0\n"
        "s_waitcnt lgkmcnt(10)\n"
        "v_mfma_f32_16x16x32_bf16 v[116:119], v[24:27], v[28:31], v[116:119]\n"
        "s_waitcnt lgkmcnt(8)\n"
        "v_mfma_f32_16x16x32_bf16 v[120:123], v[32:35], v[36:39], v[120:123]\n"
        "s_waitcnt lgkmcnt(6)\n"
        "v_mfma_f32_16x16x32_bf16 v[116:119], v[40:43], v[44:47], v[116:119]\n"
        "s_waitcnt lgkmcnt(4)\n"
        "v_mfma_f32_16x16x32_bf16 v[120:123], v[48:51], v[52:55], v[120:123]\n"
        "s_waitcnt lgkmcnt(2)\n"
        "v_mfma_f32_16x16x32_bf16 v[116:119], v[56:59], v[60:63], v[116:119]\n"
        "s_waitcnt lgkmcnt(0)\n"
        "v_mfma_f32_16x16x32_bf16 v[120:123], v[64:67], v[68:71], v[120:123]\n"
        "s_nop 7\n"
        "v_add_f32 v116, v116, v120\n"
        "v_add_f32 v117, v117, v121\n"
        "v_add_f32 v118, v118, v122\n"
        "v_add_f32 v119, v119, v123\n"
        "s_and_b32 s73, s64, 7\n"
        "s_lshl_b32 s73, s73, 23\n"
        "s_sub_u32 s73, 0x3d800000, s73\n"
        "v_mul_f32 v116, s73, v116\n"
        "v_mul_f32 v117, s73, v117\n"
        "v_mul_f32 v118, s73, v118\n"
        "v_mul_f32 v119, s73, v119\n"
        "v_cvt_pk_bf16_f32 v8, v116, v117\n"
        "v_cvt_pk_bf16_f32 v9, v118, v119\n"
        "buffer_store_dwordx2 v[8:9], v112, s[44:47], s79 offen sc1\n"
        "v_add_u32 v10, v8, v9\n"
        "s_nop 1\n"
        "v_add_u32_dpp v10, v10, v10 row_shr:1 row_mask:0xf bank_mask:0xf bound_ctrl:0\n"
        "s_nop 1\n"
        "v_add_u32_dpp v10, v10, v10 row_shr:2 row_mask:0xf bank_mask:0xf bound_ctrl:0\n"
        "s_nop 1\n"
        "v_add_u32_dpp v10, v10, v10 row_shr:4 row_mask:0xf bank_mask:0xf bound_ctrl:0\n"
        "s_nop 1\n"
        "v_add_u32_dpp v10, v10, v10 row_shr:8 row_mask:0xf bank_mask:0xf bound_ctrl:0\n"
        "s_nop 1\n"
        "v_add_u32_dpp v10, v10, v10 row_bcast:15 row_mask:0xa bank_mask:0xf\n"
        "s_nop 1\n"
        "v_add_u32_dpp v10, v10, v10 row_bcast:31 row_mask:0xc bank_mask:0xf\n"
        "s_nop 0\n"
        "v_readlane_b32 s73, v10, 63\n"
        "s_add_u32 s73, s73, s67\n"
        "v_mov_b32 v12, s66\n"
        "v_mov_b32 v13, s73\n"
        "s_mov_b64 exec, 1\n"
        "buffer_store_dwordx2 v[12:13], v2, s[44:47], s78 offen sc1\n"
        "s_mov_b64 exec, -1\n"
        "s_branch .Lspecial_done\n"
        ".Lflag_out:\n"
        "s_waitcnt vmcnt(0)\n"
        "v_mov_b32 v12, s66\n"
        "v_mov_b32 v13, s67\n"
        "s_mov_b64 exec, 1\n"
        "buffer_store_dwordx2 v[12:13], v2, s[44:47], s78 offen sc1\n"
        "s_mov_b64 exec, -1\n"
        ".Lspecial_done:\n"
        "ds_add_u32 v2, v3 offset:45516\n"
        ".Lheld:\n"
        "s_setprio 0\n"
        "s_cmp_ge_u32 s2, s56\n"
        "s_cbranch_scc1 .Lend_l\n"
        "v_mov_b32 v52, 0\n"
        "v_mov_b32 v53, 0\n"
        "v_mov_b32 v54, 0\n"
        "v_bfrev_b32 v55, 1\n"
        "v_lshlrev_b32 v47, 4, v0\n"
        "v_add_u32 v57, 130304, v47\n"
        "v_lshrrev_b32 v7, 2, v47\n"
        "v_add_u32 v7, 128256, v7\n"
        "ds_read_b32 v8, v2 offset:45516\n"
        "s_waitcnt lgkmcnt(0)\n"
        "v_readfirstlane_b32 s73, v8\n"
        "s_cmp_ge_u32 s73, 4\n"
        "s_cbranch_scc0 .Lw_7\n"
        ".Le_8:\n"
        "s_setprio 0\n"
        "s_mul_i32 s76, s72, 5120\n"
        "s_mov_b32 m0, s76\n"
        "s_add_u32 s77, s76, 0x1000\n"
        "buffer_load_dwordx4 v4, s[8:11], s76 offen offset:0 lds\n"
        "buffer_load_dwordx4 v4, s[8:11], s76 offen offset:1024 lds\n"
        "buffer_load_dwordx4 v4, s[8:11], s76 offen offset:2048 lds\n"
        "buffer_load_dwordx4 v4, s[8:11], s76 offen offset:3072 lds\n"
        "s_mov_b32 m0, s77\n"
        "s_nop 0\n"
        "buffer_load_dwordx4 v4, s[8:11], s77 offen lds\n"
        "s_setprio 0\n"
        "ds_write_b128 v57, v[52:55] offset:0\n"
        "ds_write_b128 v57, v[52:55] offset:4096\n"
        "ds_write_b128 v57, v[52:55] offset:8192\n"
        "ds_write_b128 v57, v[52:55] offset:12288\n"
        "ds_write_b128 v57, v[52:55] offset:16384\n"
        "ds_write_b128 v57, v[52:55] offset:20480\n"
        "ds_write2st64_b32 v7, v52, v52 offset1:4\n"
        "v_and_b32 v7, 0xffffff00, v7\n"
        "s_and_b32 s73, s2, 7\n"
        "s_lshl_b32 s73, s73, 1\n"
        "s_and_b32 s74, s2, 0xf0\n"
        "s_add_u32 s73, s73, s74\n"
        "s_bfe_u32 s74, s2, 0x10003\n"
        "s_add_u32 s73, s73, s74\n"
        "s_lshl_b32 s73, s73, 6\n"
        "s_add_u32 s40, s73, 466944\n"
        "s_add_u32 s41, s73, 2564096\n"
        "s_add_u32 s42, s73, 483328\n"
        "s_add_u32 s43, s73, 2580480\n"
        "v_and_b32 v44, 48, v47\n"
        "v_lshrrev_b32 v47, 6, v47\n"
        "v_mul_u32_u24 v47, 32768, v47\n"
        "v_add_u32 v47, v47, v44\n"
        "v_and_b32 v44, 3, v1\n"
        "v_lshlrev_b32 v44, 11, v44\n"
        "v_mov_b32 v46, 16\n"
        "s_mul_i32 s74, s2, 80\n"
        "s_lshl_b32 s75, s72, 1\n"
        "s_add_u32 s74, s74, s75\n"
        "v_lshl_add_u32 v45, v1, 3, s74\n"
        "v_min_u32 v45, 20478, v45\n"
        "s_cmp_gt_u32 s72, 0\n"
        "s_cselect_b32 s14, 1, 0\n"
        "s_cmp_gt_u32 s72, 1\n"
        "s_cselect_b32 s15, 1, 0\n"
        "s_cmp_gt_u32 s72, 2\n"
        "s_cselect_b32 s16, 1, 0\n"
        "v_and_b32 v10, 15, v1\n"
        "v_lshrrev_b32 v24, 5, v1\n"
        "v_mul_u32_u24 v24, 272, v24\n"
        "v_lshl_add_u32 v24, v10, 4, v24\n"
        "v_bfe_u32 v25, v1, 4, 1\n"
        "v_lshl_add_u32 v24, v25, 3, v24\n"
        "s_mul_i32 s73, s72, 544\n"
        "s_add_u32 s73, s73, 0\n"
        "v_add_u32 v51, s73, v24\n"
        "v_mov_b64 v[56:57], 0\n"
        "v_mov_b64 v[58:59], 0\n"
        "v_lshrrev_b32 v24, 5, v1\n"
        "v_lshlrev_b32 v24, 7, v24\n"
        "v_lshl_add_u32 v24, v10, 3, v24\n"
        "v_lshl_add_u32 v24, v25, 2, v24\n"
        "s_add_u32 s73, s73, 17408\n"
        "v_add_u32 v42, s73, v24\n"
        "s_mul_i32 s12, s72, 6144\n"
        "s_add_u32 s12, s12, 130304\n"
        "v_mov_b32 v43, v2\n"
        "v_lshrrev_b32 v16, 4, v1\n"
        "v_lshlrev_b32 v85, 2, v1\n"
        "v_add_u32 v84, 128256, v85\n"
        "v_mul_u32_u24 v83, 6144, v16\n"
        "v_add_u32 v83, 130304, v83\n"
        "v_cmp_gt_u32 s[18:19], 10, v10\n"
        "v_mov_b32 v82, 154880\n"
        "s_movk_i32 s17, 0xfc\n"
        "v_cndmask_b32 v83, v82, v83, s[18:19]\n"
        "s_add_u32 s73, s12, 452\n"
        "v_lshl_add_u32 v24, v16, 4, s73\n"
        "v_and_b32 v25, 4, v1\n"
        "v_sub_u32 v24, v24, v25\n"
        "v_mov_b32 v25, 129280\n"
        "v_cmp_gt_u32 s[76:77], 8, v10\n"
        "v_and_b32 v26, 3, v1\n"
        "v_mul_u32_u24 v54, 0x5555, v26\n"
        "v_mov_b32 v26, 512\n"
        "v_cndmask_b32 v52, v25, v24, s[76:77]\n"
        "v_cndmask_b32 v53, v24, v25, s[76:77]\n"
        "v_cndmask_b32 v55, 0, v26, s[76:77]\n"
        "v_sub_u32 v60, v26, v55\n"
        "s_add_u32 s73, s12, 12\n"
        "v_and_b32 v48, 31, v1\n"
        "v_lshlrev_b32 v48, 4, v48\n"
        "v_lshrrev_b32 v16, 5, v1\n"
        "v_lshl_add_u32 v49, v16, 4, s73\n"
        "s_lshl_b32 s13, s72, 4\n"
        ".Lpd:\n"
        "s_memrealtime s[60:61]\n"
        "s_waitcnt lgkmcnt(0)\n"
        "s_sub_u32 s73, s60, s62\n"
        "s_cmp_ge_u32 s73, 250\n"
        "s_cbranch_scc1 .Llook\n"
        "s_sleep 2\n"
        "s_branch .Lpd\n"
        ".Llook:\n"
        "s_setprio 3\n"
        "buffer_load_dwordx4 v[64:67], v47, s[44:47], s40 offen sc1\n"
        "buffer_load_dwordx4 v[68:71], v47, s[44:47], s41 offen sc1\n"
        "s_setprio 2\n"
        "s_mov_b32 s73, 0xff800003\n"
        "s_movk_i32 s82, 0x2000\n"
        "s_waitcnt vmcnt(0)\n"
        "ds_add_u32 v2, v3 offset:45512\n"
        "v_xor_b32 v64, s68, v64\n"
        "v_xor_b32 v65, s69, v65\n"
        "v_xor_b32 v66, s70, v66\n"
        "v_xor_b32 v67, s71, v67\n"
        "v_or3_b32 v8, v64, v65, v66\n"
        "v_or_b32 v8, v8, v67\n"
        "v_and_b32 v8, s73, v8\n"
        "v_cmp_ne_u32 s[74:75], 0, v8\n"
        "v_xor_b32 v68, s68, v68\n"
        "v_xor_b32 v69, s69, v69\n"
        "v_xor_b32 v70, s70, v70\n"
        "v_xor_b32 v71, s71, v71\n"
        "v_or3_b32 v9, v68, v69, v70\n"
        "v_or_b32 v9, v9, v71\n"
        "v_and_b32 v9, s73, v9\n"
        "v_cmp_ne_u32 s[76:77], 0, v9\n"
        "v_mov_b32_dpp v10, v64 quad_perm:[0,0,0,0] row_mask:0xf bank_mask:0xf\n"
        "v_mov_b32_dpp v11, v68 quad_perm:[0,0,0,0] row_mask:0xf bank_mask:0xf\n"
        "v_cmp_ge_u32 s[32:33], v10, v44\n"
        "v_cmp_le_u32 s[36:37], s82, v10\n"
        "v_cmp_ge_u32 s[34:35], v11, v44\n"
        "v_cmp_le_u32 s[38:39], s82, v11\n"
        "s_or_b64 s[78:79], s[74:75], s[76:77]\n"
        ".Lbk:\n"
        "s_cmp_lg_u64 s[78:79], 0\n"
        "s_cbranch_scc1 .Lbk_again\n"
        "s_or_b64 s[80:81], s[36:37], s[38:39]\n"
        "s_cbranch_scc1 .Lbk_more\n"
        ".Lbk_done:\n"
        "s_mov_b64 exec, s[32:33]\n"
        "v_and_or_b32 v8, v64, s17, v7\n"
        "v_lshrrev_b32 v24, 8, v64\n"
        "ds_add_rtn_u32 v8, v8, v46\n"
        "v_and_or_b32 v9, v65, s17, v7\n"
        "v_lshrrev_b32 v25, 8, v65\n"
        "ds_add_rtn_u32 v9, v9, v46\n"
        "v_and_or_b32 v10, v66, s17, v7\n"
        "v_lshrrev_b32 v26, 8, v66\n"
        "ds_add_rtn_u32 v10, v10, v46\n"
        "v_and_or_b32 v11, v67, s17, v7\n"
        "v_lshrrev_b32 v27, 8, v67\n"
        "ds_add_rtn_u32 v11, v11, v46\n"
        "s_mov_b64 exec, s[34:35]\n"
        "v_and_or_b32 v12, v68, s17, v7\n"
        "v_lshrrev_b32 v28, 8, v68\n"
        "ds_add_rtn_u32 v12, v12, v46\n"
        "v_and_or_b32 v13, v69, s17, v7\n"
        "v_lshrrev_b32 v29, 8, v69\n"
        "ds_add_rtn_u32 v13, v13, v46\n"
        "v_and_or_b32 v14, v70, s17, v7\n"
        "v_lshrrev_b32 v30, 8, v70\n"
        "ds_add_rtn_u32 v14, v14, v46\n"
        "v_and_or_b32 v15, v71, s17, v7\n"
        "v_lshrrev_b32 v31, 8, v71\n"
        "ds_add_rtn_u32 v15, v15, v46\n"
        "s_cmp_lg_u64 s[80:81], 0\n"
        "s_cbranch_scc1 .Len0\n"
        ".Len0_e:\n"
        "s_mov_b64 exec, -1\n"
        "ds_add_u32 v2, v3 offset:45528\n"
        "ds_read_b32 v86, v2 offset:45512\n"
        "s_waitcnt lgkmcnt(0)\n"
        "v_readfirstlane_b32 s73, v86\n"
        "s_cmp_ge_u32 s73, 4\n"
        "s_cbranch_scc0 .Lw_9\n"
        ".Le_10:\n"
        "ds_read_u16 v6, v45\n"
        "s_mov_b64 exec, s[32:33]\n"
        "ds_read_u16 v24, v24\n"
        "ds_read_u16 v25, v25\n"
        "ds_read_u16 v26, v26\n"
        "ds_read_u16 v27, v27\n"
        "s_mov_b64 exec, s[34:35]\n"
        "ds_read_u16 v28, v28\n"
        "ds_read_u16 v29, v29\n"
        "ds_read_u16 v30, v30\n"
        "ds_read_u16 v31, v31\n"
        "s_cmp_lg_u64 s[80:81], 0\n"
        "s_cbranch_scc1 .Len1\n"
        ".Len1_e:\n"
        "s_mov_b64 exec, -1\n"
        "s_mov_b32 s74, 0x40000\n"
        ".Lcn:\n"
        "ds_read_b32 v112, v85 offset:45440\n"
        "ds_read2st64_b32 v[108:109], v84 offset1:1\n"
        "ds_read2st64_b32 v[110:111], v84 offset0:2 offset1:3\n"
        "s_waitcnt lgkmcnt(0)\n"
        "v_readlane_b32 s73, v112, 22\n"
        "s_cmp_lt_u32 s73, 4\n"
        "s_cbranch_scc1 .Lcn_again\n"
        ".Lcn_done:\n"
        "v_add3_u32 v113, v108, v109, v110\n"
        "v_add3_u32 v113, v113, v111, 48\n"
        "v_readlane_b32 s75, v112, 13\n"
        "v_readlane_b32 s77, v113, 10\n"
        "v_and_b32 v113, 0xffffffc0, v113\n"
        "v_max_u32 v113, 64, v113\n"
        "v_cndmask_b32 v113, 0, v113, s[18:19]\n"
        "v_mad_u32_u24 v83, v108, s14, v83\n"
        "v_mad_u32_u24 v83, v109, s15, v83\n"
        "v_add_u32_dpp v114, v113, v113 row_shr:1 row_mask:0xf bank_mask:0xf bound_ctrl:0\n"
        "v_mad_u32_u24 v83, v110, s16, v83\n"
        "s_xor_b32 s77, s77, 48\n"
        "v_add_u32_dpp v114, v114, v114 row_shr:2 row_mask:0xf bank_mask:0xf bound_ctrl:0\n"
        "v_sub_u32 v83, v83, v113\n"
        "s_or_b32 s75, s75, s77\n"
        "v_add_u32_dpp v114, v114, v114 row_shr:4 row_mask:0xf bank_mask:0xf bound_ctrl:0\n"
        "v_lshrrev_b32 v50, 6, v113\n"
        "v_lshlrev_b32 v6, 16, v6\n"
        "v_add_u32_dpp v114, v114, v114 row_shr:8 row_mask:0xf bank_mask:0xf bound_ctrl:0\n"
        "s_mov_b32 s73, 0xfffffe00\n"
        "v_cmp_lt_u32 vcc, 5376, v114\n"
        "v_cndmask_b32 v115, 0, v114, s[18:19]\n"
        "v_add_u32 v83, v83, v115\n"
        "s_or_b32 s75, s75, vcc_lo\n"
        "s_or_b32 s75, s75, vcc_hi\n"
        "s_cbranch_scc1 .Lfail_l\n"
        "ds_bpermute_b32 v92, v64, v83\n"
        "ds_bpermute_b32 v93, v65, v83\n"
        "ds_bpermute_b32 v94, v66, v83\n"
        "ds_bpermute_b32 v95, v67, v83\n"
        "ds_bpermute_b32 v96, v68, v83\n"
        "ds_bpermute_b32 v97, v69, v83\n"
        "ds_bpermute_b32 v98, v70, v83\n"
        "ds_bpermute_b32 v99, v71, v83\n"
        "s_cmp_lg_u64 s[80:81], 0\n"
        "s_cbranch_scc1 .Lbp\n"
        ".Lbp_e:\n"
        "v_and_b32 v64, s73, v64\n"
        "v_and_b32 v65, s73, v65\n"
        "v_and_b32 v66, s73, v66\n"
        "v_and_b32 v67, s73, v67\n"
        "v_and_b32 v68, s73, v68\n"
        "v_and_b32 v69, s73, v69\n"
        "v_and_b32 v70, s73, v70\n"
        "v_and_b32 v71, s73, v71\n"
        "s_add_u32 s76, s13, 15\n"
        "v_readlane_b32 s77, v114, s76\n"
        "v_subrev_u32 v50, 1, v50\n"
        "s_waitcnt lgkmcnt(0)\n"
        "s_mov_b64 exec, s[32:33]\n"
        "v_add_u32 v8, v8, v92\n"
        "ds_write2_b32 v8, v64, v24 offset0:3 offset1:113\n"
        "v_add_u32 v9, v9, v93\n"
        "ds_write2_b32 v9, v65, v25 offset0:3 offset1:113\n"
        "v_add_u32 v10, v10, v94\n"
        "ds_write2_b32 v10, v66, v26 offset0:3 offset1:113\n"
        "v_add_u32 v11, v11, v95\n"
        "ds_write2_b32 v11, v67, v27 offset0:3 offset1:113\n"
        "s_mov_b64 exec, s[34:35]\n"
        "v_add_u32 v12, v12, v96\n"
        "ds_write2_b32 v12, v68, v28 offset0:3 offset1:113\n"
        "v_add_u32 v13, v13, v97\n"
        "ds_write2_b32 v13, v69, v29 offset0:3 offset1:113\n"
        "v_add_u32 v14, v14, v98\n"
        "ds_write2_b32 v14, v70, v30 offset0:3 offset1:113\n"
        "v_add_u32 v15, v15, v99\n"
        "ds_write2_b32 v15, v71, v31 offset0:3 offset1:113\n"
        "s_cmp_lg_u64 s[80:81], 0\n"
        "s_cbranch_scc1 .Len2\n"
        ".Len2_e:\n"
        "s_mov_b64 exec, -1\n"
        "ds_add_u32 v2, v3 offset:45504\n"
        "s_lshr_b32 s9, s77, 4\n"
        "s_sub_i32 s9, s9, 14\n"
        "s_max_i32 s9, s9, 0\n"
        "s_lshr_b32 s9, s9, 1\n"
        "s_mov_b32 s10, 3\n"
        "ds_read_b32 v40, v2 offset:45504\n"
        "s_waitcnt lgkmcnt(0)\n"
        "v_readfirstlane_b32 s73, v40\n"
        "s_cmp_ge_u32 s73, 4\n"
        "s_cbranch_scc0 .Lw_11\n"
        ".Le_12:\n"
        "ds_read_b32 v40, v2 offset:45520\n"
        "s_waitcnt lgkmcnt(0)\n"
        "v_readfirstlane_b32 s73, v40\n"
        "s_cmp_ge_u32 s73, 4\n"
        "s_cbranch_scc0 .Lw_13\n"
        ".Le_14:\n"
        "ds_read2_b32 v[8:9], v49 offset0:0 offset1:8\n"
        "ds_read2_b32 v[10:11], v49 offset0:16 offset1:24\n"
        "ds_read2_b32 v[12:13], v49 offset0:32 offset1:40\n"
        "ds_read2_b32 v[14:15], v49 offset0:48 offset1:56\n"
        "ds_read2_b32 v[16:17], v49 offset0:64 offset1:72\n"
        "ds_read2_b32 v[18:19], v49 offset0:80 offset1:88\n"
        "ds_read2_b32 v[20:21], v49 offset0:96 offset1:104\n"
        "v_add_u32 v49, 448, v49\n"
        "ds_read2_b32 v[116:117], v52 offset0:0 offset1:1\n"
        "ds_read2_b32 v[118:119], v53 offset0:0 offset1:1\n"
        "ds_read2_b32 v[40:41], v49 offset0:0 offset1:8\n"
        "ds_read2_b32 v[120:121], v52 offset0:16 offset1:17\n"
        "ds_read2_b32 v[122:123], v53 offset0:16 offset1:17\n"
        "ds_read2_b32 v[62:63], v49 offset0:16 offset1:24\n"
        "s_waitcnt lgkmcnt(12)\n"
        "v_or_b32 v8, v8, v48\n"
        "buffer_load_dwordx4 v[124:127], v8, s[4:7], 0 offen\n"
        "v_or_b32 v9, v9, v48\n"
        "buffer_load_dwordx4 v[128:131], v9, s[4:7], 0 offen\n"
        "s_waitcnt lgkmcnt(11)\n"
        "v_or_b32 v10, v10, v48\n"
        "buffer_load_dwordx4 v[132:135], v10, s[4:7], 0 offen\n"
        "v_or_b32 v11, v11, v48\n"
        "buffer_load_dwordx4 v[136:139], v11, s[4:7], 0 offen\n"
        "s_waitcnt lgkmcnt(10)\n"
        "v_or_b32 v12, v12, v48\n"
        "buffer_load_dwordx4 v[140:143], v12, s[4:7], 0 offen\n"
        "v_or_b32 v13, v13, v48\n"
        "buffer_load_dwordx4 v[144:147], v13, s[4:7], 0 offen\n"
        "s_waitcnt lgkmcnt(9)\n"
        "v_or_b32 v14, v14, v48\n"
        "buffer_load_dwordx4 v[148:151], v14, s[4:7], 0 offen\n"
        "v_or_b32 v15, v15, v48\n"
        "buffer_load_dwordx4 v[152:155], v15, s[4:7], 0 offen\n"
        "s_waitcnt lgkmcnt(8)\n"
        "v_or_b32 v16, v16, v48\n"
        "buffer_load_dwordx4 v[156:159], v16, s[4:7], 0 offen\n"
        "v_or_b32 v17, v17, v48\n"
        "buffer_load_dwordx4 v[160:163], v17, s[4:7], 0 offen\n"
        "s_waitcnt lgkmcnt(7)\n"
        "v_or_b32 v18, v18, v48\n"
        "buffer_load_dwordx4 v[164:167], v18, s[4:7], 0 offen\n"
        "v_or_b32 v19, v19, v48\n"
        "buffer_load_dwordx4 v[168:171], v19, s[4:7], 0 offen\n"
        "s_waitcnt lgkmcnt(6)\n"
        "v_or_b32 v20, v20, v48\n"
        "buffer_load_dwordx4 v[172:175], v20, s[4:7], 0 offen\n"
        "v_or_b32 v21, v21, v48\n"
        "buffer_load_dwordx4 v[176:179], v21, s[4:7], 0 offen\n"
        "v_readlane_b32 s74, v50, s13\n"
        "s_mov_b32 s75, 0\n"
        "s_getpc_b64 s[82:83]\n"
        ".Lrg_base:\n"
        "s_mov_b32 s73, 40\n"
        "s_setprio 2\n"
        ".Lrg_m:\n"
        ".Lrg_m0:\n"
        "ds_read2_b32 v[24:25], v52 offset0:32 offset1:33\n"
        "ds_read2_b32 v[26:27], v53 offset0:32 offset1:33\n"
        "ds_read2_b32 v[32:33], v49 offset0:32 offset1:40\n"
        "s_waitcnt vmcnt(12) lgkmcnt(6)\n"
        "v_or_b32 v40, v40, v48\n"
        "v_or_b32 v41, v41, v48\n"
        "s_sub_u32 s74, s74, 1\n"
        "v_smfmac_f32_16x16x64_bf16 v[56:59], v[116:119], v[124:131], v54\n"
        "buffer_load_dwordx4 v[180:183], v40, s[4:7], 0 offen\n"
        "buffer_load_dwordx4 v[184:187], v41, s[4:7], 0 offen\n"
        "s_cbranch_scc1 .Lrg_me0\n"
        ".Lrg_m1:\n"
        "ds_read2_b32 v[28:29], v52 offset0:48 offset1:49\n"
        "ds_read2_b32 v[30:31], v53 offset0:48 offset1:49\n"
        "ds_read2_b32 v[34:35], v49 offset0:48 offset1:56\n"
        "s_waitcnt vmcnt(12) lgkmcnt(6)\n"
        "v_or_b32 v62, v62, v48\n"
        "v_or_b32 v63, v63, v48\n"
        "s_sub_u32 s74, s74, 1\n"
        "v_smfmac_f32_16x16x64_bf16 v[56:59], v[120:123], v[132:139], v54\n"
        "buffer_load_dwordx4 v[124:127], v62, s[4:7], 0 offen\n"
        "buffer_load_dwordx4 v[128:131], v63, s[4:7], 0 offen\n"
        "s_cbranch_scc1 .Lrg_me1\n"
        ".Lrg_m2:\n"
        "ds_read2_b32 v[116:117], v52 offset0:64 offset1:65\n"
        "ds_read2_b32 v[118:119], v53 offset0:64 offset1:65\n"
        "ds_read2_b32 v[40:41], v49 offset0:64 offset1:72\n"
        "s_waitcnt vmcnt(12) lgkmcnt(6)\n"
        "v_or_b32 v32, v32, v48\n"
        "v_or_b32 v33, v33, v48\n"
        "s_sub_u32 s74, s74, 1\n"
        "v_smfmac_f32_16x16x64_bf16 v[56:59], v[24:27], v[140:147], v54\n"
        "buffer_load_dwordx4 v[132:135], v32, s[4:7], 0 offen\n"
        "buffer_load_dwordx4 v[136:139], v33, s[4:7], 0 offen\n"
        "s_cbranch_scc1 .Lrg_me2\n"
        ".Lrg_m3:\n"
        "ds_read2_b32 v[120:121], v52 offset0:80 offset1:81\n"
        "ds_read2_b32 v[122:123], v53 offset0:80 offset1:81\n"
        "ds_read2_b32 v[62:63], v49 offset0:80 offset1:88\n"
        "s_waitcnt vmcnt(12) lgkmcnt(6)\n"
        "v_or_b32 v34, v34, v48\n"
        "v_or_b32 v35, v35, v48\n"
        "s_sub_u32 s74, s74, 1\n"
        "v_smfmac_f32_16x16x64_bf16 v[56:59], v[28:31], v[148:155], v54\n"
        "buffer_load_dwordx4 v[140:143], v34, s[4:7], 0 offen\n"
        "buffer_load_dwordx4 v[144:147], v35, s[4:7], 0 offen\n"
        "s_cbranch_scc1 .Lrg_me3\n"
        ".Lrg_m4:\n"
        "ds_read2_b32 v[24:25], v52 offset0:96 offset1:97\n"
        "ds_read2_b32 v[26:27], v53 offset0:96 offset1:97\n"
        "ds_read2_b32 v[32:33], v49 offset0:96 offset1:104\n"
        "s_waitcnt vmcnt(12) lgkmcnt(6)\n"
        "v_or_b32 v40, v40, v48\n"
        "v_or_b32 v41, v41, v48\n"
        "s_sub_u32 s74, s74, 1\n"
        "v_smfmac_f32_16x16x64_bf16 v[56:59], v[116:119], v[156:163], v54\n"
        "buffer_load_dwordx4 v[148:151], v40, s[4:7], 0 offen\n"
        "buffer_load_dwordx4 v[152:155], v41, s[4:7], 0 offen\n"
        "s_cbranch_scc1 .Lrg_me4\n"
        ".Lrg_m5:\n"
        "ds_read2_b32 v[28:29], v52 offset0:112 offset1:113\n"
        "ds_read2_b32 v[30:31], v53 offset0:112 offset1:113\n"
        "ds_read2_b32 v[34:35], v49 offset0:112 offset1:120\n"
        "s_waitcnt vmcnt(12) lgkmcnt(6)\n"
        "v_or_b32 v62, v62, v48\n"
        "v_or_b32 v63, v63, v48\n"
        "s_sub_u32 s74, s74, 1\n"
        "v_smfmac_f32_16x16x64_bf16 v[56:59], v[120:123], v[164:171], v54\n"
        "buffer_load_dwordx4 v[156:159], v62, s[4:7], 0 offen\n"
        "buffer_load_dwordx4 v[160:163], v63, s[4:7], 0 offen\n"
        "s_cbranch_scc1 .Lrg_me5\n"
        ".Lrg_m6:\n"
        "ds_read2_b32 v[116:117], v52 offset0:128 offset1:129\n"
        "ds_read2_b32 v[118:119], v53 offset0:128 offset1:129\n"
        "ds_read2_b32 v[40:41], v49 offset0:128 offset1:136\n"
        "s_waitcnt vmcnt(12) lgkmcnt(6)\n"
        "v_or_b32 v32, v32, v48\n"
        "v_or_b32 v33, v33, v48\n"
        "s_sub_u32 s74, s74, 1\n"
        "v_smfmac_f32_16x16x64_bf16 v[56:59], v[24:27], v[172:179], v54\n"
        "buffer_load_dwordx4 v[164:167], v32, s[4:7], 0 offen\n"
        "buffer_load_dwordx4 v[168:171], v33, s[4:7], 0 offen\n"
        "s_cbranch_scc1 .Lrg_me6\n"
        ".Lrg_m7:\n"
        "ds_read2_b32 v[120:121], v52 offset0:144 offset1:145\n"
        "ds_read2_b32 v[122:123], v53 offset0:144 offset1:145\n"
        "ds_read2_b32 v[62:63], v49 offset0:144 offset1:152\n"
        "s_waitcnt vmcnt(12) lgkmcnt(6)\n"
        "v_or_b32 v34, v34, v48\n"
        "v_or_b32 v35, v35, v48\n"
        "s_sub_u32 s74, s74, 1\n"
        "v_smfmac_f32_16x16x64_bf16 v[56:59], v[28:31], v[180:187], v54\n"
        "buffer_load_dwordx4 v[172:175], v34, s[4:7], 0 offen\n"
        "buffer_load_dwordx4 v[176:179], v35, s[4:7], 0 offen\n"
        "s_cbranch_scc1 .Lrg_me7\n"
        ".Lrg_m8:\n"
        "v_add_u32 v49, 512, v49\n"
        "v_add_u32 v52, v55, v52\n"
        "v_add_u32 v53, v60, v53\n"
        "s_sub_u32 s73, s73, 1\n"
        "s_cbranch_scc0 .Lrg_m\n"
        "s_branch .Lrg_done\n"
        ".Lrg_me0:\n"
        "s_add_u32 s80, s82, .Lrg_m1-.Lrg_base\n"
        "s_branch .Lrg_flush\n"
        ".Lrg_me1:\n"
        "s_add_u32 s80, s82, .Lrg_m2-.Lrg_base\n"
        "s_branch .Lrg_flush\n"
        ".Lrg_me2:\n"
        "s_add_u32 s80, s82, .Lrg_m3-.Lrg_base\n"
        "s_branch .Lrg_flush\n"
        ".Lrg_me3:\n"
        "s_add_u32 s80, s82, .Lrg_m4-.Lrg_base\n"
        "s_branch .Lrg_flush\n"
        ".Lrg_me4:\n"
        "s_add_u32 s80, s82, .Lrg_m5-.Lrg_base\n"
        "s_branch .Lrg_flush\n"
        ".Lrg_me5:\n"
        "s_add_u32 s80, s82, .Lrg_m6-.Lrg_base\n"
        "s_branch .Lrg_flush\n"
        ".Lrg_me6:\n"
        "s_add_u32 s80, s82, .Lrg_m7-.Lrg_base\n"
        "s_branch .Lrg_flush\n"
        ".Lrg_me7:\n"
        "s_add_u32 s80, s82, .Lrg_m8-.Lrg_base\n"
        ".Lrg_flush:\n"
        "s_addc_u32 s81, s83, 0\n"
        "v_readlane_b32 s77, v6, s75\n"
        "s_add_u32 s13, s13, 1\n"
        "s_add_u32 s75, s75, 1\n"
        "v_readlane_b32 s74, v50, s13\n"
        "s_cmp_ge_u32 s75, 9\n"
        "v_mul_f32 v8, s77, v56\n"
        "v_mul_f32 v9, s77, v57\n"
        "v_mul_f32 v10, s77, v58\n"
        "v_mul_f32 v11, s77, v59\n"
        "v_cvt_pk_bf16_f32 v8, v8, v9\n"
        "v_cvt_pk_bf16_f32 v9, v10, v11\n"
        "s_cbranch_scc1 .Lrg_last\n"
        "v_mov_b64 v[56:57], 0\n"
        "v_mov_b64 v[58:59], 0\n"
        "ds_write_b64 v51, v[8:9]\n"
        "ds_add_u32 v43, v3 offset:45440\n"
        "v_add_u32 v51, 2176, v51\n"
        "v_add_u32 v43, 4, v43\n"
        "s_setpc_b64 s[80:81]\n"
        ".Lrg_last:\n"
        "ds_write2_b32 v42, v8, v9 offset1:68\n"
        "ds_add_u32 v43, v3 offset:45440\n"
        "s_cmp_ge_u32 s75, 10\n"
        "v_mov_b64 v[56:57], 0\n"
        "v_mov_b64 v[58:59], 0\n"
        "v_add_u32 v42, 2176, v42\n"
        "v_add_u32 v43, 4, v43\n"
        "s_cbranch_scc1 .Lrg_done\n"
        "s_setpc_b64 s[80:81]\n"
        ".Lrg_done:\n"
        ".Lend_l:\n"
        "s_endpgm\n"
        ".Lpre_a:\n"
        "v_bfe_u32 v5, v0, 5, 3\n"
        "v_and_b32 v7, 31, v0\n"
        "v_xor_b32 v7, v7, v5\n"
        "v_lshlrev_b32 v5, 9, v5\n"
        "v_lshl_or_b32 v6, v7, 4, v5\n"
        "v_xor_b32 v7, 0x80, v6\n"
        "s_lshl_b32 s73, s72, 9\n"
        "s_lshl_b32 s76, s72, 10\n"
        "s_add_u32 m0, s73, 21760\n"
        "s_mul_i32 s74, s2, 640\n"
        "s_mul_i32 s75, s72, 160\n"
        "s_add_u32 s74, s74, s75\n"
        "s_mul_i32 s75, s2, 20480\n"
        "s_barrier\n"
        "s_waitcnt lgkmcnt(0)\n"
        "s_load_dwordx2 s[64:65], s[44:45], 0x0\n"
        "buffer_load_dword v8, s[36:39], s74 offen lds\n"
        "s_add_u32 m0, m0, 256\n"
        "s_nop 0\n"
        "buffer_load_dword v8, s[40:43], s74 offen lds\n"
        "s_add_u32 m0, s76, 24064\n"
        "s_mov_b32 s78, m0\n"
        "s_lshr_b32 s73, s2, 3\n"
        "s_lshl_b32 s73, s73, 12\n"
        "s_add_u32 s73, s73, s76\n"
        "v_lshlrev_b32 v9, 7, v1\n"
        "s_mov_b32 m0, 23808\n"
        "s_mov_b64 exec, 0x1ff\n"
        "buffer_load_dword v9, s[16:19], s73 offen lds\n"
        "s_lshr_b32 s73, s73, 2\n"
        "s_mov_b64 exec, 3\n"
        "buffer_load_dword v9, s[8:11], s73 offen lds\n"
        "s_mov_b64 exec, -1\n"
        "s_mov_b32 m0, s78\n"
        "s_nop 0\n"
        "buffer_load_dwordx4 v6, s[4:7], s75 offen lds\n"
        "s_add_u32 m0, m0, 0x1000\n"
        "s_add_u32 s75, s75, 0x1000\n"
        "buffer_load_dwordx4 v7, s[4:7], s75 offen lds\n"
        "s_add_u32 m0, m0, 0x1000\n"
        "s_add_u32 s75, s75, 0x1000\n"
        "buffer_load_dwordx4 v6, s[4:7], s75 offen lds\n"
        "s_add_u32 m0, m0, 0x1000\n"
        "s_add_u32 s75, s75, 0x1000\n"
        "buffer_load_dwordx4 v7, s[4:7], s75 offen lds\n"
        "s_add_u32 m0, m0, 0x1000\n"
        "s_add_u32 s75, s75, 0x1000\n"
        "buffer_load_dwordx4 v6, s[4:7], s75 offen lds\n"
        ".Lrole_a:\n"
        "s_bitcmp0_b32 s55, 0\n"
        "s_cbranch_scc1 .Lexit\n"
        "s_memrealtime s[38:39]\n"
        "s_cmp_ge_u32 s2, s56\n"
        "s_cbranch_scc1 .Lno_tile\n"
        "s_sub_u32 s73, s83, 128\n"
        "s_cmp_lt_u32 s73, 72\n"
        "s_cbranch_scc0 .Lmh\n"
        "ds_read_b32 v28, v2 offset:45516\n"
        "s_waitcnt lgkmcnt(0)\n"
        "v_readfirstlane_b32 s73, v28\n"
        "s_cmp_ge_u32 s73, 4\n"
        "s_cbranch_scc0 .Lw_15\n"
        ".Le_16:\n"
        ".Lmh:\n"
        "v_bfe_u32 v6, v1, 3, 1\n"
        "v_lshlrev_b32 v6, 14, v6\n"
        "v_lshrrev_b32 v7, 4, v1\n"
        "v_lshl_or_b32 v6, v7, 12, v6\n"
        "v_and_b32 v7, 1, v1\n"
        "v_lshl_or_b32 v6, v7, 6, v6\n"
        "v_and_b32 v7, 4, v1\n"
        "v_lshl_or_b32 v6, v7, 3, v6\n"
        "v_and_b32 v7, 2, v1\n"
        "v_lshl_or_b32 v6, v7, 3, v6\n"
        "s_lshl_b32 s73, s72, 7\n"
        "v_add_u32 v5, s73, v6\n"
        "s_mov_b32 s32, 0x8000\n"
        "s_mov_b32 s33, 0x10000\n"
        "s_mov_b32 s34, 0x18000\n"
        "buffer_load_dwordx4 v[76:79], v5, s[16:19], 0 offen offset:0\n"
        "buffer_load_dwordx4 v[80:83], v5, s[16:19], 0 offen offset:512\n"
        "buffer_load_dwordx4 v[84:87], v5, s[16:19], 0 offen offset:1024\n"
        "buffer_load_dwordx4 v[88:91], v5, s[16:19], 0 offen offset:1536\n"
        "buffer_load_dwordx4 v[92:95], v5, s[16:19], 0 offen offset:2048\n"
        "buffer_load_dwordx4 v[96:99], v5, s[16:19], 0 offen offset:2560\n"
        "buffer_load_dwordx4 v[100:103], v5, s[16:19], 0 offen offset:3072\n"
        "buffer_load_dwordx4 v[104:107], v5, s[16:19], 0 offen offset:3584\n"
        "v_lshrrev_b32 v6, 5, v1\n"
        "s_lshl_b32 s73, s72, 9\n"
        "v_lshl_add_u32 v6, v6, 2, s73\n"
        "v_lshl_add_u32 v7, v1, 2, s73\n"
        "v_add_u32 v6, 21760, v6\n"
        "v_lshrrev_b32 v30, 3, v1\n"
        "v_and_b32 v30, 4, v30\n"
        "v_xor_b32 v30, v30, v1\n"
        "s_and_b32 s40, s72, 1\n"
        "s_mul_i32 s40, s40, 3\n"
        "v_xor_b32 v30, s40, v30\n"
        "v_and_b32 v30, 31, v30\n"
        "v_lshlrev_b32 v200, 4, v30\n"
        "v_mov_b32 v31, v2\n"
        "v_writelane_b32 v31, 4, 1\n"
        "v_xor_b32 v201, 128, v200\n"
        "v_xor_b32 v202, 32, v200\n"
        "v_xor_b32 v203, 160, v200\n"
        "v_xor_b32 v204, 48, v200\n"
        "v_xor_b32 v205, 176, v200\n"
        "v_xor_b32 v206, 16, v200\n"
        "v_xor_b32 v207, 144, v200\n"
        "s_mul_i32 s74, s2, 160\n"
        "s_mul_i32 s75, s72, 40\n"
        "s_add_u32 s74, s74, s75\n"
        "v_add_u32 v29, s74, v1\n"
        "v_lshrrev_b32 v29, 2, v29\n"
        "s_sub_i32 s75, s54, s74\n"
        "s_max_i32 s75, s75, 0\n"
        "s_min_i32 s75, s75, 40\n"
        "s_bfm_b64 s[80:81], s75, 0\n"
        "s_mul_i32 s82, s72, 20480\n"
        "s_add_u32 m0, s82, 45056\n"
        "s_waitcnt vmcnt(8)\n"
        "ds_add_u32 v2, v3 offset:45508\n"
        "ds_read2_b32 v[176:177], v6 offset0:0 offset1:2\n"
        "ds_read2_b32 v[178:179], v6 offset0:4 offset1:6\n"
        "ds_read2_b32 v[180:181], v6 offset0:8 offset1:10\n"
        "ds_read2_b32 v[182:183], v6 offset0:12 offset1:14\n"
        "ds_read2_b32 v[184:185], v6 offset0:16 offset1:18\n"
        "ds_read2_b32 v[186:187], v6 offset0:20 offset1:22\n"
        "ds_read2_b32 v[188:189], v6 offset0:24 offset1:26\n"
        "ds_read2_b32 v[190:191], v6 offset0:28 offset1:30\n"
        "ds_read2_b32 v[192:193], v6 offset0:32 offset1:34\n"
        "ds_read2_b32 v[194:195], v6 offset0:36 offset1:38\n"
        "ds_read_b32 v28, v7 offset:22016\n"
        "s_waitcnt lgkmcnt(0)\n"
        "s_add_u32 s73, s64, 1\n"
        "s_addc_u32 s74, s65, 0\n"
        "s_mul_i32 s76, s73, 0x7f4a7c15\n"
        "s_mul_hi_u32 s77, s73, 0x7f4a7c15\n"
        "s_mul_i32 s75, s73, 0x9e3779b9\n"
        "s_add_u32 s77, s77, s75\n"
        "s_mul_i32 s75, s74, 0x7f4a7c15\n"
        "s_add_u32 s77, s77, s75\n"
        "s_xor_b32 s66, s76, 0xd192ed03\n"
        "s_or_b32 s66, s66, 3\n"
        "s_xor_b32 s67, s77, 0xd1b54a32\n"
        "v_cmp_ne_u32 vcc, v28, v29\n"
        "s_and_b64 s[80:81], s[80:81], vcc\n"
        "s_cselect_b32 s73, 0x101, 1\n"
        "s_bitcmp1_b32 s55, 1\n"
        "s_cselect_b32 s73, s73, 0x101\n"
        "v_mov_b32 v28, s73\n"
        "ds_add_u32 v2, v28 offset:45480\n"
        "s_cmp_lg_u32 s3, 4\n"
        "s_cbranch_scc1 .Lflag_done\n"
        "s_mov_b32 s74, 0x40000\n"
        ".Lep:\n"
        "ds_read_b32 v28, v2 offset:45480\n"
        "s_waitcnt lgkmcnt(0)\n"
        "v_readfirstlane_b32 s73, v28\n"
        "s_and_b32 s75, s73, 255\n"
        "s_cmp_ge_u32 s75, 8\n"
        "s_cbranch_scc1 .Lep_done\n"
        "s_sleep 1\n"
        "s_sub_u32 s74, s74, 1\n"
        "s_cbranch_scc0 .Lep\n"
        ".Lep_done:\n"
        "s_xor_b32 s74, s66, 2\n"
        "s_cmp_gt_u32 s73, 255\n"
        "s_cselect_b32 s35, s74, s66\n"
        "s_lshl_b32 s73, s2, 3\n"
        "s_add_u32 s73, s73, 256\n"
        "v_mov_b32 v28, s35\n"
        "v_mov_b32 v29, s67\n"
        "s_mov_b64 exec, 1\n"
        "buffer_store_dwordx2 v[28:29], v2, s[44:47], s73 offen sc1\n"
        "s_mov_b64 exec, -1\n"
        ".Lflag_done:\n"
        "ds_read_b32 v28, v2 offset:45500\n"
        "s_waitcnt lgkmcnt(0)\n"
        "v_readfirstlane_b32 s73, v28\n"
        "s_cmp_ge_u32 s73, 4\n"
        "s_cbranch_scc0 .Lw_17\n"
        ".Le_18:\n"
        "s_bfe_u32 s57, s2, 0x10003\n"
        "s_add_u32 m0, m0, 0x400\n"
        "v_lshl_or_b32 v176, v176, 9, v200\n"
        "buffer_load_dwordx4 v176, s[4:7], 0 offen lds\n"
        "s_add_u32 m0, m0, 0x400\n"
        "v_lshl_or_b32 v177, v177, 9, v201\n"
        "buffer_load_dwordx4 v177, s[4:7], 0 offen lds\n"
        "s_add_u32 m0, m0, 0x400\n"
        "v_lshl_or_b32 v178, v178, 9, v202\n"
        "buffer_load_dwordx4 v178, s[4:7], 0 offen lds\n"
        "s_add_u32 m0, m0, 0x400\n"
        "v_lshl_or_b32 v179, v179, 9, v203\n"
        "buffer_load_dwordx4 v179, s[4:7], 0 offen lds\n"
        "s_add_u32 m0, m0, 0x400\n"
        "v_lshl_or_b32 v180, v180, 9, v204\n"
        "buffer_load_dwordx4 v180, s[4:7], 0 offen lds\n"
        "s_add_u32 m0, m0, 0x400\n"
        "v_lshl_or_b32 v181, v181, 9, v205\n"
        "buffer_load_dwordx4 v181, s[4:7], 0 offen lds\n"
        "s_cmp_lg_u32 s57, 0\n"
        "s_cbranch_scc1 .Lk_19\n"
        "s_add_u32 m0, m0, 0x400\n"
        "v_lshl_or_b32 v182, v182, 9, v206\n"
        "buffer_load_dwordx4 v182, s[4:7], 0 offen lds\n"
        "s_add_u32 m0, m0, 0x400\n"
        "v_lshl_or_b32 v183, v183, 9, v207\n"
        "buffer_load_dwordx4 v183, s[4:7], 0 offen lds\n"
        "s_add_u32 m0, m0, 0x400\n"
        "v_lshl_or_b32 v184, v184, 9, v200\n"
        "buffer_load_dwordx4 v184, s[4:7], 0 offen lds\n"
        "s_add_u32 m0, m0, 0x400\n"
        "v_lshl_or_b32 v185, v185, 9, v201\n"
        "buffer_load_dwordx4 v185, s[4:7], 0 offen lds\n"
        "s_add_u32 m0, m0, 0x400\n"
        "v_lshl_or_b32 v186, v186, 9, v202\n"
        "buffer_load_dwordx4 v186, s[4:7], 0 offen lds\n"
        "s_add_u32 m0, m0, 0x400\n"
        "v_lshl_or_b32 v187, v187, 9, v203\n"
        "buffer_load_dwordx4 v187, s[4:7], 0 offen lds\n"
        "ds_read_b32 v28, v2 offset:45516\n"
        "s_waitcnt lgkmcnt(0)\n"
        "v_readfirstlane_b32 s73, v28\n"
        "s_cmp_ge_u32 s73, 4\n"
        "s_cbranch_scc0 .Lw_20\n"
        ".Le_21:\n"
        "s_add_u32 m0, m0, 0x400\n"
        "v_lshl_or_b32 v188, v188, 9, v204\n"
        "buffer_load_dwordx4 v188, s[4:7], 0 offen lds\n"
        "s_add_u32 m0, m0, 0x400\n"
        "v_lshl_or_b32 v189, v189, 9, v205\n"
        "buffer_load_dwordx4 v189, s[4:7], 0 offen lds\n"
        ".Lk_19:\n"
        "buffer_load_dwordx4 v[108:111], v5, s[16:19], s32 offen offset:0\n"
        "buffer_load_dwordx4 v[112:115], v5, s[16:19], s32 offen offset:512\n"
        "buffer_load_dwordx4 v[116:119], v5, s[16:19], s32 offen offset:1024\n"
        "buffer_load_dwordx4 v[120:123], v5, s[16:19], s32 offen offset:1536\n"
        "buffer_load_dwordx4 v[124:127], v5, s[16:19], s32 offen offset:2048\n"
        "buffer_load_dwordx4 v[128:131], v5, s[16:19], s32 offen offset:2560\n"
        "buffer_load_dwordx4 v[132:135], v5, s[16:19], s32 offen offset:3072\n"
        "buffer_load_dwordx4 v[136:139], v5, s[16:19], s32 offen offset:3584\n"
        "buffer_load_dwordx4 v[140:143], v5, s[16:19], s33 offen offset:0\n"
        "buffer_load_dwordx4 v[144:147], v5, s[16:19], s33 offen offset:512\n"
        "buffer_load_dwordx4 v[148:151], v5, s[16:19], s33 offen offset:1024\n"
        "buffer_load_dwordx4 v[152:155], v5, s[16:19], s33 offen offset:1536\n"
        "buffer_load_dwordx4 v[156:159], v5, s[16:19], s33 offen offset:2048\n"
        "buffer_load_dwordx4 v[160:163], v5, s[16:19], s33 offen offset:2560\n"
        "buffer_load_dwordx4 v[164:167], v5, s[16:19], s33 offen offset:3072\n"
        "buffer_load_dwordx4 v[168:171], v5, s[16:19], s33 offen offset:3584\n"
        "s_mov_b32 s78, 0x05040100\n"
        "s_mov_b32 s79, 0x07060302\n"
        "s_cmp_eq_u32 s57, 0\n"
        "s_cbranch_scc1 .Ld_22\n"
        "s_waitcnt vmcnt(22)\n"
        ".Ld_22:\n"
        "s_waitcnt vmcnt(30)\n"
        "v_mov_b32 v172, v76\n"
        "v_mov_b32 v173, v77\n"
        "v_mov_b32 v174, v80\n"
        "v_mov_b32 v175, v81\n"
        "v_mov_b32_dpp v76, v78 row_ror:8 row_mask:0xf bank_mask:0xc\n"
        "v_mov_b32_dpp v77, v79 row_ror:8 row_mask:0xf bank_mask:0xc\n"
        "v_mov_b32_dpp v80, v82 row_ror:8 row_mask:0xf bank_mask:0xc\n"
        "v_mov_b32_dpp v81, v83 row_ror:8 row_mask:0xf bank_mask:0xc\n"
        "v_mov_b32_dpp v78, v172 row_ror:8 row_mask:0xf bank_mask:0x3\n"
        "v_mov_b32_dpp v79, v173 row_ror:8 row_mask:0xf bank_mask:0x3\n"
        "v_mov_b32_dpp v82, v174 row_ror:8 row_mask:0xf bank_mask:0x3\n"
        "v_mov_b32_dpp v83, v175 row_ror:8 row_mask:0xf bank_mask:0x3\n"
        "v_perm_b32 v44, v80, v76, s78\n"
        "v_perm_b32 v52, v80, v76, s79\n"
        "v_perm_b32 v60, v81, v77, s78\n"
        "v_perm_b32 v68, v81, v77, s79\n"
        "v_perm_b32 v48, v82, v78, s78\n"
        "v_perm_b32 v56, v82, v78, s79\n"
        "v_perm_b32 v64, v83, v79, s78\n"
        "v_perm_b32 v72, v83, v79, s79\n"
        "v_mov_b32 v172, v84\n"
        "v_mov_b32 v173, v85\n"
        "v_mov_b32 v174, v88\n"
        "v_mov_b32 v175, v89\n"
        "v_mov_b32_dpp v84, v86 row_ror:8 row_mask:0xf bank_mask:0xc\n"
        "v_mov_b32_dpp v85, v87 row_ror:8 row_mask:0xf bank_mask:0xc\n"
        "v_mov_b32_dpp v88, v90 row_ror:8 row_mask:0xf bank_mask:0xc\n"
        "v_mov_b32_dpp v89, v91 row_ror:8 row_mask:0xf bank_mask:0xc\n"
        "v_mov_b32_dpp v86, v172 row_ror:8 row_mask:0xf bank_mask:0x3\n"
        "v_mov_b32_dpp v87, v173 row_ror:8 row_mask:0xf bank_mask:0x3\n"
        "v_mov_b32_dpp v90, v174 row_ror:8 row_mask:0xf bank_mask:0x3\n"
        "v_mov_b32_dpp v91, v175 row_ror:8 row_mask:0xf bank_mask:0x3\n"
        "v_perm_b32 v45, v88, v84, s78\n"
        "v_perm_b32 v53, v88, v84, s79\n"
        "v_perm_b32 v61, v89, v85, s78\n"
        "v_perm_b32 v69, v89, v85, s79\n"
        "v_perm_b32 v49, v90, v86, s78\n"
        "v_perm_b32 v57, v90, v86, s79\n"
        "v_perm_b32 v65, v91, v87, s78\n"
        "v_perm_b32 v73, v91, v87, s79\n"
        "v_mov_b32 v172, v92\n"
        "v_mov_b32 v173, v93\n"
        "v_mov_b32 v174, v96\n"
        "v_mov_b32 v175, v97\n"
        "v_mov_b32_dpp v92, v94 row_ror:8 row_mask:0xf bank_mask:0xc\n"
        "v_mov_b32_dpp v93, v95 row_ror:8 row_mask:0xf bank_mask:0xc\n"
        "v_mov_b32_dpp v96, v98 row_ror:8 row_mask:0xf bank_mask:0xc\n"
        "v_mov_b32_dpp v97, v99 row_ror:8 row_mask:0xf bank_mask:0xc\n"
        "v_mov_b32_dpp v94, v172 row_ror:8 row_mask:0xf bank_mask:0x3\n"
        "v_mov_b32_dpp v95, v173 row_ror:8 row_mask:0xf bank_mask:0x3\n"
        "v_mov_b32_dpp v98, v174 row_ror:8 row_mask:0xf bank_mask:0x3\n"
        "v_mov_b32_dpp v99, v175 row_ror:8 row_mask:0xf bank_mask:0x3\n"
        "v_perm_b32 v46, v96, v92, s78\n"
        "v_perm_b32 v54, v96, v92, s79\n"
        "v_perm_b32 v62, v97, v93, s78\n"
        "v_perm_b32 v70, v97, v93, s79\n"
        "v_perm_b32 v50, v98, v94, s78\n"
        "v_perm_b32 v58, v98, v94, s79\n"
        "v_perm_b32 v66, v99, v95, s78\n"
        "v_perm_b32 v74, v99, v95, s79\n"
        "v_mov_b32 v172, v100\n"
        "v_mov_b32 v173, v101\n"
        "v_mov_b32 v174, v104\n"
        "v_mov_b32 v175, v105\n"
        "v_mov_b32_dpp v100, v102 row_ror:8 row_mask:0xf bank_mask:0xc\n"
        "v_mov_b32_dpp v101, v103 row_ror:8 row_mask:0xf bank_mask:0xc\n"
        "v_mov_b32_dpp v104, v106 row_ror:8 row_mask:0xf bank_mask:0xc\n"
        "v_mov_b32_dpp v105, v107 row_ror:8 row_mask:0xf bank_mask:0xc\n"
        "v_mov_b32_dpp v102, v172 row_ror:8 row_mask:0xf bank_mask:0x3\n"
        "v_mov_b32_dpp v103, v173 row_ror:8 row_mask:0xf bank_mask:0x3\n"
        "v_mov_b32_dpp v106, v174 row_ror:8 row_mask:0xf bank_mask:0x3\n"
        "v_mov_b32_dpp v107, v175 row_ror:8 row_mask:0xf bank_mask:0x3\n"
        "v_perm_b32 v47, v104, v100, s78\n"
        "v_perm_b32 v55, v104, v100, s79\n"
        "v_perm_b32 v63, v105, v101, s78\n"
        "v_perm_b32 v71, v105, v101, s79\n"
        "v_perm_b32 v51, v106, v102, s78\n"
        "v_perm_b32 v59, v106, v102, s79\n"
        "v_perm_b32 v67, v107, v103, s78\n"
        "v_perm_b32 v75, v107, v103, s79\n"
        "s_mov_b32 s73, s82\n"
        "v_and_b32 v8, 15, v1\n"
        "v_lshrrev_b32 v9, 4, v1\n"
        "v_xor_b32 v9, v9, v8\n"
        "v_and_b32 v9, 3, v9\n"
        "v_and_or_b32 v9, v1, 12, v9\n"
        "v_lshlrev_b32 v8, 9, v8\n"
        "v_lshl_add_u32 v36, v9, 4, v8\n"
        "v_add_u32 v36, 24064, v36\n"
        "v_xor_b32 v37, 64, v36\n"
        "v_xor_b32 v38, 128, v36\n"
        "v_xor_b32 v39, 192, v36\n"
        "v_mov_b64 v[12:13], 0\n"
        "s_lshl_b32 s74, s72, 9\n"
        "v_lshl_add_u32 v8, v1, 3, s74\n"
        "ds_write_b64 v8, v[12:13] offset:21760\n"
        "v_and_b32 v8, 15, v1\n"
        "v_lshrrev_b32 v9, 4, v1\n"
        "v_mov_b32 v5, v4\n"
        "s_movk_i32 s43, 256\n"
        "s_mov_b32 s61, 1\n"
        "v_lshrrev_b32 v10, 2, v8\n"
        "v_lshrrev_b32 v11, 3, v8\n"
        "v_xor_b32 v10, v10, v11\n"
        "v_and_b32 v10, 1, v10\n"
        "v_lshl_or_b32 v10, v10, 1, v11\n"
        "v_and_b32 v11, 3, v8\n"
        "v_lshl_or_b32 v10, v11, 2, v10\n"
        "v_xor_b32 v10, s40, v10\n"
        "v_xor_b32 v10, v10, v9\n"
        "s_add_u32 s73, s73, 46080\n"
        "v_lshl_add_u32 v12, v8, 9, s73\n"
        "v_lshl_add_u32 v6, v10, 4, v12\n"
        "v_mul_u32_u24 v10, 528, v8\n"
        "v_lshl_add_u32 v23, v9, 4, v10\n"
        "s_mul_i32 s74, s72, 5280\n"
        "v_add_u32 v7, s74, v23\n"
        "s_lshl_b32 s73, s72, 7\n"
        "v_add_u32 v41, s73, v23\n"
        "s_mul_i32 s74, s72, 160\n"
        "v_lshl_add_u32 v10, v8, 4, s74\n"
        "v_and_b32 v12, 1, v8\n"
        "v_lshl_add_u32 v40, v12, 3, v10\n"
        "v_and_b32 v10, 12, v8\n"
        "v_and_b32 v12, 1, v9\n"
        "v_mad_u32_u24 v10, v12, 3, v10\n"
        "s_lshl_b32 s74, s72, 3\n"
        "v_add_u32 v11, s74, v11\n"
        "v_xor_b32 v10, v10, v11\n"
        "v_lshrrev_b32 v11, 2, v8\n"
        "v_lshl_add_u32 v11, v9, 3, v11\n"
        "v_lshlrev_b32 v11, 9, v11\n"
        "v_lshl_add_u32 v11, v10, 4, v11\n"
        "v_add_u32 v20, 46080, v11\n"
        "v_lshrrev_b32 v10, 1, v8\n"
        "v_xor_b32 v10, v10, v9\n"
        "v_lshlrev_b32 v11, 4, v8\n"
        "v_mov_b32 v12, 768\n"
        "v_cmp_eq_u32 s[74:75], 0, v10\n"
        "v_cmp_eq_u32 s[76:77], 4, v10\n"
        "s_nop 0\n"
        "v_cndmask_b32 v21, v12, v11, s[74:75]\n"
        "v_cndmask_b32 v22, v12, v11, s[76:77]\n"
        "v_mul_u32_u24 v10, 544, v8\n"
        "v_lshl_add_u32 v10, v9, 4, v10\n"
        "v_add_u32 v34, 0, v10\n"
        "v_and_b32 v10, 1, v9\n"
        "v_lshlrev_b32 v10, 5, v10\n"
        "v_lshl_or_b32 v10, v8, 9, v10\n"
        "v_lshrrev_b32 v11, 1, v9\n"
        "v_lshl_or_b32 v35, v11, 4, v10\n"
        "v_and_b32 v10, 1, v8\n"
        "v_mul_u32_u24 v10, 0xaaaa, v10\n"
        "v_add_u32 v0, 0x4444, v10\n"
        "s_lshl_b32 s8, s72, 15\n"
        "s_add_u32 s8, s8, 8192\n"
        "s_add_u32 s9, s8, 4096\n"
        "s_add_u32 s10, s8, 8192\n"
        "s_add_u32 s11, s8, 12288\n"
        "s_add_u32 s12, s8, 16384\n"
        "s_add_u32 s13, s8, 20480\n"
        "s_add_u32 s14, s8, 24576\n"
        "s_add_u32 s15, s8, 28672\n"
        "s_lshl_b32 s16, s72, 16\n"
        "s_add_u32 s16, s16, 139264\n"
        "s_lshl_b32 s73, s72, 9\n"
        "s_add_u32 s73, s73, 2304\n"
        "s_lshl_b32 s74, s72, 6\n"
        "s_add_u32 s74, s74, 3840\n"
        "v_mov_b32 v42, s73\n"
        "v_mov_b32 v43, s74\n"
        "v_cmp_gt_u32 s[76:77], 32, v1\n"
        "s_nop 1\n"
        "v_cndmask_b32 v42, v43, v42, s[76:77]\n"
        "v_add_u32 v42, v42, v4\n"
        ".Lpda:\n"
        "s_memrealtime s[36:37]\n"
        "s_waitcnt lgkmcnt(0)\n"
        "s_sub_u32 s73, s36, s38\n"
        "s_cmp_ge_u32 s73, 200\n"
        "s_cbranch_scc1 .Lpoll\n"
        "s_sleep 2\n"
        "s_branch .Lpda\n"
        ".Lpoll:\n"
        "s_setprio 3\n"
        "s_lshl_b32 s41, s72, 9\n"
        "s_add_u32 s41, s41, 2304\n"
        "s_mov_b32 exec_hi, 0\n"
        "buffer_load_dwordx4 v[24:27], v4, s[44:47], s41 offen sc1\n"
        "s_mov_b32 exec_hi, -1\n"
        "buffer_load_dwordx4 v[224:227], v4, s[44:47], s8 offen offset:0 sc1\n"
        "buffer_load_dwordx4 v[228:231], v4, s[44:47], s10 offen offset:0 sc1\n"
        "buffer_load_dwordx4 v[232:235], v4, s[44:47], s12 offen offset:0 sc1\n"
        "buffer_load_dwordx4 v[236:239], v4, s[44:47], s14 offen offset:0 sc1\n"
        "buffer_load_dwordx4 v[240:243], v4, s[44:47], s8 offen offset:1024 sc1\n"
        "buffer_load_dwordx4 v[244:247], v4, s[44:47], s10 offen offset:1024 sc1\n"
        "buffer_load_dwordx4 v[248:251], v4, s[44:47], s12 offen offset:1024 sc1\n"
        "buffer_load_dwordx4 v[252:255], v4, s[44:47], s14 offen offset:1024 sc1\n"
        "s_mov_b32 s78, 0x05040100\n"
        "s_mov_b32 s79, 0x07060302\n"
        "s_waitcnt vmcnt(17)\n"
        "v_mov_b32 v172, v108\n"
        "v_mov_b32 v173, v109\n"
        "v_mov_b32 v174, v112\n"
        "v_mov_b32 v175, v113\n"
        "v_mov_b32_dpp v108, v110 row_ror:8 row_mask:0xf bank_mask:0xc\n"
        "v_mov_b32_dpp v109, v111 row_ror:8 row_mask:0xf bank_mask:0xc\n"
        "v_mov_b32_dpp v112, v114 row_ror:8 row_mask:0xf bank_mask:0xc\n"
        "v_mov_b32_dpp v113, v115 row_ror:8 row_mask:0xf bank_mask:0xc\n"
        "v_mov_b32_dpp v110, v172 row_ror:8 row_mask:0xf bank_mask:0x3\n"
        "v_mov_b32_dpp v111, v173 row_ror:8 row_mask:0xf bank_mask:0x3\n"
        "v_mov_b32_dpp v114, v174 row_ror:8 row_mask:0xf bank_mask:0x3\n"
        "v_mov_b32_dpp v115, v175 row_ror:8 row_mask:0xf bank_mask:0x3\n"
        "v_perm_b32 v76, v112, v108, s78\n"
        "v_perm_b32 v84, v112, v108, s79\n"
        "v_perm_b32 v92, v113, v109, s78\n"
        "v_perm_b32 v100, v113, v109, s79\n"
        "v_perm_b32 v80, v114, v110, s78\n"
        "v_perm_b32 v88, v114, v110, s79\n"
        "v_perm_b32 v96, v115, v111, s78\n"
        "v_perm_b32 v104, v115, v111, s79\n"
        "v_mov_b32 v172, v116\n"
        "v_mov_b32 v173, v117\n"
        "v_mov_b32 v174, v120\n"
        "v_mov_b32 v175, v121\n"
        "v_mov_b32_dpp v116, v118 row_ror:8 row_mask:0xf bank_mask:0xc\n"
        "v_mov_b32_dpp v117, v119 row_ror:8 row_mask:0xf bank_mask:0xc\n"
        "v_mov_b32_dpp v120, v122 row_ror:8 row_mask:0xf bank_mask:0xc\n"
        "v_mov_b32_dpp v121, v123 row_ror:8 row_mask:0xf bank_mask:0xc\n"
        "v_mov_b32_dpp v118, v172 row_ror:8 row_mask:0xf bank_mask:0x3\n"
        "v_mov_b32_dpp v119, v173 row_ror:8 row_mask:0xf bank_mask:0x3\n"
        "v_mov_b32_dpp v122, v174 row_ror:8 row_mask:0xf bank_mask:0x3\n"
        "v_mov_b32_dpp v123, v175 row_ror:8 row_mask:0xf bank_mask:0x3\n"
        "v_perm_b32 v77, v120, v116, s78\n"
        "v_perm_b32 v85, v120, v116, s79\n"
        "v_perm_b32 v93, v121, v117, s78\n"
        "v_perm_b32 v101, v121, v117, s79\n"
        "v_perm_b32 v81, v122, v118, s78\n"
        "v_perm_b32 v89, v122, v118, s79\n"
        "v_perm_b32 v97, v123, v119, s78\n"
        "v_perm_b32 v105, v123, v119, s79\n"
        "v_mov_b32 v172, v124\n"
        "v_mov_b32 v173, v125\n"
        "v_mov_b32 v174, v128\n"
        "v_mov_b32 v175, v129\n"
        "v_mov_b32_dpp v124, v126 row_ror:8 row_mask:0xf bank_mask:0xc\n"
        "v_mov_b32_dpp v125, v127 row_ror:8 row_mask:0xf bank_mask:0xc\n"
        "v_mov_b32_dpp v128, v130 row_ror:8 row_mask:0xf bank_mask:0xc\n"
        "v_mov_b32_dpp v129, v131 row_ror:8 row_mask:0xf bank_mask:0xc\n"
        "v_mov_b32_dpp v126, v172 row_ror:8 row_mask:0xf bank_mask:0x3\n"
        "v_mov_b32_dpp v127, v173 row_ror:8 row_mask:0xf bank_mask:0x3\n"
        "v_mov_b32_dpp v130, v174 row_ror:8 row_mask:0xf bank_mask:0x3\n"
        "v_mov_b32_dpp v131, v175 row_ror:8 row_mask:0xf bank_mask:0x3\n"
        "v_perm_b32 v78, v128, v124, s78\n"
        "v_perm_b32 v86, v128, v124, s79\n"
        "v_perm_b32 v94, v129, v125, s78\n"
        "v_perm_b32 v102, v129, v125, s79\n"
        "v_perm_b32 v82, v130, v126, s78\n"
        "v_perm_b32 v90, v130, v126, s79\n"
        "v_perm_b32 v98, v131, v127, s78\n"
        "v_perm_b32 v106, v131, v127, s79\n"
        "v_mov_b32 v172, v132\n"
        "v_mov_b32 v173, v133\n"
        "v_mov_b32 v174, v136\n"
        "v_mov_b32 v175, v137\n"
        "v_mov_b32_dpp v132, v134 row_ror:8 row_mask:0xf bank_mask:0xc\n"
        "v_mov_b32_dpp v133, v135 row_ror:8 row_mask:0xf bank_mask:0xc\n"
        "v_mov_b32_dpp v136, v138 row_ror:8 row_mask:0xf bank_mask:0xc\n"
        "v_mov_b32_dpp v137, v139 row_ror:8 row_mask:0xf bank_mask:0xc\n"
        "v_mov_b32_dpp v134, v172 row_ror:8 row_mask:0xf bank_mask:0x3\n"
        "v_mov_b32_dpp v135, v173 row_ror:8 row_mask:0xf bank_mask:0x3\n"
        "v_mov_b32_dpp v138, v174 row_ror:8 row_mask:0xf bank_mask:0x3\n"
        "v_mov_b32_dpp v139, v175 row_ror:8 row_mask:0xf bank_mask:0x3\n"
        "v_perm_b32 v79, v136, v132, s78\n"
        "v_perm_b32 v87, v136, v132, s79\n"
        "v_perm_b32 v95, v137, v133, s78\n"
        "v_perm_b32 v103, v137, v133, s79\n"
        "v_perm_b32 v83, v138, v134, s78\n"
        "v_perm_b32 v91, v138, v134, s79\n"
        "v_perm_b32 v99, v139, v135, s78\n"
        "v_perm_b32 v107, v139, v135, s79\n"
        "s_waitcnt vmcnt(9)\n"
        "v_mov_b32 v172, v140\n"
        "v_mov_b32 v173, v141\n"
        "v_mov_b32 v174, v144\n"
        "v_mov_b32 v175, v145\n"
        "v_mov_b32_dpp v140, v142 row_ror:8 row_mask:0xf bank_mask:0xc\n"
        "v_mov_b32_dpp v141, v143 row_ror:8 row_mask:0xf bank_mask:0xc\n"
        "v_mov_b32_dpp v144, v146 row_ror:8 row_mask:0xf bank_mask:0xc\n"
        "v_mov_b32_dpp v145, v147 row_ror:8 row_mask:0xf bank_mask:0xc\n"
        "v_mov_b32_dpp v142, v172 row_ror:8 row_mask:0xf bank_mask:0x3\n"
        "v_mov_b32_dpp v143, v173 row_ror:8 row_mask:0xf bank_mask:0x3\n"
        "v_mov_b32_dpp v146, v174 row_ror:8 row_mask:0xf bank_mask:0x3\n"
        "v_mov_b32_dpp v147, v175 row_ror:8 row_mask:0xf bank_mask:0x3\n"
        "v_perm_b32 v108, v144, v140, s78\n"
        "v_perm_b32 v116, v144, v140, s79\n"
        "v_perm_b32 v124, v145, v141, s78\n"
        "v_perm_b32 v132, v145, v141, s79\n"
        "v_perm_b32 v112, v146, v142, s78\n"
        "v_perm_b32 v120, v146, v142, s79\n"
        "v_perm_b32 v128, v147, v143, s78\n"
        "v_perm_b32 v136, v147, v143, s79\n"
        "v_mov_b32 v172, v148\n"
        "v_mov_b32 v173, v149\n"
        "v_mov_b32 v174, v152\n"
        "v_mov_b32 v175, v153\n"
        "v_mov_b32_dpp v148, v150 row_ror:8 row_mask:0xf bank_mask:0xc\n"
        "v_mov_b32_dpp v149, v151 row_ror:8 row_mask:0xf bank_mask:0xc\n"
        "v_mov_b32_dpp v152, v154 row_ror:8 row_mask:0xf bank_mask:0xc\n"
        "v_mov_b32_dpp v153, v155 row_ror:8 row_mask:0xf bank_mask:0xc\n"
        "v_mov_b32_dpp v150, v172 row_ror:8 row_mask:0xf bank_mask:0x3\n"
        "v_mov_b32_dpp v151, v173 row_ror:8 row_mask:0xf bank_mask:0x3\n"
        "v_mov_b32_dpp v154, v174 row_ror:8 row_mask:0xf bank_mask:0x3\n"
        "v_mov_b32_dpp v155, v175 row_ror:8 row_mask:0xf bank_mask:0x3\n"
        "v_perm_b32 v109, v152, v148, s78\n"
        "v_perm_b32 v117, v152, v148, s79\n"
        "v_perm_b32 v125, v153, v149, s78\n"
        "v_perm_b32 v133, v153, v149, s79\n"
        "v_perm_b32 v113, v154, v150, s78\n"
        "v_perm_b32 v121, v154, v150, s79\n"
        "v_perm_b32 v129, v155, v151, s78\n"
        "v_perm_b32 v137, v155, v151, s79\n"
        "v_mov_b32 v172, v156\n"
        "v_mov_b32 v173, v157\n"
        "v_mov_b32 v174, v160\n"
        "v_mov_b32 v175, v161\n"
        "v_mov_b32_dpp v156, v158 row_ror:8 row_mask:0xf bank_mask:0xc\n"
        "v_mov_b32_dpp v157, v159 row_ror:8 row_mask:0xf bank_mask:0xc\n"
        "v_mov_b32_dpp v160, v162 row_ror:8 row_mask:0xf bank_mask:0xc\n"
        "v_mov_b32_dpp v161, v163 row_ror:8 row_mask:0xf bank_mask:0xc\n"
        "v_mov_b32_dpp v158, v172 row_ror:8 row_mask:0xf bank_mask:0x3\n"
        "v_mov_b32_dpp v159, v173 row_ror:8 row_mask:0xf bank_mask:0x3\n"
        "v_mov_b32_dpp v162, v174 row_ror:8 row_mask:0xf bank_mask:0x3\n"
        "v_mov_b32_dpp v163, v175 row_ror:8 row_mask:0xf bank_mask:0x3\n"
        "v_perm_b32 v110, v160, v156, s78\n"
        "v_perm_b32 v118, v160, v156, s79\n"
        "v_perm_b32 v126, v161, v157, s78\n"
        "v_perm_b32 v134, v161, v157, s79\n"
        "v_perm_b32 v114, v162, v158, s78\n"
        "v_perm_b32 v122, v162, v158, s79\n"
        "v_perm_b32 v130, v163, v159, s78\n"
        "v_perm_b32 v138, v163, v159, s79\n"
        "v_mov_b32 v172, v164\n"
        "v_mov_b32 v173, v165\n"
        "v_mov_b32 v174, v168\n"
        "v_mov_b32 v175, v169\n"
        "v_mov_b32_dpp v164, v166 row_ror:8 row_mask:0xf bank_mask:0xc\n"
        "v_mov_b32_dpp v165, v167 row_ror:8 row_mask:0xf bank_mask:0xc\n"
        "v_mov_b32_dpp v168, v170 row_ror:8 row_mask:0xf bank_mask:0xc\n"
        "v_mov_b32_dpp v169, v171 row_ror:8 row_mask:0xf bank_mask:0xc\n"
        "v_mov_b32_dpp v166, v172 row_ror:8 row_mask:0xf bank_mask:0x3\n"
        "v_mov_b32_dpp v167, v173 row_ror:8 row_mask:0xf bank_mask:0x3\n"
        "v_mov_b32_dpp v170, v174 row_ror:8 row_mask:0xf bank_mask:0x3\n"
        "v_mov_b32_dpp v171, v175 row_ror:8 row_mask:0xf bank_mask:0x3\n"
        "v_perm_b32 v111, v168, v164, s78\n"
        "v_perm_b32 v119, v168, v164, s79\n"
        "v_perm_b32 v127, v169, v165, s78\n"
        "v_perm_b32 v135, v169, v165, s79\n"
        "v_perm_b32 v115, v170, v166, s78\n"
        "v_perm_b32 v123, v170, v166, s79\n"
        "v_perm_b32 v131, v171, v167, s78\n"
        "v_perm_b32 v139, v171, v167, s79\n"
        "buffer_load_dwordx4 v[156:159], v4, s[44:47], s8 offen offset:2048 sc1\n"
        "buffer_load_dwordx4 v[160:163], v4, s[44:47], s10 offen offset:2048 sc1\n"
        "buffer_load_dwordx4 v[164:167], v4, s[44:47], s12 offen offset:2048 sc1\n"
        "buffer_load_dwordx4 v[168:171], v4, s[44:47], s14 offen offset:2048 sc1\n"
        "buffer_load_dwordx4 v[140:143], v4, s[44:47], s8 offen offset:3072 sc1\n"
        "buffer_load_dwordx4 v[144:147], v4, s[44:47], s10 offen offset:3072 sc1\n"
        "buffer_load_dwordx4 v[148:151], v4, s[44:47], s12 offen offset:3072 sc1\n"
        "buffer_load_dwordx4 v[152:155], v4, s[44:47], s14 offen offset:3072 sc1\n"
        "ds_add_u32 v2, v3 offset:45520\n"
        "s_setprio 3\n"
        "ds_read_b32 v28, v2 offset:45516\n"
        "s_waitcnt lgkmcnt(0)\n"
        "v_readfirstlane_b32 s73, v28\n"
        "s_cmp_ge_u32 s73, 4\n"
        "s_cbranch_scc0 .Lw_23\n"
        ".Le_24:\n"
        "s_cmp_lg_u32 s57, 1\n"
        "s_cbranch_scc1 .Lk_25\n"
        "s_add_u32 m0, m0, 0x400\n"
        "v_lshl_or_b32 v182, v182, 9, v206\n"
        "buffer_load_dwordx4 v182, s[4:7], 0 offen lds\n"
        "s_add_u32 m0, m0, 0x400\n"
        "v_lshl_or_b32 v183, v183, 9, v207\n"
        "buffer_load_dwordx4 v183, s[4:7], 0 offen lds\n"
        "s_add_u32 m0, m0, 0x400\n"
        "v_lshl_or_b32 v184, v184, 9, v200\n"
        "buffer_load_dwordx4 v184, s[4:7], 0 offen lds\n"
        "s_add_u32 m0, m0, 0x400\n"
        "v_lshl_or_b32 v185, v185, 9, v201\n"
        "buffer_load_dwordx4 v185, s[4:7], 0 offen lds\n"
        "s_add_u32 m0, m0, 0x400\n"
        "v_lshl_or_b32 v186, v186, 9, v202\n"
        "buffer_load_dwordx4 v186, s[4:7], 0 offen lds\n"
        "s_add_u32 m0, m0, 0x400\n"
        "v_lshl_or_b32 v187, v187, 9, v203\n"
        "buffer_load_dwordx4 v187, s[4:7], 0 offen lds\n"
        "s_add_u32 m0, m0, 0x400\n"
        "v_lshl_or_b32 v188, v188, 9, v204\n"
        "buffer_load_dwordx4 v188, s[4:7], 0 offen lds\n"
        "s_add_u32 m0, m0, 0x400\n"
        "v_lshl_or_b32 v189, v189, 9, v205\n"
        "buffer_load_dwordx4 v189, s[4:7], 0 offen lds\n"
        ".Lk_25:\n"
        "s_add_u32 m0, m0, 0x400\n"
        "v_lshl_or_b32 v190, v190, 9, v206\n"
        "buffer_load_dwordx4 v190, s[4:7], 0 offen lds\n"
        "s_add_u32 m0, m0, 0x400\n"
        "v_lshl_or_b32 v191, v191, 9, v207\n"
        "buffer_load_dwordx4 v191, s[4:7], 0 offen lds\n"
        "s_add_u32 m0, m0, 0x400\n"
        "v_lshl_or_b32 v192, v192, 9, v200\n"
        "buffer_load_dwordx4 v192, s[4:7], 0 offen lds\n"
        "s_add_u32 m0, m0, 0x400\n"
        "v_lshl_or_b32 v193, v193, 9, v201\n"
        "buffer_load_dwordx4 v193, s[4:7], 0 offen lds\n"
        "s_add_u32 m0, m0, 0x400\n"
        "v_lshl_or_b32 v194, v194, 9, v202\n"
        "buffer_load_dwordx4 v194, s[4:7], 0 offen lds\n"
        "s_add_u32 m0, m0, 0x400\n"
        "v_lshl_or_b32 v195, v195, 9, v203\n"
        "buffer_load_dwordx4 v195, s[4:7], 0 offen lds\n"
        "s_setprio 1\n"
        "ds_read_b32 v8, v2 offset:45508\n"
        "s_waitcnt lgkmcnt(0)\n"
        "v_readfirstlane_b32 s73, v8\n"
        "s_cmp_ge_u32 s73, 4\n"
        "s_cbranch_scc0 .Lw_26\n"
        ".Le_27:\n"
        "s_cmp_eq_u32 s57, 1\n"
        "s_cbranch_scc1 .Ld_28\n"
        "s_waitcnt vmcnt(22)\n"
        ".Ld_28:\n"
        "s_waitcnt vmcnt(30)\n"
        "v_cmp_ne_u32 s[74:75], s66, v24\n"
        "v_cmp_ne_u32 vcc, s66, v26\n"
        "s_or_b32 s74, s74, vcc_lo\n"
        "s_cbranch_scc1 .Lhint\n"
        ".Lhint_e:\n"
        "ds_read_b128 v[8:11], v36 offset:0\n"
        "ds_read_b128 v[12:15], v36 offset:8192\n"
        "ds_read_b128 v[16:19], v36 offset:16384\n"
        "s_cmp_eq_u32 s57, 1\n"
        "s_cbranch_scc1 .Ld_29\n"
        "s_waitcnt vmcnt(18)\n"
        ".Ld_29:\n"
        "s_waitcnt vmcnt(26)\n"
        "s_waitcnt lgkmcnt(2)\n"
        "v_mfma_f32_16x16x32_bf16 v[176:179], v[224:227], v[8:11], 0\n"
        "v_add_u32 v43, v224, v225\n"
        "v_mfma_f32_16x16x32_bf16 v[180:183], v[228:231], v[8:11], 0\n"
        "v_add3_u32 v43, v43, v226, v227\n"
        "v_mfma_f32_16x16x32_bf16 v[184:187], v[232:235], v[8:11], 0\n"
        "v_add3_u32 v43, v43, v228, v229\n"
        "v_mfma_f32_16x16x32_bf16 v[188:191], v[236:239], v[8:11], 0\n"
        "v_add3_u32 v43, v43, v230, v231\n"
        "ds_read_b128 v[8:11], v37 offset:0\n"
        "s_waitcnt lgkmcnt(2)\n"
        "v_mfma_f32_16x16x32_bf16 v[192:195], v[224:227], v[12:15], 0\n"
        "v_add3_u32 v43, v43, v232, v233\n"
        "v_mfma_f32_16x16x32_bf16 v[196:199], v[228:231], v[12:15], 0\n"
        "v_add3_u32 v43, v43, v234, v235\n"
        "v_mfma_f32_16x16x32_bf16 v[200:203], v[232:235], v[12:15], 0\n"
        "v_add3_u32 v43, v43, v236, v237\n"
        "v_mfma_f32_16x16x32_bf16 v[204:207], v[236:239], v[12:15], 0\n"
        "v_add3_u32 v43, v43, v238, v239\n"
        "ds_read_b128 v[12:15], v37 offset:8192\n"
        "s_waitcnt lgkmcnt(2)\n"
        "v_mfma_f32_16x16x32_bf16 v[208:211], v[224:227], v[16:19], 0\n"
        "v_mfma_f32_16x16x32_bf16 v[212:215], v[228:231], v[16:19], 0\n"
        "v_mfma_f32_16x16x32_bf16 v[216:219], v[232:235], v[16:19], 0\n"
        "v_mfma_f32_16x16x32_bf16 v[220:223], v[236:239], v[16:19], 0\n"
        "ds_read_b128 v[16:19], v37 offset:16384\n"
        "s_setprio 3\n"
        "buffer_load_dwordx4 v[224:227], v4, s[44:47], s9 offen offset:0 sc1\n"
        "buffer_load_dwordx4 v[228:231], v4, s[44:47], s11 offen offset:0 sc1\n"
        "buffer_load_dwordx4 v[232:235], v4, s[44:47], s13 offen offset:0 sc1\n"
        "buffer_load_dwordx4 v[236:239], v4, s[44:47], s15 offen offset:0 sc1\n"
        "s_setprio 1\n"
        "s_cmp_eq_u32 s57, 1\n"
        "s_cbranch_scc1 .Ld_30\n"
        "s_waitcnt vmcnt(18)\n"
        ".Ld_30:\n"
        "s_waitcnt vmcnt(26)\n"
        "s_waitcnt lgkmcnt(2)\n"
        "v_mfma_f32_16x16x32_bf16 v[176:179], v[240:243], v[8:11], v[176:179]\n"
        "v_add3_u32 v43, v43, v240, v241\n"
        "v_mfma_f32_16x16x32_bf16 v[180:183], v[244:247], v[8:11], v[180:183]\n"
        "v_add3_u32 v43, v43, v242, v243\n"
        "v_mfma_f32_16x16x32_bf16 v[184:187], v[248:251], v[8:11], v[184:187]\n"
        "v_add3_u32 v43, v43, v244, v245\n"
        "v_mfma_f32_16x16x32_bf16 v[188:191], v[252:255], v[8:11], v[188:191]\n"
        "v_add3_u32 v43, v43, v246, v247\n"
        "ds_read_b128 v[8:11], v38 offset:0\n"
        "s_waitcnt lgkmcnt(2)\n"
        "v_mfma_f32_16x16x32_bf16 v[192:195], v[240:243], v[12:15], v[192:195]\n"
        "v_add3_u32 v43, v43, v248, v249\n"
        "v_mfma_f32_16x16x32_bf16 v[196:199], v[244:247], v[12:15], v[196:199]\n"
        "v_add3_u32 v43, v43, v250, v251\n"
        "v_mfma_f32_16x16x32_bf16 v[200:203], v[248:251], v[12:15], v[200:203]\n"
        "v_add3_u32 v43, v43, v252, v253\n"
        "v_mfma_f32_16x16x32_bf16 v[204:207], v[252:255], v[12:15], v[204:207]\n"
        "v_add3_u32 v43, v43, v254, v255\n"
        "ds_read_b128 v[12:15], v38 offset:8192\n"
        "s_waitcnt lgkmcnt(2)\n"
        "v_mfma_f32_16x16x32_bf16 v[208:211], v[240:243], v[16:19], v[208:211]\n"
        "v_mfma_f32_16x16x32_bf16 v[212:215], v[244:247], v[16:19], v[212:215]\n"
        "v_mfma_f32_16x16x32_bf16 v[216:219], v[248:251], v[16:19], v[216:219]\n"
        "v_mfma_f32_16x16x32_bf16 v[220:223], v[252:255], v[16:19], v[220:223]\n"
        "ds_read_b128 v[16:19], v38 offset:16384\n"
        "s_setprio 3\n"
        "buffer_load_dwordx4 v[240:243], v4, s[44:47], s9 offen offset:1024 sc1\n"
        "buffer_load_dwordx4 v[244:247], v4, s[44:47], s11 offen offset:1024 sc1\n"
        "buffer_load_dwordx4 v[248:251], v4, s[44:47], s13 offen offset:1024 sc1\n"
        "buffer_load_dwordx4 v[252:255], v4, s[44:47], s15 offen offset:1024 sc1\n"
        "s_setprio 1\n"
        "s_cmp_eq_u32 s57, 1\n"
        "s_cbranch_scc1 .Ld_31\n"
        "s_waitcnt vmcnt(18)\n"
        ".Ld_31:\n"
        "s_waitcnt vmcnt(26)\n"
        "s_waitcnt lgkmcnt(2)\n"
        "v_mfma_f32_16x16x32_bf16 v[176:179], v[156:159], v[8:11], v[176:179]\n"
        "v_add3_u32 v43, v43, v156, v157\n"
        "v_mfma_f32_16x16x32_bf16 v[180:183], v[160:163], v[8:11], v[180:183]\n"
        "v_add3_u32 v43, v43, v158, v159\n"
        "v_mfma_f32_16x16x32_bf16 v[184:187], v[164:167], v[8:11], v[184:187]\n"
        "v_add3_u32 v43, v43, v160, v161\n"
        "v_mfma_f32_16x16x32_bf16 v[188:191], v[168:171], v[8:11], v[188:191]\n"
        "v_add3_u32 v43, v43, v162, v163\n"
        "ds_read_b128 v[8:11], v39 offset:0\n"
        "s_waitcnt lgkmcnt(2)\n"
        "v_mfma_f32_16x16x32_bf16 v[192:195], v[156:159], v[12:15], v[192:195]\n"
        "v_add3_u32 v43, v43, v164, v165\n"
        "v_mfma_f32_16x16x32_bf16 v[196:199], v[160:163], v[12:15], v[196:199]\n"
        "v_add3_u32 v43, v43, v166, v167\n"
        "v_mfma_f32_16x16x32_bf16 v[200:203], v[164:167], v[12:15], v[200:203]\n"
        "v_add3_u32 v43, v43, v168, v169\n"
        "v_mfma_f32_16x16x32_bf16 v[204:207], v[168:171], v[12:15], v[204:207]\n"
        "v_add3_u32 v43, v43, v170, v171\n"
        "ds_read_b128 v[12:15], v39 offset:8192\n"
        "s_waitcnt lgkmcnt(2)\n"
        "v_mfma_f32_16x16x32_bf16 v[208:211], v[156:159], v[16:19], v[208:211]\n"
        "v_mfma_f32_16x16x32_bf16 v[212:215], v[160:163], v[16:19], v[212:215]\n"
        "v_mfma_f32_16x16x32_bf16 v[216:219], v[164:167], v[16:19], v[216:219]\n"
        "v_mfma_f32_16x16x32_bf16 v[220:223], v[168:171], v[16:19], v[220:223]\n"
        "ds_read_b128 v[16:19], v39 offset:16384\n"
        "s_setprio 3\n"
        "buffer_load_dwordx4 v[156:159], v4, s[44:47], s9 offen offset:2048 sc1\n"
        "buffer_load_dwordx4 v[160:163], v4, s[44:47], s11 offen offset:2048 sc1\n"
        "buffer_load_dwordx4 v[164:167], v4, s[44:47], s13 offen offset:2048 sc1\n"
        "buffer_load_dwordx4 v[168:171], v4, s[44:47], s15 offen offset:2048 sc1\n"
        "s_setprio 1\n"
        "s_cmp_eq_u32 s57, 1\n"
        "s_cbranch_scc1 .Ld_32\n"
        "s_waitcnt vmcnt(18)\n"
        ".Ld_32:\n"
        "s_waitcnt vmcnt(26)\n"
        "s_waitcnt lgkmcnt(2)\n"
        "v_mfma_f32_16x16x32_bf16 v[176:179], v[140:143], v[8:11], v[176:179]\n"
        "v_add3_u32 v43, v43, v140, v141\n"
        "v_mfma_f32_16x16x32_bf16 v[180:183], v[144:147], v[8:11], v[180:183]\n"
        "v_add3_u32 v43, v43, v142, v143\n"
        "v_mfma_f32_16x16x32_bf16 v[184:187], v[148:151], v[8:11], v[184:187]\n"
        "v_add3_u32 v43, v43, v144, v145\n"
        "v_mfma_f32_16x16x32_bf16 v[188:191], v[152:155], v[8:11], v[188:191]\n"
        "v_add3_u32 v43, v43, v146, v147\n"
        "ds_read_b128 v[8:11], v36 offset:256\n"
        "s_waitcnt lgkmcnt(2)\n"
        "v_mfma_f32_16x16x32_bf16 v[192:195], v[140:143], v[12:15], v[192:195]\n"
        "v_add3_u32 v43, v43, v148, v149\n"
        "v_mfma_f32_16x16x32_bf16 v[196:199], v[144:147], v[12:15], v[196:199]\n"
        "v_add3_u32 v43, v43, v150, v151\n"
        "v_mfma_f32_16x16x32_bf16 v[200:203], v[148:151], v[12:15], v[200:203]\n"
        "v_add3_u32 v43, v43, v152, v153\n"
        "v_mfma_f32_16x16x32_bf16 v[204:207], v[152:155], v[12:15], v[204:207]\n"
        "v_add3_u32 v43, v43, v154, v155\n"
        "ds_read_b128 v[12:15], v36 offset:8448\n"
        "s_waitcnt lgkmcnt(2)\n"
        "v_mfma_f32_16x16x32_bf16 v[208:211], v[140:143], v[16:19], v[208:211]\n"
        "v_mfma_f32_16x16x32_bf16 v[212:215], v[144:147], v[16:19], v[212:215]\n"
        "v_mfma_f32_16x16x32_bf16 v[216:219], v[148:151], v[16:19], v[216:219]\n"
        "v_mfma_f32_16x16x32_bf16 v[220:223], v[152:155], v[16:19], v[220:223]\n"
        "ds_read_b128 v[16:19], v36 offset:16640\n"
        "s_setprio 3\n"
        "buffer_load_dwordx4 v[140:143], v4, s[44:47], s9 offen offset:3072 sc1\n"
        "buffer_load_dwordx4 v[144:147], v4, s[44:47], s11 offen offset:3072 sc1\n"
        "buffer_load_dwordx4 v[148:151], v4, s[44:47], s13 offen offset:3072 sc1\n"
        "buffer_load_dwordx4 v[152:155], v4, s[44:47], s15 offen offset:3072 sc1\n"
        "s_setprio 1\n"
        "s_waitcnt vmcnt(12)\n"
        "s_waitcnt lgkmcnt(2)\n"
        "v_mfma_f32_16x16x32_bf16 v[176:179], v[224:227], v[8:11], v[176:179]\n"
        "v_add3_u32 v43, v43, v224, v225\n"
        "v_mfma_f32_16x16x32_bf16 v[180:183], v[228:231], v[8:11], v[180:183]\n"
        "v_add3_u32 v43, v43, v226, v227\n"
        "v_mfma_f32_16x16x32_bf16 v[184:187], v[232:235], v[8:11], v[184:187]\n"
        "v_add3_u32 v43, v43, v228, v229\n"
        "v_mfma_f32_16x16x32_bf16 v[188:191], v[236:239], v[8:11], v[188:191]\n"
        "v_add3_u32 v43, v43, v230, v231\n"
        "ds_read_b128 v[8:11], v37 offset:256\n"
        "s_waitcnt lgkmcnt(2)\n"
        "v_mfma_f32_16x16x32_bf16 v[192:195], v[224:227], v[12:15], v[192:195]\n"
        "v_add3_u32 v43, v43, v232, v233\n"
        "v_mfma_f32_16x16x32_bf16 v[196:199], v[228:231], v[12:15], v[196:199]\n"
        "v_add3_u32 v43, v43, v234, v235\n"
        "v_mfma_f32_16x16x32_bf16 v[200:203], v[232:235], v[12:15], v[200:203]\n"
        "v_add3_u32 v43, v43, v236, v237\n"
        "v_mfma_f32_16x16x32_bf16 v[204:207], v[236:239], v[12:15], v[204:207]\n"
        "v_add3_u32 v43, v43, v238, v239\n"
        "ds_read_b128 v[12:15], v37 offset:8448\n"
        "s_waitcnt lgkmcnt(2)\n"
        "v_mfma_f32_16x16x32_bf16 v[208:211], v[224:227], v[16:19], v[208:211]\n"
        "v_mfma_f32_16x16x32_bf16 v[212:215], v[228:231], v[16:19], v[212:215]\n"
        "v_mfma_f32_16x16x32_bf16 v[216:219], v[232:235], v[16:19], v[216:219]\n"
        "v_mfma_f32_16x16x32_bf16 v[220:223], v[236:239], v[16:19], v[220:223]\n"
        "ds_read_b128 v[16:19], v37 offset:16640\n"
        "s_setprio 3\n"
        "s_mov_b32 exec_hi, 15\n"
        "buffer_load_dwordx4 v[172:175], v42, s[44:47], 0 offen sc1\n"
        "s_mov_b64 exec, -1\n"
        "s_cmp_lg_u32 s3, 4\n"
        "s_cbranch_scc1 .Lsw1\n"
        "buffer_load_dwordx4 v[24:27], v5, s[44:47], s43 offen sc1\n"
        "buffer_load_dwordx4 v[28:31], v5, s[44:47], s43 offen offset:1024 sc1\n"
        ".Lsw1:\n"
        "s_setprio 1\n"
        "s_waitcnt vmcnt(9)\n"
        "s_waitcnt lgkmcnt(2)\n"
        "v_mfma_f32_16x16x32_bf16 v[176:179], v[240:243], v[8:11], v[176:179]\n"
        "v_add3_u32 v43, v43, v240, v241\n"
        "v_mfma_f32_16x16x32_bf16 v[180:183], v[244:247], v[8:11], v[180:183]\n"
        "v_add3_u32 v43, v43, v242, v243\n"
        "v_mfma_f32_16x16x32_bf16 v[184:187], v[248:251], v[8:11], v[184:187]\n"
        "v_add3_u32 v43, v43, v244, v245\n"
        "v_mfma_f32_16x16x32_bf16 v[188:191], v[252:255], v[8:11], v[188:191]\n"
        "v_add3_u32 v43, v43, v246, v247\n"
        "ds_read_b128 v[8:11], v38 offset:256\n"
        "s_waitcnt lgkmcnt(2)\n"
        "v_mfma_f32_16x16x32_bf16 v[192:195], v[240:243], v[12:15], v[192:195]\n"
        "v_add3_u32 v43, v43, v248, v249\n"
        "v_mfma_f32_16x16x32_bf16 v[196:199], v[244:247], v[12:15], v[196:199]\n"
        "v_add3_u32 v43, v43, v250, v251\n"
        "v_mfma_f32_16x16x32_bf16 v[200:203], v[248:251], v[12:15], v[200:203]\n"
        "v_add3_u32 v43, v43, v252, v253\n"
        "v_mfma_f32_16x16x32_bf16 v[204:207], v[252:255], v[12:15], v[204:207]\n"
        "v_add3_u32 v43, v43, v254, v255\n"
        "ds_read_b128 v[12:15], v38 offset:8448\n"
        "s_waitcnt lgkmcnt(2)\n"
        "v_mfma_f32_16x16x32_bf16 v[208:211], v[240:243], v[16:19], v[208:211]\n"
        "v_mfma_f32_16x16x32_bf16 v[212:215], v[244:247], v[16:19], v[212:215]\n"
        "v_mfma_f32_16x16x32_bf16 v[216:219], v[248:251], v[16:19], v[216:219]\n"
        "v_mfma_f32_16x16x32_bf16 v[220:223], v[252:255], v[16:19], v[220:223]\n"
        "ds_read_b128 v[16:19], v38 offset:16640\n"
        "s_waitcnt vmcnt(5)\n"
        "s_waitcnt lgkmcnt(2)\n"
        "v_mfma_f32_16x16x32_bf16 v[176:179], v[156:159], v[8:11], v[176:179]\n"
        "v_add3_u32 v43, v43, v156, v157\n"
        "v_mfma_f32_16x16x32_bf16 v[180:183], v[160:163], v[8:11], v[180:183]\n"
        "v_add3_u32 v43, v43, v158, v159\n"
        "v_mfma_f32_16x16x32_bf16 v[184:187], v[164:167], v[8:11], v[184:187]\n"
        "v_add3_u32 v43, v43, v160, v161\n"
        "v_mfma_f32_16x16x32_bf16 v[188:191], v[168:171], v[8:11], v[188:191]\n"
        "v_add3_u32 v43, v43, v162, v163\n"
        "ds_read_b128 v[8:11], v39 offset:256\n"
        "s_waitcnt lgkmcnt(2)\n"
        "v_mfma_f32_16x16x32_bf16 v[192:195], v[156:159], v[12:15], v[192:195]\n"
        "v_add3_u32 v43, v43, v164, v165\n"
        "v_mfma_f32_16x16x32_bf16 v[196:199], v[160:163], v[12:15], v[196:199]\n"
        "v_add3_u32 v43, v43, v166, v167\n"
        "v_mfma_f32_16x16x32_bf16 v[200:203], v[164:167], v[12:15], v[200:203]\n"
        "v_add3_u32 v43, v43, v168, v169\n"
        "v_mfma_f32_16x16x32_bf16 v[204:207], v[168:171], v[12:15], v[204:207]\n"
        "v_add3_u32 v43, v43, v170, v171\n"
        "ds_read_b128 v[12:15], v39 offset:8448\n"
        "s_waitcnt lgkmcnt(2)\n"
        "v_mfma_f32_16x16x32_bf16 v[208:211], v[156:159], v[16:19], v[208:211]\n"
        "v_mfma_f32_16x16x32_bf16 v[212:215], v[160:163], v[16:19], v[212:215]\n"
        "v_mfma_f32_16x16x32_bf16 v[216:219], v[164:167], v[16:19], v[216:219]\n"
        "v_mfma_f32_16x16x32_bf16 v[220:223], v[168:171], v[16:19], v[220:223]\n"
        "ds_read_b128 v[16:19], v39 offset:16640\n"
        "s_waitcnt vmcnt(1)\n"
        "s_waitcnt vmcnt(0)\n"
        "s_waitcnt lgkmcnt(2)\n"
        "v_mfma_f32_16x16x32_bf16 v[176:179], v[140:143], v[8:11], v[176:179]\n"
        "v_add3_u32 v43, v43, v140, v141\n"
        "v_add3_u32 v43, v43, v142, v143\n"
        "v_mfma_f32_16x16x32_bf16 v[180:183], v[144:147], v[8:11], v[180:183]\n"
        "v_add3_u32 v43, v43, v144, v145\n"
        "v_add3_u32 v43, v43, v146, v147\n"
        "v_mfma_f32_16x16x32_bf16 v[184:187], v[148:151], v[8:11], v[184:187]\n"
        "v_add3_u32 v43, v43, v148, v149\n"
        "v_add3_u32 v43, v43, v150, v151\n"
        "v_mfma_f32_16x16x32_bf16 v[188:191], v[152:155], v[8:11], v[188:191]\n"
        "v_add3_u32 v43, v43, v152, v153\n"
        "v_add3_u32 v43, v43, v154, v155\n"
        "s_waitcnt lgkmcnt(1)\n"
        "v_mfma_f32_16x16x32_bf16 v[192:195], v[140:143], v[12:15], v[192:195]\n"
        "s_mov_b32 exec_hi, 0\n"
        "v_sub_u32 v43, v43, v173\n"
        "v_cmp_ne_u32 s[74:75], s66, v172\n"
        "v_sub_u32 v43, v43, v175\n"
        "v_cmp_ne_u32 s[76:77], s66, v174\n"
        "s_mov_b32 exec_hi, -1\n"
        "v_add_u32_dpp v43, v43, v43 row_shr:1 row_mask:0xf bank_mask:0xf bound_ctrl:0\n"
        "v_mfma_f32_16x16x32_bf16 v[196:199], v[144:147], v[12:15], v[196:199]\n"
        "s_nop 0\n"
        "v_add_u32_dpp v43, v43, v43 row_shr:2 row_mask:0xf bank_mask:0xf bound_ctrl:0\n"
        "s_nop 1\n"
        "v_add_u32_dpp v43, v43, v43 row_shr:4 row_mask:0xf bank_mask:0xf bound_ctrl:0\n"
        "v_mfma_f32_16x16x32_bf16 v[200:203], v[148:151], v[12:15], v[200:203]\n"
        "s_nop 0\n"
        "v_add_u32_dpp v43, v43, v43 row_shr:8 row_mask:0xf bank_mask:0xf bound_ctrl:0\n"
        "s_nop 1\n"
        "v_add_u32_dpp v43, v43, v43 row_bcast:15 row_mask:0xa bank_mask:0xf\n"
        "v_mfma_f32_16x16x32_bf16 v[204:207], v[152:155], v[12:15], v[204:207]\n"
        "s_nop 0\n"
        "v_add_u32_dpp v43, v43, v43 row_bcast:31 row_mask:0xc bank_mask:0xf\n"
        "s_lshl_b32 s75, s67, 6\n"
        "s_or_b32 s74, s74, s76\n"
        "s_waitcnt lgkmcnt(0)\n"
        "v_mfma_f32_16x16x32_bf16 v[208:211], v[140:143], v[16:19], v[208:211]\n"
        "v_readlane_b32 s73, v43, 63\n"
        "s_add_u32 s73, s73, s75\n"
        "s_or_b32 s73, s73, s74\n"
        "v_mfma_f32_16x16x32_bf16 v[212:215], v[144:147], v[16:19], v[212:215]\n"
        "v_mfma_f32_16x16x32_bf16 v[216:219], v[148:151], v[16:19], v[216:219]\n"
        "v_mfma_f32_16x16x32_bf16 v[220:223], v[152:155], v[16:19], v[220:223]\n"
        "s_cbranch_scc1 .Lqm_again\n"
        ".Lqm_done:\n"
        "ds_add_u32 v2, v3 offset:45496\n"
        "s_add_u32 s8, s16, 32768\n"
        "s_add_u32 s9, s16, 36864\n"
        "s_add_u32 s10, s16, 40960\n"
        "s_add_u32 s11, s16, 45056\n"
        "s_add_u32 s12, s16, 49152\n"
        "s_add_u32 s13, s16, 53248\n"
        "s_add_u32 s14, s16, 57344\n"
        "s_add_u32 s15, s16, 61440\n"
        "ds_read_b32 v8, v2 offset:45496\n"
        "s_waitcnt lgkmcnt(0)\n"
        "v_readfirstlane_b32 s73, v8\n"
        "s_cmp_ge_u32 s73, 4\n"
        "s_cbranch_scc0 .Lw_33\n"
        ".Le_34:\n"
        "v_cvt_pk_bf16_f32 v224, v176, v177\n"
        "v_cvt_pk_bf16_f32 v225, v178, v179\n"
        "v_cvt_pk_bf16_f32 v226, v180, v181\n"
        "v_cvt_pk_bf16_f32 v227, v182, v183\n"
        "ds_write_b128 v41, v[224:227] offset:24064\n"
        "v_cvt_pk_bf16_f32 v228, v184, v185\n"
        "v_cvt_pk_bf16_f32 v229, v186, v187\n"
        "v_cvt_pk_bf16_f32 v230, v188, v189\n"
        "v_cvt_pk_bf16_f32 v231, v190, v191\n"
        "ds_write_b128 v41, v[228:231] offset:24128\n"
        "v_cvt_pk_bf16_f32 v232, v192, v193\n"
        "v_cvt_pk_bf16_f32 v233, v194, v195\n"
        "v_cvt_pk_bf16_f32 v234, v196, v197\n"
        "v_cvt_pk_bf16_f32 v235, v198, v199\n"
        "ds_write_b128 v41, v[232:235] offset:32512\n"
        "v_cvt_pk_bf16_f32 v236, v200, v201\n"
        "v_cvt_pk_bf16_f32 v237, v202, v203\n"
        "v_cvt_pk_bf16_f32 v238, v204, v205\n"
        "v_cvt_pk_bf16_f32 v239, v206, v207\n"
        "ds_write_b128 v41, v[236:239] offset:32576\n"
        "v_cvt_pk_bf16_f32 v240, v208, v209\n"
        "v_cvt_pk_bf16_f32 v241, v210, v211\n"
        "v_cvt_pk_bf16_f32 v242, v212, v213\n"
        "v_cvt_pk_bf16_f32 v243, v214, v215\n"
        "s_mov_b32 exec_lo, 0x00ff00ff\n"
        "s_mov_b32 exec_hi, 0x00ff00ff\n"
        "ds_write_b128 v41, v[240:243] offset:40960\n"
        "v_cvt_pk_bf16_f32 v244, v216, v217\n"
        "v_cvt_pk_bf16_f32 v245, v218, v219\n"
        "v_cvt_pk_bf16_f32 v246, v220, v221\n"
        "v_cvt_pk_bf16_f32 v247, v222, v223\n"
        "ds_write_b128 v41, v[244:247] offset:41024\n"
        "s_mov_b64 exec, -1\n"
        "ds_add_u32 v2, v3 offset:45484\n"
        "ds_read_b128 v[180:183], v6 offset:0\n"
        "ds_read_b128 v[184:187], v6 offset:8192\n"
        "ds_read_b128 v[188:191], v6 offset:16384\n"
        "v_xor_b32 v6, 64, v6\n"
        "ds_read_b128 v[196:199], v6 offset:0\n"
        "ds_read_b128 v[200:203], v6 offset:8192\n"
        "ds_read_b128 v[204:207], v6 offset:16384\n"
        "v_xor_b32 v6, 192, v6\n"
        "ds_read_b128 v[212:215], v6 offset:0\n"
        "ds_read_b128 v[216:219], v6 offset:8192\n"
        "ds_read_b128 v[220:223], v6 offset:16384\n"
        "v_xor_b32 v6, 64, v6\n"
        "ds_read_b128 v[228:231], v6 offset:0\n"
        "ds_read_b128 v[232:235], v6 offset:8192\n"
        "ds_read_b128 v[236:239], v6 offset:16384\n"
        "v_xor_b32 v6, 192, v6\n"
        "ds_read_b128 v[244:247], v6 offset:256\n"
        "ds_read_b128 v[248:251], v6 offset:8448\n"
        "ds_read_b128 v[252:255], v6 offset:16640\n"
        "v_cmp_ne_u32 s[74:75], s66, v172\n"
        "v_cmp_ne_u32 s[76:77], s66, v174\n"
        "v_cmp_ne_u32 s[78:79], s67, v173\n"
        "v_cmp_ne_u32 vcc, s67, v175\n"
        "s_or_b64 s[74:75], s[74:75], s[76:77]\n"
        "s_or_b64 s[78:79], s[78:79], vcc\n"
        "s_or_b64 s[74:75], s[74:75], s[78:79]\n"
        "s_and_b32 s74, s75, 11\n"
        "s_cbranch_scc1 .Lwf\n"
        ".Lwf_e:\n"
        "s_setprio 3\n"
        "buffer_load_dwordx4 v[140:143], v5, s[44:47], s14 offen offset:0 sc1\n"
        "buffer_load_dwordx4 v[144:147], v5, s[44:47], s14 offen offset:1024 sc1\n"
        "buffer_load_dwordx4 v[148:151], v5, s[44:47], s14 offen offset:2048 sc1\n"
        "buffer_load_dwordx4 v[152:155], v5, s[44:47], s14 offen offset:3072 sc1\n"
        "buffer_load_dwordx4 v[156:159], v5, s[44:47], s15 offen offset:0 sc1\n"
        "buffer_load_dwordx4 v[160:163], v5, s[44:47], s15 offen offset:1024 sc1\n"
        "buffer_load_dwordx4 v[164:167], v5, s[44:47], s15 offen offset:2048 sc1\n"
        "buffer_load_dwordx4 v[168:171], v5, s[44:47], s15 offen offset:3072 sc1\n"
        "s_setprio 0\n"
        "s_add_u32 s8, s16, 0\n"
        "s_add_u32 s9, s16, 4096\n"
        "s_add_u32 s10, s16, 8192\n"
        "s_add_u32 s11, s16, 12288\n"
        "s_add_u32 s12, s16, 16384\n"
        "s_add_u32 s13, s16, 20480\n"
        "s_add_u32 s14, s16, 24576\n"
        "s_add_u32 s15, s16, 28672\n"
        "s_cmp_lg_u32 s3, 4\n"
        "s_cbranch_scc1 .Lmove_done\n"
        "s_waitcnt vmcnt(8)\n"
        "v_and_b32 v32, v24, v26\n"
        "v_and_b32 v33, v28, v30\n"
        "v_and_b32 v32, v32, v33\n"
        "v_or_b32 v24, 3, v24\n"
        "v_xor_b32 v24, s66, v24\n"
        "v_xor_b32 v25, s67, v25\n"
        "v_or_b32 v26, 3, v26\n"
        "v_xor_b32 v26, s66, v26\n"
        "v_xor_b32 v27, s67, v27\n"
        "v_or_b32 v28, 3, v28\n"
        "v_xor_b32 v28, s66, v28\n"
        "v_xor_b32 v29, s67, v29\n"
        "v_or_b32 v30, 3, v30\n"
        "v_xor_b32 v30, s66, v30\n"
        "v_xor_b32 v31, s67, v31\n"
        "v_or3_b32 v24, v24, v25, v26\n"
        "v_or3_b32 v27, v27, v28, v29\n"
        "v_or3_b32 v24, v24, v30, v31\n"
        "v_and_b32 v33, 1, v32\n"
        "v_or_b32 v24, v24, v27\n"
        "v_and_b32 v32, 2, v32\n"
        "v_cmp_eq_u32 s[74:75], 0, v33\n"
        "v_cmp_eq_u32 s[76:77], 0, v32\n"
        "v_cmp_ne_u32 vcc, 0, v24\n"
        "s_cmp_eq_u64 s[74:75], 0\n"
        "s_cselect_b32 s62, 1, 0\n"
        "s_cmp_eq_u64 s[76:77], 0\n"
        "s_cselect_b32 s73, 2, 0\n"
        "s_or_b32 s62, s62, s73\n"
        "s_cmp_eq_u64 vcc, 0\n"
        "s_cselect_b32 s73, 4, 0\n"
        "s_or_b32 s62, s62, s73\n"
        "s_bitcmp0_b32 s62, 2\n"
        "s_cbranch_scc1 .Lagain\n"
        ".Lall:\n"
        "s_add_u32 s75, s2, 1\n"
        "s_cmp_eq_u32 s75, s56\n"
        "s_cbranch_scc1 .Lmove\n"
        "s_bitcmp1_b32 s62, 0\n"
        "s_cbranch_scc1 .Ltell\n"
        ".Lmove:\n"
        "s_add_u32 s73, s64, 1\n"
        "s_addc_u32 s74, s65, 0\n"
        "v_mov_b32 v24, s73\n"
        "v_mov_b32 v25, s74\n"
        "s_mov_b64 exec, 1\n"
        "buffer_store_dwordx2 v[24:25], v2, s[44:47], 0 offen sc1\n"
        "s_mov_b64 exec, -1\n"
        ".Ltell:\n"
        "s_cmp_ge_u32 s2, s56\n"
        "s_cbranch_scc1 .Lend_l\n"
        "s_and_b32 s73, s62, 6\n"
        "s_cmp_eq_u32 s73, 6\n"
        "s_cselect_b32 s61, s61, 0x1000\n"
        ".Lmove_done:\n"
        "v_mov_b32 v36, s61\n"
        "ds_add_u32 v2, v36 offset:45452\n"
        "v_xor_b32 v37, 32, v20\n"
        "v_xor_b32 v38, 64, v20\n"
        "v_xor_b32 v39, 96, v20\n"
        "v_and_b32 v8, 15, v1\n"
        "v_mul_u32_u24 v8, 272, v8\n"
        "v_lshrrev_b32 v9, 4, v1\n"
        "v_lshl_add_u32 v33, v9, 4, v8\n"
        "v_lshlrev_b32 v8, 1, v8\n"
        "v_lshl_add_u32 v8, v9, 3, v8\n"
        "s_lshl_b32 s73, s72, 6\n"
        "s_add_u32 s73, s73, 40960\n"
        "v_add_u32 v43, s73, v8\n"
        "v_lshrrev_b32 v8, 3, v1\n"
        "v_and_b32 v9, 1, v1\n"
        "v_lshlrev_b32 v9, 6, v9\n"
        "v_lshl_or_b32 v8, v8, 9, v9\n"
        "v_and_b32 v9, 6, v1\n"
        "v_lshl_or_b32 v42, v9, 3, v8\n"
        "ds_read_b32 v8, v2 offset:45484\n"
        "s_waitcnt lgkmcnt(0)\n"
        "v_readfirstlane_b32 s73, v8\n"
        "s_cmp_ge_u32 s73, 4\n"
        "s_cbranch_scc0 .Lw_35\n"
        ".Le_36:\n"
        "s_setprio 0\n"
        "ds_read_b128 v[176:179], v7 offset:24064\n"
        "ds_read_b128 v[192:195], v7 offset:24128\n"
        "ds_read_b128 v[208:211], v7 offset:24192\n"
        "ds_read_b128 v[224:227], v7 offset:24256\n"
        "ds_read_b128 v[240:243], v7 offset:24320\n"
        "s_waitcnt lgkmcnt(4)\n"
        "v_mfma_f32_16x16x32_bf16 v[8:11], v[180:183], v[176:179], 0\n"
        "v_mfma_f32_16x16x32_bf16 v[12:15], v[184:187], v[176:179], 0\n"
        "v_mfma_f32_16x16x32_bf16 v[16:19], v[188:191], v[176:179], 0\n"
        "v_xor_b32 v6, 64, v6\n"
        "ds_read_b128 v[180:183], v6 offset:256\n"
        "ds_read_b128 v[184:187], v6 offset:8448\n"
        "ds_read_b128 v[188:191], v6 offset:16640\n"
        "ds_read_b128 v[176:179], v7 offset:24384\n"
        "s_waitcnt lgkmcnt(7)\n"
        "v_mfma_f32_16x16x32_bf16 v[8:11], v[196:199], v[192:195], v[8:11]\n"
        "v_mfma_f32_16x16x32_bf16 v[12:15], v[200:203], v[192:195], v[12:15]\n"
        "v_mfma_f32_16x16x32_bf16 v[16:19], v[204:207], v[192:195], v[16:19]\n"
        "v_xor_b32 v6, 192, v6\n"
        "ds_read_b128 v[196:199], v6 offset:256\n"
        "ds_read_b128 v[200:203], v6 offset:8448\n"
        "ds_read_b128 v[204:207], v6 offset:16640\n"
        "ds_read_b128 v[192:195], v7 offset:24448\n"
        "s_waitcnt lgkmcnt(10)\n"
        "v_mfma_f32_16x16x32_bf16 v[8:11], v[212:215], v[208:211], v[8:11]\n"
        "v_mfma_f32_16x16x32_bf16 v[12:15], v[216:219], v[208:211], v[12:15]\n"
        "v_mfma_f32_16x16x32_bf16 v[16:19], v[220:223], v[208:211], v[16:19]\n"
        "v_xor_b32 v6, 64, v6\n"
        "ds_read_b128 v[212:215], v6 offset:256\n"
        "ds_read_b128 v[216:219], v6 offset:8448\n"
        "ds_read_b128 v[220:223], v6 offset:16640\n"
        "ds_read_b128 v[208:211], v7 offset:24512\n"
        "s_waitcnt lgkmcnt(13)\n"
        "v_mfma_f32_16x16x32_bf16 v[8:11], v[228:231], v[224:227], v[8:11]\n"
        "v_mfma_f32_16x16x32_bf16 v[12:15], v[232:235], v[224:227], v[12:15]\n"
        "v_mfma_f32_16x16x32_bf16 v[16:19], v[236:239], v[224:227], v[16:19]\n"
        "ds_read_b64_tr_b16 v[224:225], v20 offset:0\n"
        "ds_read_b64_tr_b16 v[226:227], v37 offset:2048\n"
        "ds_read_b64_tr_b16 v[228:229], v20 offset:8\n"
        "ds_read_b64_tr_b16 v[230:231], v37 offset:2056\n"
        "ds_read_b64_tr_b16 v[232:233], v38 offset:0\n"
        "ds_read_b64_tr_b16 v[234:235], v39 offset:2048\n"
        "ds_read_b64_tr_b16 v[236:237], v38 offset:8\n"
        "ds_read_b64_tr_b16 v[238:239], v39 offset:2056\n"
        "s_waitcnt lgkmcnt(15)\n"
        "v_mfma_f32_16x16x32_bf16 v[8:11], v[244:247], v[240:243], v[8:11]\n"
        "v_mfma_f32_16x16x32_bf16 v[12:15], v[248:251], v[240:243], v[12:15]\n"
        "v_mfma_f32_16x16x32_bf16 v[16:19], v[252:255], v[240:243], v[16:19]\n"
        "ds_read_b64_tr_b16 v[240:241], v20 offset:16384\n"
        "ds_read_b64_tr_b16 v[242:243], v37 offset:18432\n"
        "ds_read_b64_tr_b16 v[244:245], v20 offset:16392\n"
        "ds_read_b64_tr_b16 v[246:247], v37 offset:18440\n"
        "ds_read_b64_tr_b16 v[248:249], v38 offset:16384\n"
        "ds_read_b64_tr_b16 v[250:251], v39 offset:18432\n"
        "ds_read_b64_tr_b16 v[252:253], v38 offset:16392\n"
        "ds_read_b64_tr_b16 v[254:255], v39 offset:18440\n"
        "v_mfma_f32_16x16x32_bf16 v[8:11], v[180:183], v[176:179], v[8:11]\n"
        "v_mfma_f32_16x16x32_bf16 v[12:15], v[184:187], v[176:179], v[12:15]\n"
        "v_mfma_f32_16x16x32_bf16 v[16:19], v[188:191], v[176:179], v[16:19]\n"
        "s_waitcnt lgkmcnt(15)\n"
        "v_mfma_f32_16x16x32_bf16 v[8:11], v[196:199], v[192:195], v[8:11]\n"
        "v_mfma_f32_16x16x32_bf16 v[12:15], v[200:203], v[192:195], v[12:15]\n"
        "v_mfma_f32_16x16x32_bf16 v[16:19], v[204:207], v[192:195], v[16:19]\n"
        "v_mfma_f32_16x16x32_bf16 v[8:11], v[212:215], v[208:211], v[8:11]\n"
        "v_mfma_f32_16x16x32_bf16 v[12:15], v[216:219], v[208:211], v[12:15]\n"
        "v_mfma_f32_16x16x32_bf16 v[16:19], v[220:223], v[208:211], v[16:19]\n"
        "s_mov_b32 exec_lo, 0xf000f0\n"
        "s_mov_b32 exec_hi, 0xf000f0\n"
        "s_nop 4\n"
        "v_mov_b64 v[8:9], v[12:13]\n"
        "v_mov_b64 v[10:11], v[14:15]\n"
        "s_mov_b32 exec_lo, 0xf000f00\n"
        "s_mov_b32 exec_hi, 0xf000f00\n"
        "v_mov_b64 v[8:9], v[16:17]\n"
        "v_mov_b64 v[10:11], v[18:19]\n"
        "s_mov_b32 exec_lo, 0x02220111\n"
        "s_mov_b32 exec_hi, 0x00880044\n"
        "s_and_b32 s73, s64, 7\n"
        "s_lshl_b32 s73, s73, 23\n"
        "s_add_u32 s73, s73, 0x3fb8aa3b\n"
        "v_mul_f32 v8, s73, v8\n"
        "v_mul_f32 v9, s73, v9\n"
        "v_mul_f32 v10, s73, v10\n"
        "v_mul_f32 v11, s73, v11\n"
        "v_exp_f32 v8, v8\n"
        "v_exp_f32 v9, v9\n"
        "v_exp_f32 v10, v10\n"
        "v_exp_f32 v11, v11\n"
        "v_add_f32 v12, v8, v9\n"
        "v_add_f32 v13, v10, v11\n"
        "v_add_f32 v12, v12, v13\n"
        "v_add_f32 v12, 0x3089705f, v12\n"
        "v_rcp_f32 v12, v12\n"
        "s_nop 0\n"
        "v_mul_f32 v8, v8, v12\n"
        "v_mul_f32 v9, v9, v12\n"
        "v_mul_f32 v10, v10, v12\n"
        "v_mul_f32 v11, v11, v12\n"
        "v_cvt_pk_bf16_f32 v8, v8, v9\n"
        "v_cvt_pk_bf16_f32 v9, v10, v11\n"
        "ds_write_b64 v40, v[8:9] offset:21760\n"
        "s_mov_b64 exec, -1\n"
        "ds_add_u32 v2, v3 offset:45532\n"
        "ds_read_b64_tr_b16 v[176:177], v20 offset:32768\n"
        "ds_read_b64_tr_b16 v[178:179], v37 offset:34816\n"
        "ds_read_b64_tr_b16 v[180:181], v20 offset:32776\n"
        "ds_read_b64_tr_b16 v[182:183], v37 offset:34824\n"
        "ds_read_b64_tr_b16 v[184:185], v38 offset:32768\n"
        "ds_read_b64_tr_b16 v[186:187], v39 offset:34816\n"
        "ds_read_b64_tr_b16 v[188:189], v38 offset:32776\n"
        "ds_read_b64_tr_b16 v[190:191], v39 offset:34824\n"
        "ds_read_b64_tr_b16 v[192:193], v20 offset:49152\n"
        "ds_read_b64_tr_b16 v[194:195], v37 offset:51200\n"
        "ds_read_b64_tr_b16 v[196:197], v20 offset:49160\n"
        "ds_read_b64_tr_b16 v[198:199], v37 offset:51208\n"
        "ds_read_b64_tr_b16 v[200:201], v38 offset:49152\n"
        "ds_read_b64_tr_b16 v[202:203], v39 offset:51200\n"
        "ds_read_b64_tr_b16 v[204:205], v38 offset:49160\n"
        "ds_read_b64_tr_b16 v[206:207], v39 offset:51208\n"
        "v_add_u32 v20, 0x10000, v20\n"
        "v_add_u32 v37, 0x10000, v37\n"
        "v_add_u32 v38, 0x10000, v38\n"
        "v_add_u32 v39, 0x10000, v39\n"
        "ds_read_b64_tr_b16 v[208:209], v20 offset:0\n"
        "ds_read_b64_tr_b16 v[210:211], v37 offset:2048\n"
        "ds_read_b64_tr_b16 v[212:213], v20 offset:8\n"
        "ds_read_b64_tr_b16 v[214:215], v37 offset:2056\n"
        "ds_read_b64_tr_b16 v[216:217], v38 offset:0\n"
        "ds_read_b64_tr_b16 v[218:219], v39 offset:2048\n"
        "ds_read_b64_tr_b16 v[220:221], v38 offset:8\n"
        "ds_read_b64_tr_b16 v[222:223], v39 offset:2056\n"
        "ds_read_b32 v32, v2 offset:45532\n"
        "s_waitcnt lgkmcnt(0)\n"
        "v_readfirstlane_b32 s73, v32\n"
        "s_cmp_ge_u32 s73, 4\n"
        "s_cbranch_scc0 .Lw_37\n"
        ".Le_38:\n"
        "ds_read_b128 v[8:11], v21 offset:21760\n"
        "ds_read_b128 v[12:15], v22 offset:21760\n"
        "ds_read_b128 v[16:19], v21 offset:22016\n"
        "ds_read_b128 v[24:27], v22 offset:22016\n"
        "ds_read_b128 v[28:31], v21 offset:22272\n"
        "s_waitcnt lgkmcnt(4)\n"
        "v_mfma_f32_16x16x32_bf16 v[224:227], v[224:227], v[8:11], 0\n"
        "v_mfma_f32_16x16x32_bf16 v[228:231], v[228:231], v[8:11], 0\n"
        "v_mfma_f32_16x16x32_bf16 v[232:235], v[232:235], v[8:11], 0\n"
        "v_mfma_f32_16x16x32_bf16 v[236:239], v[236:239], v[8:11], 0\n"
        "s_waitcnt lgkmcnt(3)\n"
        "v_mfma_f32_16x16x32_bf16 v[224:227], v[240:243], v[12:15], v[224:227]\n"
        "v_mfma_f32_16x16x32_bf16 v[228:231], v[244:247], v[12:15], v[228:231]\n"
        "v_mfma_f32_16x16x32_bf16 v[232:235], v[248:251], v[12:15], v[232:235]\n"
        "v_mfma_f32_16x16x32_bf16 v[236:239], v[252:255], v[12:15], v[236:239]\n"
        "s_waitcnt lgkmcnt(2)\n"
        "v_mfma_f32_16x16x32_bf16 v[176:179], v[176:179], v[16:19], 0\n"
        "v_mfma_f32_16x16x32_bf16 v[180:183], v[180:183], v[16:19], 0\n"
        "v_mfma_f32_16x16x32_bf16 v[184:187], v[184:187], v[16:19], 0\n"
        "v_mfma_f32_16x16x32_bf16 v[188:191], v[188:191], v[16:19], 0\n"
        "v_cvt_pk_bf16_f32 v224, v224, v225\n"
        "v_cvt_pk_bf16_f32 v225, v226, v227\n"
        "v_cvt_pk_bf16_f32 v226, v228, v229\n"
        "v_cvt_pk_bf16_f32 v227, v230, v231\n"
        "ds_write_b128 v41, v[224:227] offset:24064\n"
        "v_cvt_pk_bf16_f32 v232, v232, v233\n"
        "v_cvt_pk_bf16_f32 v233, v234, v235\n"
        "v_cvt_pk_bf16_f32 v234, v236, v237\n"
        "v_cvt_pk_bf16_f32 v235, v238, v239\n"
        "ds_write_b128 v41, v[232:235] offset:24128\n"
        "s_waitcnt lgkmcnt(3)\n"
        "v_mfma_f32_16x16x32_bf16 v[176:179], v[192:195], v[24:27], v[176:179]\n"
        "v_mfma_f32_16x16x32_bf16 v[180:183], v[196:199], v[24:27], v[180:183]\n"
        "v_mfma_f32_16x16x32_bf16 v[184:187], v[200:203], v[24:27], v[184:187]\n"
        "v_mfma_f32_16x16x32_bf16 v[188:191], v[204:207], v[24:27], v[188:191]\n"
        "s_waitcnt lgkmcnt(2)\n"
        "v_mfma_f32_16x16x32_bf16 v[208:211], v[208:211], v[28:31], 0\n"
        "v_mfma_f32_16x16x32_bf16 v[212:215], v[212:215], v[28:31], 0\n"
        "v_mfma_f32_16x16x32_bf16 v[216:219], v[216:219], v[28:31], 0\n"
        "v_mfma_f32_16x16x32_bf16 v[220:223], v[220:223], v[28:31], 0\n"
        "v_cvt_pk_bf16_f32 v176, v176, v177\n"
        "v_cvt_pk_bf16_f32 v177, v178, v179\n"
        "v_cvt_pk_bf16_f32 v178, v180, v181\n"
        "v_cvt_pk_bf16_f32 v179, v182, v183\n"
        "ds_write_b128 v41, v[176:179] offset:32512\n"
        "v_cvt_pk_bf16_f32 v184, v184, v185\n"
        "v_cvt_pk_bf16_f32 v185, v186, v187\n"
        "v_cvt_pk_bf16_f32 v186, v188, v189\n"
        "v_cvt_pk_bf16_f32 v187, v190, v191\n"
        "ds_write_b128 v41, v[184:187] offset:32576\n"
        "s_mov_b32 exec_lo, 0x00ff00ff\n"
        "s_mov_b32 exec_hi, 0x00ff00ff\n"
        "v_cvt_pk_bf16_f32 v208, v208, v209\n"
        "v_cvt_pk_bf16_f32 v209, v212, v213\n"
        "v_cvt_pk_bf16_f32 v210, v210, v211\n"
        "v_cvt_pk_bf16_f32 v211, v214, v215\n"
        "ds_write2_b64 v43, v[208:209], v[210:211] offset0:0 offset1:34\n"
        "v_cvt_pk_bf16_f32 v216, v216, v217\n"
        "v_cvt_pk_bf16_f32 v217, v220, v221\n"
        "v_cvt_pk_bf16_f32 v218, v218, v219\n"
        "v_cvt_pk_bf16_f32 v219, v222, v223\n"
        "ds_write2_b64 v43, v[216:217], v[218:219] offset0:4 offset1:38\n"
        "s_mov_b64 exec, -1\n"
        "ds_add_u32 v2, v3 offset:45488\n"
        "s_setprio 0\n"
        "s_mul_i32 s76, s2, 20480\n"
        "s_lshl_b32 s77, s72, 7\n"
        "s_add_u32 s76, s76, s77\n"
        "ds_read_b32 v8, v2 offset:45488\n"
        "s_waitcnt lgkmcnt(0)\n"
        "v_readfirstlane_b32 s73, v8\n"
        "s_cmp_ge_u32 s73, 4\n"
        "s_cbranch_scc0 .Lw_39\n"
        ".Le_40:\n"
        "ds_read_b128 v[224:227], v23 offset:24064\n"
        "ds_read_b128 v[228:231], v23 offset:24128\n"
        "ds_read_b128 v[232:235], v23 offset:32512\n"
        "ds_read_b128 v[236:239], v23 offset:32576\n"
        "ds_read_b128 v[240:243], v33 offset:40960\n"
        "v_mov_b64 v[208:209], 0\n"
        "v_mov_b64 v[210:211], 0\n"
        "v_mov_b64 v[212:213], 0\n"
        "v_mov_b64 v[214:215], 0\n"
        "v_mov_b64 v[216:217], 0\n"
        "v_mov_b64 v[218:219], 0\n"
        "v_mov_b64 v[220:221], 0\n"
        "v_mov_b64 v[222:223], 0\n"
        "ds_read_b128 v[248:251], v23 offset:24192\n"
        "ds_read_b128 v[252:255], v23 offset:24256\n"
        "ds_read_b128 v[8:11], v23 offset:32640\n"
        "ds_read_b128 v[12:15], v23 offset:32704\n"
        "ds_read_b128 v[16:19], v33 offset:41024\n"
        "s_waitcnt lgkmcnt(8)\n"
        "v_mfma_f32_16x16x32_bf16 v[176:179], v[44:47], v[224:227], 0\n"
        "v_mfma_f32_16x16x32_bf16 v[180:183], v[52:55], v[224:227], 0\n"
        "v_mfma_f32_16x16x32_bf16 v[184:187], v[60:63], v[224:227], 0\n"
        "v_mfma_f32_16x16x32_bf16 v[188:191], v[68:71], v[224:227], 0\n"
        "v_mfma_f32_16x16x32_bf16 v[176:179], v[48:51], v[228:231], v[176:179]\n"
        "v_mfma_f32_16x16x32_bf16 v[180:183], v[56:59], v[228:231], v[180:183]\n"
        "v_mfma_f32_16x16x32_bf16 v[184:187], v[64:67], v[228:231], v[184:187]\n"
        "v_mfma_f32_16x16x32_bf16 v[188:191], v[72:75], v[228:231], v[188:191]\n"
        "s_waitcnt lgkmcnt(6)\n"
        "v_mfma_f32_16x16x32_bf16 v[192:195], v[44:47], v[232:235], 0\n"
        "v_mfma_f32_16x16x32_bf16 v[196:199], v[52:55], v[232:235], 0\n"
        "v_mfma_f32_16x16x32_bf16 v[200:203], v[60:63], v[232:235], 0\n"
        "v_mfma_f32_16x16x32_bf16 v[204:207], v[68:71], v[232:235], 0\n"
        "v_mfma_f32_16x16x32_bf16 v[192:195], v[48:51], v[236:239], v[192:195]\n"
        "v_mfma_f32_16x16x32_bf16 v[196:199], v[56:59], v[236:239], v[196:199]\n"
        "v_mfma_f32_16x16x32_bf16 v[200:203], v[64:67], v[236:239], v[200:203]\n"
        "v_mfma_f32_16x16x32_bf16 v[204:207], v[72:75], v[236:239], v[204:207]\n"
        "s_waitcnt lgkmcnt(5)\n"
        "v_smfmac_f32_16x16x64_bf16 v[208:211], v[240:243], v[44:51], v0\n"
        "v_smfmac_f32_16x16x64_bf16 v[212:215], v[240:243], v[52:59], v0\n"
        "v_smfmac_f32_16x16x64_bf16 v[216:219], v[240:243], v[60:67], v0\n"
        "v_smfmac_f32_16x16x64_bf16 v[220:223], v[240:243], v[68:75], v0\n"
        "s_setprio 3\n"
        "buffer_load_dwordx4 v[44:47], v5, s[44:47], s8 offen offset:0 sc1\n"
        "buffer_load_dwordx4 v[48:51], v5, s[44:47], s8 offen offset:1024 sc1\n"
        "buffer_load_dwordx4 v[52:55], v5, s[44:47], s8 offen offset:2048 sc1\n"
        "buffer_load_dwordx4 v[56:59], v5, s[44:47], s8 offen offset:3072 sc1\n"
        "buffer_load_dwordx4 v[60:63], v5, s[44:47], s9 offen offset:0 sc1\n"
        "buffer_load_dwordx4 v[64:67], v5, s[44:47], s9 offen offset:1024 sc1\n"
        "buffer_load_dwordx4 v[68:71], v5, s[44:47], s9 offen offset:2048 sc1\n"
        "buffer_load_dwordx4 v[72:75], v5, s[44:47], s9 offen offset:3072 sc1\n"
        "s_setprio 0\n"
        "ds_read_b128 v[224:227], v23 offset:24320\n"
        "ds_read_b128 v[228:231], v23 offset:24384\n"
        "ds_read_b128 v[232:235], v23 offset:32768\n"
        "ds_read_b128 v[236:239], v23 offset:32832\n"
        "ds_read_b128 v[240:243], v33 offset:41088\n"
        "s_waitcnt lgkmcnt(8)\n"
        "v_mfma_f32_16x16x32_bf16 v[176:179], v[76:79], v[248:251], v[176:179]\n"
        "v_mfma_f32_16x16x32_bf16 v[180:183], v[84:87], v[248:251], v[180:183]\n"
        "v_mfma_f32_16x16x32_bf16 v[184:187], v[92:95], v[248:251], v[184:187]\n"
        "v_mfma_f32_16x16x32_bf16 v[188:191], v[100:103], v[248:251], v[188:191]\n"
        "v_mfma_f32_16x16x32_bf16 v[176:179], v[80:83], v[252:255], v[176:179]\n"
        "v_mfma_f32_16x16x32_bf16 v[180:183], v[88:91], v[252:255], v[180:183]\n"
        "v_mfma_f32_16x16x32_bf16 v[184:187], v[96:99], v[252:255], v[184:187]\n"
        "v_mfma_f32_16x16x32_bf16 v[188:191], v[104:107], v[252:255], v[188:191]\n"
        "s_waitcnt lgkmcnt(6)\n"
        "v_mfma_f32_16x16x32_bf16 v[192:195], v[76:79], v[8:11], v[192:195]\n"
        "v_mfma_f32_16x16x32_bf16 v[196:199], v[84:87], v[8:11], v[196:199]\n"
        "v_mfma_f32_16x16x32_bf16 v[200:203], v[92:95], v[8:11], v[200:203]\n"
        "v_mfma_f32_16x16x32_bf16 v[204:207], v[100:103], v[8:11], v[204:207]\n"
        "v_mfma_f32_16x16x32_bf16 v[192:195], v[80:83], v[12:15], v[192:195]\n"
        "v_mfma_f32_16x16x32_bf16 v[196:199], v[88:91], v[12:15], v[196:199]\n"
        "v_mfma_f32_16x16x32_bf16 v[200:203], v[96:99], v[12:15], v[200:203]\n"
        "v_mfma_f32_16x16x32_bf16 v[204:207], v[104:107], v[12:15], v[204:207]\n"
        "s_waitcnt lgkmcnt(5)\n"
        "v_smfmac_f32_16x16x64_bf16 v[208:211], v[16:19], v[76:83], v0\n"
        "v_smfmac_f32_16x16x64_bf16 v[212:215], v[16:19], v[84:91], v0\n"
        "v_smfmac_f32_16x16x64_bf16 v[216:219], v[16:19], v[92:99], v0\n"
        "v_smfmac_f32_16x16x64_bf16 v[220:223], v[16:19], v[100:107], v0\n"
        "s_setprio 3\n"
        "buffer_load_dwordx4 v[76:79], v5, s[44:47], s10 offen offset:0 sc1\n"
        "buffer_load_dwordx4 v[80:83], v5, s[44:47], s10 offen offset:1024 sc1\n"
        "buffer_load_dwordx4 v[84:87], v5, s[44:47], s10 offen offset:2048 sc1\n"
        "buffer_load_dwordx4 v[88:91], v5, s[44:47], s10 offen offset:3072 sc1\n"
        "buffer_load_dwordx4 v[92:95], v5, s[44:47], s11 offen offset:0 sc1\n"
        "buffer_load_dwordx4 v[96:99], v5, s[44:47], s11 offen offset:1024 sc1\n"
        "buffer_load_dwordx4 v[100:103], v5, s[44:47], s11 offen offset:2048 sc1\n"
        "buffer_load_dwordx4 v[104:107], v5, s[44:47], s11 offen offset:3072 sc1\n"
        "s_setprio 0\n"
        "ds_read_b128 v[248:251], v23 offset:24448\n"
        "ds_read_b128 v[252:255], v23 offset:24512\n"
        "ds_read_b128 v[8:11], v23 offset:32896\n"
        "ds_read_b128 v[12:15], v23 offset:32960\n"
        "ds_read_b128 v[16:19], v33 offset:41152\n"
        "s_waitcnt lgkmcnt(8)\n"
        "v_mfma_f32_16x16x32_bf16 v[176:179], v[108:111], v[224:227], v[176:179]\n"
        "v_mfma_f32_16x16x32_bf16 v[180:183], v[116:119], v[224:227], v[180:183]\n"
        "v_mfma_f32_16x16x32_bf16 v[184:187], v[124:127], v[224:227], v[184:187]\n"
        "v_mfma_f32_16x16x32_bf16 v[188:191], v[132:135], v[224:227], v[188:191]\n"
        "v_mfma_f32_16x16x32_bf16 v[176:179], v[112:115], v[228:231], v[176:179]\n"
        "v_mfma_f32_16x16x32_bf16 v[180:183], v[120:123], v[228:231], v[180:183]\n"
        "v_mfma_f32_16x16x32_bf16 v[184:187], v[128:131], v[228:231], v[184:187]\n"
        "v_mfma_f32_16x16x32_bf16 v[188:191], v[136:139], v[228:231], v[188:191]\n"
        "s_waitcnt lgkmcnt(6)\n"
        "v_mfma_f32_16x16x32_bf16 v[192:195], v[108:111], v[232:235], v[192:195]\n"
        "v_mfma_f32_16x16x32_bf16 v[196:199], v[116:119], v[232:235], v[196:199]\n"
        "v_mfma_f32_16x16x32_bf16 v[200:203], v[124:127], v[232:235], v[200:203]\n"
        "v_mfma_f32_16x16x32_bf16 v[204:207], v[132:135], v[232:235], v[204:207]\n"
        "v_mfma_f32_16x16x32_bf16 v[192:195], v[112:115], v[236:239], v[192:195]\n"
        "v_mfma_f32_16x16x32_bf16 v[196:199], v[120:123], v[236:239], v[196:199]\n"
        "v_mfma_f32_16x16x32_bf16 v[200:203], v[128:131], v[236:239], v[200:203]\n"
        "v_mfma_f32_16x16x32_bf16 v[204:207], v[136:139], v[236:239], v[204:207]\n"
        "s_waitcnt lgkmcnt(5)\n"
        "v_smfmac_f32_16x16x64_bf16 v[208:211], v[240:243], v[108:115], v0\n"
        "v_smfmac_f32_16x16x64_bf16 v[212:215], v[240:243], v[116:123], v0\n"
        "v_smfmac_f32_16x16x64_bf16 v[216:219], v[240:243], v[124:131], v0\n"
        "v_smfmac_f32_16x16x64_bf16 v[220:223], v[240:243], v[132:139], v0\n"
        "s_setprio 3\n"
        "buffer_load_dwordx4 v[108:111], v5, s[44:47], s12 offen offset:0 sc1\n"
        "buffer_load_dwordx4 v[112:115], v5, s[44:47], s12 offen offset:1024 sc1\n"
        "buffer_load_dwordx4 v[116:119], v5, s[44:47], s12 offen offset:2048 sc1\n"
        "buffer_load_dwordx4 v[120:123], v5, s[44:47], s12 offen offset:3072 sc1\n"
        "buffer_load_dwordx4 v[124:127], v5, s[44:47], s13 offen offset:0 sc1\n"
        "buffer_load_dwordx4 v[128:131], v5, s[44:47], s13 offen offset:1024 sc1\n"
        "buffer_load_dwordx4 v[132:135], v5, s[44:47], s13 offen offset:2048 sc1\n"
        "buffer_load_dwordx4 v[136:139], v5, s[44:47], s13 offen offset:3072 sc1\n"
        "s_setprio 0\n"
        "s_waitcnt vmcnt(24)\n"
        "s_waitcnt lgkmcnt(3)\n"
        "v_mfma_f32_16x16x32_bf16 v[176:179], v[140:143], v[248:251], v[176:179]\n"
        "v_mfma_f32_16x16x32_bf16 v[180:183], v[148:151], v[248:251], v[180:183]\n"
        "v_mfma_f32_16x16x32_bf16 v[184:187], v[156:159], v[248:251], v[184:187]\n"
        "v_mfma_f32_16x16x32_bf16 v[188:191], v[164:167], v[248:251], v[188:191]\n"
        "v_mfma_f32_16x16x32_bf16 v[176:179], v[144:147], v[252:255], v[176:179]\n"
        "v_mfma_f32_16x16x32_bf16 v[180:183], v[152:155], v[252:255], v[180:183]\n"
        "v_mfma_f32_16x16x32_bf16 v[184:187], v[160:163], v[252:255], v[184:187]\n"
        "v_mfma_f32_16x16x32_bf16 v[188:191], v[168:171], v[252:255], v[188:191]\n"
        "s_waitcnt lgkmcnt(1)\n"
        "v_mfma_f32_16x16x32_bf16 v[192:195], v[140:143], v[8:11], v[192:195]\n"
        "v_mfma_f32_16x16x32_bf16 v[196:199], v[148:151], v[8:11], v[196:199]\n"
        "v_mfma_f32_16x16x32_bf16 v[200:203], v[156:159], v[8:11], v[200:203]\n"
        "v_mfma_f32_16x16x32_bf16 v[204:207], v[164:167], v[8:11], v[204:207]\n"
        "v_mfma_f32_16x16x32_bf16 v[192:195], v[144:147], v[12:15], v[192:195]\n"
        "v_mfma_f32_16x16x32_bf16 v[196:199], v[152:155], v[12:15], v[196:199]\n"
        "v_mfma_f32_16x16x32_bf16 v[200:203], v[160:163], v[12:15], v[200:203]\n"
        "v_mfma_f32_16x16x32_bf16 v[204:207], v[168:171], v[12:15], v[204:207]\n"
        "s_waitcnt lgkmcnt(0)\n"
        "v_smfmac_f32_16x16x64_bf16 v[208:211], v[16:19], v[140:147], v0\n"
        "v_smfmac_f32_16x16x64_bf16 v[212:215], v[16:19], v[148:155], v0\n"
        "v_smfmac_f32_16x16x64_bf16 v[216:219], v[16:19], v[156:163], v0\n"
        "v_smfmac_f32_16x16x64_bf16 v[220:223], v[16:19], v[164:171], v0\n"
        "s_setprio 3\n"
        "buffer_load_dwordx4 v[140:143], v5, s[44:47], s14 offen offset:0 sc1\n"
        "buffer_load_dwordx4 v[144:147], v5, s[44:47], s14 offen offset:1024 sc1\n"
        "buffer_load_dwordx4 v[148:151], v5, s[44:47], s14 offen offset:2048 sc1\n"
        "buffer_load_dwordx4 v[152:155], v5, s[44:47], s14 offen offset:3072 sc1\n"
        "buffer_load_dwordx4 v[156:159], v5, s[44:47], s15 offen offset:0 sc1\n"
        "buffer_load_dwordx4 v[160:163], v5, s[44:47], s15 offen offset:1024 sc1\n"
        "buffer_load_dwordx4 v[164:167], v5, s[44:47], s15 offen offset:2048 sc1\n"
        "buffer_load_dwordx4 v[168:171], v5, s[44:47], s15 offen offset:3072 sc1\n"
        "s_setprio 0\n"
        "v_add_u32 v33, 17408, v33\n"
        "ds_read_b32 v8, v2 offset:45452\n"
        "s_waitcnt lgkmcnt(0)\n"
        "v_readfirstlane_b32 s73, v8\n"
        "s_cmp_ge_u32 s73, 8\n"
        "s_cbranch_scc0 .Lw_41\n"
        ".Le_42:\n"
        "s_cmp_ge_u32 s73, 0x1000\n"
        "s_cbranch_scc1 .Lexit\n"
        "ds_read_b128 v[224:227], v34 offset:0\n"
        "ds_read_b128 v[228:231], v34 offset:64\n"
        "ds_read_b128 v[232:235], v34 offset:128\n"
        "ds_read_b128 v[236:239], v34 offset:192\n"
        "ds_read_b128 v[240:243], v34 offset:272\n"
        "ds_read_b128 v[244:247], v34 offset:336\n"
        "ds_read_b128 v[248:251], v34 offset:400\n"
        "ds_read_b128 v[252:255], v34 offset:464\n"
        "s_waitcnt vmcnt(24)\n"
        "s_waitcnt lgkmcnt(7)\n"
        "v_mfma_f32_16x16x32_bf16 v[176:179], v[44:47], v[224:227], v[176:179]\n"
        "v_mfma_f32_16x16x32_bf16 v[180:183], v[52:55], v[224:227], v[180:183]\n"
        "v_mfma_f32_16x16x32_bf16 v[184:187], v[60:63], v[224:227], v[184:187]\n"
        "v_mfma_f32_16x16x32_bf16 v[188:191], v[68:71], v[224:227], v[188:191]\n"
        "s_waitcnt lgkmcnt(6)\n"
        "v_mfma_f32_16x16x32_bf16 v[176:179], v[48:51], v[228:231], v[176:179]\n"
        "v_mfma_f32_16x16x32_bf16 v[180:183], v[56:59], v[228:231], v[180:183]\n"
        "v_mfma_f32_16x16x32_bf16 v[184:187], v[64:67], v[228:231], v[184:187]\n"
        "v_mfma_f32_16x16x32_bf16 v[188:191], v[72:75], v[228:231], v[188:191]\n"
        "s_waitcnt vmcnt(16)\n"
        "s_waitcnt lgkmcnt(5)\n"
        "v_mfma_f32_16x16x32_bf16 v[176:179], v[76:79], v[232:235], v[176:179]\n"
        "v_mfma_f32_16x16x32_bf16 v[180:183], v[84:87], v[232:235], v[180:183]\n"
        "v_mfma_f32_16x16x32_bf16 v[184:187], v[92:95], v[232:235], v[184:187]\n"
        "v_mfma_f32_16x16x32_bf16 v[188:191], v[100:103], v[232:235], v[188:191]\n"
        "s_waitcnt lgkmcnt(4)\n"
        "v_mfma_f32_16x16x32_bf16 v[176:179], v[80:83], v[236:239], v[176:179]\n"
        "v_mfma_f32_16x16x32_bf16 v[180:183], v[88:91], v[236:239], v[180:183]\n"
        "v_mfma_f32_16x16x32_bf16 v[184:187], v[96:99], v[236:239], v[184:187]\n"
        "v_mfma_f32_16x16x32_bf16 v[188:191], v[104:107], v[236:239], v[188:191]\n"
        "s_waitcnt vmcnt(8)\n"
        "s_waitcnt lgkmcnt(3)\n"
        "v_mfma_f32_16x16x32_bf16 v[176:179], v[108:111], v[240:243], v[176:179]\n"
        "v_mfma_f32_16x16x32_bf16 v[180:183], v[116:119], v[240:243], v[180:183]\n"
        "v_mfma_f32_16x16x32_bf16 v[184:187], v[124:127], v[240:243], v[184:187]\n"
        "v_mfma_f32_16x16x32_bf16 v[188:191], v[132:135], v[240:243], v[188:191]\n"
        "s_waitcnt lgkmcnt(2)\n"
        "v_mfma_f32_16x16x32_bf16 v[176:179], v[112:115], v[244:247], v[176:179]\n"
        "v_mfma_f32_16x16x32_bf16 v[180:183], v[120:123], v[244:247], v[180:183]\n"
        "v_mfma_f32_16x16x32_bf16 v[184:187], v[128:131], v[244:247], v[184:187]\n"
        "v_mfma_f32_16x16x32_bf16 v[188:191], v[136:139], v[244:247], v[188:191]\n"
        "s_waitcnt vmcnt(0)\n"
        "s_waitcnt lgkmcnt(1)\n"
        "v_mfma_f32_16x16x32_bf16 v[176:179], v[140:143], v[248:251], v[176:179]\n"
        "v_mfma_f32_16x16x32_bf16 v[180:183], v[148:151], v[248:251], v[180:183]\n"
        "v_mfma_f32_16x16x32_bf16 v[184:187], v[156:159], v[248:251], v[184:187]\n"
        "v_mfma_f32_16x16x32_bf16 v[188:191], v[164:167], v[248:251], v[188:191]\n"
        "s_waitcnt lgkmcnt(0)\n"
        "v_mfma_f32_16x16x32_bf16 v[176:179], v[144:147], v[252:255], v[176:179]\n"
        "v_mfma_f32_16x16x32_bf16 v[180:183], v[152:155], v[252:255], v[180:183]\n"
        "v_mfma_f32_16x16x32_bf16 v[184:187], v[160:163], v[252:255], v[184:187]\n"
        "v_mfma_f32_16x16x32_bf16 v[188:191], v[168:171], v[252:255], v[188:191]\n"
        "ds_read_b32 v8, v2 offset:45468\n"
        "s_waitcnt lgkmcnt(0)\n"
        "v_readfirstlane_b32 s73, v8\n"
        "s_cmp_ge_u32 s73, 4\n"
        "s_cbranch_scc0 .Lw_43\n"
        ".Le_44:\n"
        "ds_read_b128 v[224:227], v34 offset:8704\n"
        "ds_read_b128 v[228:231], v34 offset:8768\n"
        "ds_read_b128 v[232:235], v34 offset:8832\n"
        "ds_read_b128 v[236:239], v34 offset:8896\n"
        "ds_read_b128 v[240:243], v34 offset:8976\n"
        "ds_read_b128 v[244:247], v34 offset:9040\n"
        "ds_read_b128 v[248:251], v34 offset:9104\n"
        "ds_read_b128 v[252:255], v34 offset:9168\n"
        "s_waitcnt lgkmcnt(7)\n"
        "v_mfma_f32_16x16x32_bf16 v[192:195], v[44:47], v[224:227], v[192:195]\n"
        "v_mfma_f32_16x16x32_bf16 v[196:199], v[52:55], v[224:227], v[196:199]\n"
        "v_mfma_f32_16x16x32_bf16 v[200:203], v[60:63], v[224:227], v[200:203]\n"
        "v_mfma_f32_16x16x32_bf16 v[204:207], v[68:71], v[224:227], v[204:207]\n"
        "s_waitcnt lgkmcnt(6)\n"
        "v_mfma_f32_16x16x32_bf16 v[192:195], v[48:51], v[228:231], v[192:195]\n"
        "v_mfma_f32_16x16x32_bf16 v[196:199], v[56:59], v[228:231], v[196:199]\n"
        "v_mfma_f32_16x16x32_bf16 v[200:203], v[64:67], v[228:231], v[200:203]\n"
        "v_mfma_f32_16x16x32_bf16 v[204:207], v[72:75], v[228:231], v[204:207]\n"
        "v_cvt_pk_bf16_f32 v12, v176, v180\n"
        "v_cvt_pk_bf16_f32 v14, v178, v182\n"
        "v_cvt_pk_bf16_f32 v13, v184, v188\n"
        "v_cvt_pk_bf16_f32 v15, v186, v190\n"
        "v_cvt_pk_bf16_f32 v16, v177, v181\n"
        "v_cvt_pk_bf16_f32 v18, v179, v183\n"
        "v_cvt_pk_bf16_f32 v17, v185, v189\n"
        "v_cvt_pk_bf16_f32 v19, v187, v191\n"
        "v_permlane32_swap_b32 v12, v14\n"
        "v_permlane32_swap_b32 v13, v15\n"
        "v_permlane32_swap_b32 v16, v18\n"
        "v_permlane32_swap_b32 v17, v19\n"
        "buffer_store_dwordx4 v[12:15], v35, s[48:51], s76 offen offset:0 sc1\n"
        "buffer_store_dwordx4 v[16:19], v35, s[48:51], s76 offen offset:64 sc1\n"
        "s_add_u32 s76, s76, 0x2000\n"
        "s_waitcnt lgkmcnt(5)\n"
        "v_mfma_f32_16x16x32_bf16 v[192:195], v[76:79], v[232:235], v[192:195]\n"
        "v_mfma_f32_16x16x32_bf16 v[196:199], v[84:87], v[232:235], v[196:199]\n"
        "v_mfma_f32_16x16x32_bf16 v[200:203], v[92:95], v[232:235], v[200:203]\n"
        "v_mfma_f32_16x16x32_bf16 v[204:207], v[100:103], v[232:235], v[204:207]\n"
        "s_waitcnt lgkmcnt(4)\n"
        "v_mfma_f32_16x16x32_bf16 v[192:195], v[80:83], v[236:239], v[192:195]\n"
        "v_mfma_f32_16x16x32_bf16 v[196:199], v[88:91], v[236:239], v[196:199]\n"
        "v_mfma_f32_16x16x32_bf16 v[200:203], v[96:99], v[236:239], v[200:203]\n"
        "v_mfma_f32_16x16x32_bf16 v[204:207], v[104:107], v[236:239], v[204:207]\n"
        "s_waitcnt lgkmcnt(3)\n"
        "v_mfma_f32_16x16x32_bf16 v[192:195], v[108:111], v[240:243], v[192:195]\n"
        "v_mfma_f32_16x16x32_bf16 v[196:199], v[116:119], v[240:243], v[196:199]\n"
        "v_mfma_f32_16x16x32_bf16 v[200:203], v[124:127], v[240:243], v[200:203]\n"
        "v_mfma_f32_16x16x32_bf16 v[204:207], v[132:135], v[240:243], v[204:207]\n"
        "s_waitcnt lgkmcnt(2)\n"
        "v_mfma_f32_16x16x32_bf16 v[192:195], v[112:115], v[244:247], v[192:195]\n"
        "v_mfma_f32_16x16x32_bf16 v[196:199], v[120:123], v[244:247], v[196:199]\n"
        "v_mfma_f32_16x16x32_bf16 v[200:203], v[128:131], v[244:247], v[200:203]\n"
        "v_mfma_f32_16x16x32_bf16 v[204:207], v[136:139], v[244:247], v[204:207]\n"
        "s_waitcnt lgkmcnt(1)\n"
        "v_mfma_f32_16x16x32_bf16 v[192:195], v[140:143], v[248:251], v[192:195]\n"
        "v_mfma_f32_16x16x32_bf16 v[196:199], v[148:151], v[248:251], v[196:199]\n"
        "v_mfma_f32_16x16x32_bf16 v[200:203], v[156:159], v[248:251], v[200:203]\n"
        "v_mfma_f32_16x16x32_bf16 v[204:207], v[164:167], v[248:251], v[204:207]\n"
        "s_waitcnt lgkmcnt(0)\n"
        "v_mfma_f32_16x16x32_bf16 v[192:195], v[144:147], v[252:255], v[192:195]\n"
        "v_mfma_f32_16x16x32_bf16 v[196:199], v[152:155], v[252:255], v[196:199]\n"
        "v_mfma_f32_16x16x32_bf16 v[200:203], v[160:163], v[252:255], v[200:203]\n"
        "v_mfma_f32_16x16x32_bf16 v[204:207], v[168:171], v[252:255], v[204:207]\n"
        "s_nop 5\n"
        "v_cvt_pk_bf16_f32 v20, v192, v196\n"
        "v_cvt_pk_bf16_f32 v22, v194, v198\n"
        "v_cvt_pk_bf16_f32 v21, v200, v204\n"
        "v_cvt_pk_bf16_f32 v23, v202, v206\n"
        "v_cvt_pk_bf16_f32 v24, v193, v197\n"
        "v_cvt_pk_bf16_f32 v26, v195, v199\n"
        "v_cvt_pk_bf16_f32 v25, v201, v205\n"
        "v_cvt_pk_bf16_f32 v27, v203, v207\n"
        "v_permlane32_swap_b32 v20, v22\n"
        "v_permlane32_swap_b32 v21, v23\n"
        "v_permlane32_swap_b32 v24, v26\n"
        "v_permlane32_swap_b32 v25, v27\n"
        "buffer_store_dwordx4 v[20:23], v35, s[48:51], s76 offen offset:0 sc1\n"
        "buffer_store_dwordx4 v[24:27], v35, s[48:51], s76 offen offset:64 sc1\n"
        "s_add_u32 s76, s76, 0x2000\n"
        "s_mov_b32 s74, 0x100000\n"
        ".Ltight:\n"
        "ds_read_b32 v8, v2 offset:45476\n"
        "ds_read_b128 v[224:227], v33 offset:0\n"
        "ds_read_b128 v[228:231], v33 offset:64\n"
        "s_sub_u32 s74, s74, 1\n"
        "s_cbranch_scc1 .Ltight_e\n"
        "s_waitcnt lgkmcnt(2)\n"
        "v_readfirstlane_b32 s73, v8\n"
        "s_cmp_lt_u32 s73, 4\n"
        "s_cbranch_scc1 .Ltight\n"
        ".Ltight_e:\n"
        "ds_read_b128 v[232:235], v33 offset:128\n"
        "ds_read_b128 v[236:239], v33 offset:192\n"
        "s_waitcnt lgkmcnt(3)\n"
        "v_smfmac_f32_16x16x64_bf16 v[208:211], v[224:227], v[44:51], v0\n"
        "v_smfmac_f32_16x16x64_bf16 v[212:215], v[224:227], v[52:59], v0\n"
        "v_smfmac_f32_16x16x64_bf16 v[216:219], v[224:227], v[60:67], v0\n"
        "v_smfmac_f32_16x16x64_bf16 v[220:223], v[224:227], v[68:75], v0\n"
        "s_waitcnt lgkmcnt(2)\n"
        "v_smfmac_f32_16x16x64_bf16 v[208:211], v[228:231], v[76:83], v0\n"
        "v_smfmac_f32_16x16x64_bf16 v[212:215], v[228:231], v[84:91], v0\n"
        "v_smfmac_f32_16x16x64_bf16 v[216:219], v[228:231], v[92:99], v0\n"
        "v_smfmac_f32_16x16x64_bf16 v[220:223], v[228:231], v[100:107], v0\n"
        "s_waitcnt lgkmcnt(1)\n"
        "v_smfmac_f32_16x16x64_bf16 v[208:211], v[232:235], v[108:115], v0\n"
        "v_smfmac_f32_16x16x64_bf16 v[212:215], v[232:235], v[116:123], v0\n"
        "v_smfmac_f32_16x16x64_bf16 v[216:219], v[232:235], v[124:131], v0\n"
        "v_smfmac_f32_16x16x64_bf16 v[220:223], v[232:235], v[132:139], v0\n"
        "s_waitcnt lgkmcnt(0)\n"
        "v_smfmac_f32_16x16x64_bf16 v[208:211], v[236:239], v[140:147], v0\n"
        "v_smfmac_f32_16x16x64_bf16 v[212:215], v[236:239], v[148:155], v0\n"
        "v_smfmac_f32_16x16x64_bf16 v[216:219], v[236:239], v[156:163], v0\n"
        "v_smfmac_f32_16x16x64_bf16 v[220:223], v[236:239], v[164:171], v0\n"
        "s_nop 4\n"
        "v_add_f32 v208, v208, v209\n"
        "v_add_f32 v212, v212, v213\n"
        "v_add_f32 v216, v216, v217\n"
        "v_add_f32 v220, v220, v221\n"
        "v_add_f32 v210, v210, v211\n"
        "v_add_f32 v214, v214, v215\n"
        "v_add_f32 v218, v218, v219\n"
        "v_add_f32 v222, v222, v223\n"
        "v_cvt_pk_bf16_f32 v12, v208, v212\n"
        "v_cvt_pk_bf16_f32 v13, v216, v220\n"
        "v_cvt_pk_bf16_f32 v16, v208, v212\n"
        "v_cvt_pk_bf16_f32 v17, v216, v220\n"
        "v_cvt_pk_bf16_f32 v14, v210, v214\n"
        "v_cvt_pk_bf16_f32 v15, v218, v222\n"
        "s_nop 0\n"
        "v_mov_b32_dpp v12, v14 row_ror:8 row_mask:0xf bank_mask:0xc\n"
        "v_mov_b32_dpp v13, v15 row_ror:8 row_mask:0xf bank_mask:0xc\n"
        "v_mov_b32_dpp v14, v16 row_ror:8 row_mask:0xf bank_mask:0x3\n"
        "v_mov_b32_dpp v15, v17 row_ror:8 row_mask:0xf bank_mask:0x3\n"
        "buffer_store_dwordx4 v[12:15], v42, s[48:51], s76 offen sc1\n"
        "s_endpgm\n"
        ".Lw_3:\n"
        "s_mov_b32 s74, 0x40000\n"
        ".Lw_3_1:\n"
        "ds_read_b32 v15, v2 offset:45500\n"
        "s_waitcnt lgkmcnt(0)\n"
        "v_readfirstlane_b32 s73, v15\n"
        "s_cmp_ge_u32 s73, 4\n"
        "s_cbranch_scc1 .Le_4\n"
        "s_sub_u32 s74, s74, 1\n"
        "s_cbranch_scc1 .Le_4\n"
        "s_sleep 1\n"
        "s_branch .Lw_3_1\n"
        ".Lw_5:\n"
        "s_mov_b32 s74, 0x40000\n"
        ".Lw_5_1:\n"
        "ds_read_b32 v113, v2 offset:45500\n"
        "s_waitcnt lgkmcnt(0)\n"
        "v_readfirstlane_b32 s73, v113\n"
        "s_cmp_ge_u32 s73, 4\n"
        "s_cbranch_scc1 .Le_6\n"
        "s_sub_u32 s74, s74, 1\n"
        "s_cbranch_scc1 .Le_6\n"
        "s_sleep 1\n"
        "s_branch .Lw_5_1\n"
        ".Lw_7:\n"
        "s_mov_b32 s74, 0x40000\n"
        ".Lw_7_1:\n"
        "ds_read_b32 v8, v2 offset:45516\n"
        "s_waitcnt lgkmcnt(0)\n"
        "v_readfirstlane_b32 s73, v8\n"
        "s_cmp_ge_u32 s73, 4\n"
        "s_cbranch_scc1 .Le_8\n"
        "s_sub_u32 s74, s74, 1\n"
        "s_cbranch_scc1 .Le_8\n"
        "s_sleep 1\n"
        "s_branch .Lw_7_1\n"
        ".Lw_9:\n"
        "s_mov_b32 s74, 0x40000\n"
        ".Lw_9_1:\n"
        "ds_read_b32 v86, v2 offset:45512\n"
        "s_waitcnt lgkmcnt(0)\n"
        "v_readfirstlane_b32 s73, v86\n"
        "s_cmp_ge_u32 s73, 4\n"
        "s_cbranch_scc1 .Le_10\n"
        "s_sub_u32 s74, s74, 1\n"
        "s_cbranch_scc1 .Le_10\n"
        "s_sleep 1\n"
        "s_branch .Lw_9_1\n"
        ".Lcn_again:\n"
        "s_sub_u32 s74, s74, 1\n"
        "s_cbranch_scc1 .Lcn_done\n"
        "s_branch .Lcn\n"
        ".Lw_11:\n"
        "s_mov_b32 s74, 0x40000\n"
        ".Lw_11_1:\n"
        "ds_read_b32 v40, v2 offset:45504\n"
        "s_waitcnt lgkmcnt(0)\n"
        "v_readfirstlane_b32 s73, v40\n"
        "s_cmp_ge_u32 s73, 4\n"
        "s_cbranch_scc1 .Le_12\n"
        "s_sub_u32 s74, s74, 1\n"
        "s_cbranch_scc1 .Le_12\n"
        "s_sleep 1\n"
        "s_branch .Lw_11_1\n"
        ".Lw_13:\n"
        "s_mov_b32 s74, 0x40000\n"
        ".Lw_13_1:\n"
        "ds_read_b32 v40, v2 offset:45520\n"
        "s_waitcnt lgkmcnt(0)\n"
        "v_readfirstlane_b32 s73, v40\n"
        "s_cmp_ge_u32 s73, 4\n"
        "s_cbranch_scc1 .Le_14\n"
        "s_sub_u32 s74, s74, 1\n"
        "s_cbranch_scc1 .Le_14\n"
        "s_sleep 1\n"
        "s_branch .Lw_13_1\n"
        ".Lw_15:\n"
        "s_mov_b32 s74, 0x40000\n"
        ".Lw_15_1:\n"
        "ds_read_b32 v28, v2 offset:45516\n"
        "s_waitcnt lgkmcnt(0)\n"
        "v_readfirstlane_b32 s73, v28\n"
        "s_cmp_ge_u32 s73, 4\n"
        "s_cbranch_scc1 .Le_16\n"
        "s_sub_u32 s74, s74, 1\n"
        "s_cbranch_scc1 .Le_16\n"
        "s_sleep 1\n"
        "s_branch .Lw_15_1\n"
        ".Lw_17:\n"
        "s_mov_b32 s74, 0x40000\n"
        ".Lw_17_1:\n"
        "ds_read_b32 v28, v2 offset:45500\n"
        "s_waitcnt lgkmcnt(0)\n"
        "v_readfirstlane_b32 s73, v28\n"
        "s_cmp_ge_u32 s73, 4\n"
        "s_cbranch_scc1 .Le_18\n"
        "s_sub_u32 s74, s74, 1\n"
        "s_cbranch_scc1 .Le_18\n"
        "s_sleep 1\n"
        "s_branch .Lw_17_1\n"
        ".Lw_20:\n"
        "s_mov_b32 s74, 0x40000\n"
        ".Lw_20_1:\n"
        "ds_read_b32 v28, v2 offset:45516\n"
        "s_waitcnt lgkmcnt(0)\n"
        "v_readfirstlane_b32 s73, v28\n"
        "s_cmp_ge_u32 s73, 4\n"
        "s_cbranch_scc1 .Le_21\n"
        "s_sub_u32 s74, s74, 1\n"
        "s_cbranch_scc1 .Le_21\n"
        "s_sleep 1\n"
        "s_branch .Lw_20_1\n"
        ".Lw_23:\n"
        "s_mov_b32 s74, 0x40000\n"
        ".Lw_23_1:\n"
        "ds_read_b32 v28, v2 offset:45516\n"
        "s_waitcnt lgkmcnt(0)\n"
        "v_readfirstlane_b32 s73, v28\n"
        "s_cmp_ge_u32 s73, 4\n"
        "s_cbranch_scc1 .Le_24\n"
        "s_sub_u32 s74, s74, 1\n"
        "s_cbranch_scc1 .Le_24\n"
        "s_sleep 1\n"
        "s_branch .Lw_23_1\n"
        ".Lw_26:\n"
        "s_mov_b32 s74, 0x40000\n"
        ".Lw_26_1:\n"
        "ds_read_b32 v8, v2 offset:45508\n"
        "s_waitcnt lgkmcnt(0)\n"
        "v_readfirstlane_b32 s73, v8\n"
        "s_cmp_ge_u32 s73, 4\n"
        "s_cbranch_scc1 .Le_27\n"
        "s_sub_u32 s74, s74, 1\n"
        "s_cbranch_scc1 .Le_27\n"
        "s_sleep 1\n"
        "s_branch .Lw_26_1\n"
        ".Lw_33:\n"
        "s_mov_b32 s74, 0x40000\n"
        ".Lw_33_1:\n"
        "ds_read_b32 v8, v2 offset:45496\n"
        "s_waitcnt lgkmcnt(0)\n"
        "v_readfirstlane_b32 s73, v8\n"
        "s_cmp_ge_u32 s73, 4\n"
        "s_cbranch_scc1 .Le_34\n"
        "s_sub_u32 s74, s74, 1\n"
        "s_cbranch_scc1 .Le_34\n"
        "s_sleep 1\n"
        "s_branch .Lw_33_1\n"
        ".Lw_35:\n"
        "s_mov_b32 s74, 0x40000\n"
        ".Lw_35_1:\n"
        "ds_read_b32 v8, v2 offset:45484\n"
        "s_waitcnt lgkmcnt(0)\n"
        "v_readfirstlane_b32 s73, v8\n"
        "s_cmp_ge_u32 s73, 4\n"
        "s_cbranch_scc1 .Le_36\n"
        "s_sub_u32 s74, s74, 1\n"
        "s_cbranch_scc1 .Le_36\n"
        "s_sleep 1\n"
        "s_branch .Lw_35_1\n"
        ".Lw_37:\n"
        "s_mov_b32 s74, 0x40000\n"
        ".Lw_37_1:\n"
        "ds_read_b32 v32, v2 offset:45532\n"
        "s_waitcnt lgkmcnt(0)\n"
        "v_readfirstlane_b32 s73, v32\n"
        "s_cmp_ge_u32 s73, 4\n"
        "s_cbranch_scc1 .Le_38\n"
        "s_sub_u32 s74, s74, 1\n"
        "s_cbranch_scc1 .Le_38\n"
        "s_sleep 1\n"
        "s_branch .Lw_37_1\n"
        ".Lw_39:\n"
        "s_mov_b32 s74, 0x40000\n"
        ".Lw_39_1:\n"
        "ds_read_b32 v8, v2 offset:45488\n"
        "s_waitcnt lgkmcnt(0)\n"
        "v_readfirstlane_b32 s73, v8\n"
        "s_cmp_ge_u32 s73, 4\n"
        "s_cbranch_scc1 .Le_40\n"
        "s_sub_u32 s74, s74, 1\n"
        "s_cbranch_scc1 .Le_40\n"
        "s_sleep 1\n"
        "s_branch .Lw_39_1\n"
        ".Lw_41:\n"
        "s_mov_b32 s74, 0x40000\n"
        ".Lw_41_1:\n"
        "ds_read_b32 v8, v2 offset:45452\n"
        "s_waitcnt lgkmcnt(0)\n"
        "v_readfirstlane_b32 s73, v8\n"
        "s_cmp_ge_u32 s73, 8\n"
        "s_cbranch_scc1 .Le_42\n"
        "s_sub_u32 s74, s74, 1\n"
        "s_cbranch_scc1 .Le_42\n"
        "s_sleep 1\n"
        "s_branch .Lw_41_1\n"
        ".Lw_43:\n"
        "s_mov_b32 s74, 0x40000\n"
        ".Lw_43_1:\n"
        "ds_read_b32 v8, v2 offset:45468\n"
        "s_waitcnt lgkmcnt(0)\n"
        "v_readfirstlane_b32 s73, v8\n"
        "s_cmp_ge_u32 s73, 4\n"
        "s_cbranch_scc1 .Le_44\n"
        "s_sub_u32 s74, s74, 1\n"
        "s_cbranch_scc1 .Le_44\n"
        "s_sleep 1\n"
        "s_branch .Lw_43_1\n"
        ".Lno_m:\n"
        "ds_add_u32 v2, v3 offset:45500\n"
        "s_waitcnt lgkmcnt(0)\n"
        "s_add_u32 s73, s64, 1\n"
        "s_addc_u32 s74, s65, 0\n"
        "s_mul_i32 s76, s73, 0x7f4a7c15\n"
        "s_mul_hi_u32 s77, s73, 0x7f4a7c15\n"
        "s_mul_i32 s75, s73, 0x9e3779b9\n"
        "s_add_u32 s77, s77, s75\n"
        "s_mul_i32 s75, s74, 0x7f4a7c15\n"
        "s_add_u32 s77, s77, s75\n"
        "s_xor_b32 s66, s76, 0xd192ed03\n"
        "s_or_b32 s66, s66, 3\n"
        "s_xor_b32 s67, s77, 0xd1b54a32\n"
        "s_xor_b32 s76, s76, 0x4f6cdd1d\n"
        "s_xor_b32 s77, s77, 0x2545f491\n"
        "s_and_b32 s73, s76, 3\n"
        "s_lshr_b32 s74, s76, 2\n"
        "s_lshl_b32 s74, s74, 23\n"
        "s_or_b32 s68, s73, s74\n"
        "s_lshr_b64 s[76:77], s[76:77], 11\n"
        "s_and_b32 s73, s76, 3\n"
        "s_lshr_b32 s74, s76, 2\n"
        "s_lshl_b32 s74, s74, 23\n"
        "s_or_b32 s69, s73, s74\n"
        "s_lshr_b64 s[76:77], s[76:77], 11\n"
        "s_and_b32 s73, s76, 3\n"
        "s_lshr_b32 s74, s76, 2\n"
        "s_lshl_b32 s74, s74, 23\n"
        "s_or_b32 s70, s73, s74\n"
        "s_lshr_b64 s[76:77], s[76:77], 11\n"
        "s_and_b32 s73, s76, 3\n"
        "s_lshr_b32 s74, s76, 2\n"
        "s_lshl_b32 s74, s74, 23\n"
        "s_or_b32 s71, s73, s74\n"
        "ds_add_u32 v2, v3 offset:45480\n"
        "s_cmp_ge_u32 s83, 200\n"
        "s_cbranch_scc1 .Lspecial_done\n"
        "s_waitcnt vmcnt(0)\n"
        "s_mov_b32 s78, 0x05040100\n"
        "s_mov_b32 s79, 0x07060302\n"
        "v_mov_b32 v172, v76\n"
        "v_mov_b32 v173, v77\n"
        "v_mov_b32 v174, v80\n"
        "v_mov_b32 v175, v81\n"
        "v_mov_b32_dpp v76, v78 row_ror:8 row_mask:0xf bank_mask:0xc\n"
        "v_mov_b32_dpp v77, v79 row_ror:8 row_mask:0xf bank_mask:0xc\n"
        "v_mov_b32_dpp v80, v82 row_ror:8 row_mask:0xf bank_mask:0xc\n"
        "v_mov_b32_dpp v81, v83 row_ror:8 row_mask:0xf bank_mask:0xc\n"
        "v_mov_b32_dpp v78, v172 row_ror:8 row_mask:0xf bank_mask:0x3\n"
        "v_mov_b32_dpp v79, v173 row_ror:8 row_mask:0xf bank_mask:0x3\n"
        "v_mov_b32_dpp v82, v174 row_ror:8 row_mask:0xf bank_mask:0x3\n"
        "v_mov_b32_dpp v83, v175 row_ror:8 row_mask:0xf bank_mask:0x3\n"
        "v_perm_b32 v44, v80, v76, s78\n"
        "v_perm_b32 v52, v80, v76, s79\n"
        "v_perm_b32 v60, v81, v77, s78\n"
        "v_perm_b32 v68, v81, v77, s79\n"
        "v_perm_b32 v48, v82, v78, s78\n"
        "v_perm_b32 v56, v82, v78, s79\n"
        "v_perm_b32 v64, v83, v79, s78\n"
        "v_perm_b32 v72, v83, v79, s79\n"
        "v_mov_b32 v172, v84\n"
        "v_mov_b32 v173, v85\n"
        "v_mov_b32 v174, v88\n"
        "v_mov_b32 v175, v89\n"
        "v_mov_b32_dpp v84, v86 row_ror:8 row_mask:0xf bank_mask:0xc\n"
        "v_mov_b32_dpp v85, v87 row_ror:8 row_mask:0xf bank_mask:0xc\n"
        "v_mov_b32_dpp v88, v90 row_ror:8 row_mask:0xf bank_mask:0xc\n"
        "v_mov_b32_dpp v89, v91 row_ror:8 row_mask:0xf bank_mask:0xc\n"
        "v_mov_b32_dpp v86, v172 row_ror:8 row_mask:0xf bank_mask:0x3\n"
        "v_mov_b32_dpp v87, v173 row_ror:8 row_mask:0xf bank_mask:0x3\n"
        "v_mov_b32_dpp v90, v174 row_ror:8 row_mask:0xf bank_mask:0x3\n"
        "v_mov_b32_dpp v91, v175 row_ror:8 row_mask:0xf bank_mask:0x3\n"
        "v_perm_b32 v45, v88, v84, s78\n"
        "v_perm_b32 v53, v88, v84, s79\n"
        "v_perm_b32 v61, v89, v85, s78\n"
        "v_perm_b32 v69, v89, v85, s79\n"
        "v_perm_b32 v49, v90, v86, s78\n"
        "v_perm_b32 v57, v90, v86, s79\n"
        "v_perm_b32 v65, v91, v87, s78\n"
        "v_perm_b32 v73, v91, v87, s79\n"
        "v_mov_b32 v172, v92\n"
        "v_mov_b32 v173, v93\n"
        "v_mov_b32 v174, v96\n"
        "v_mov_b32 v175, v97\n"
        "v_mov_b32_dpp v92, v94 row_ror:8 row_mask:0xf bank_mask:0xc\n"
        "v_mov_b32_dpp v93, v95 row_ror:8 row_mask:0xf bank_mask:0xc\n"
        "v_mov_b32_dpp v96, v98 row_ror:8 row_mask:0xf bank_mask:0xc\n"
        "v_mov_b32_dpp v97, v99 row_ror:8 row_mask:0xf bank_mask:0xc\n"
        "v_mov_b32_dpp v94, v172 row_ror:8 row_mask:0xf bank_mask:0x3\n"
        "v_mov_b32_dpp v95, v173 row_ror:8 row_mask:0xf bank_mask:0x3\n"
        "v_mov_b32_dpp v98, v174 row_ror:8 row_mask:0xf bank_mask:0x3\n"
        "v_mov_b32_dpp v99, v175 row_ror:8 row_mask:0xf bank_mask:0x3\n"
        "v_perm_b32 v46, v96, v92, s78\n"
        "v_perm_b32 v54, v96, v92, s79\n"
        "v_perm_b32 v62, v97, v93, s78\n"
        "v_perm_b32 v70, v97, v93, s79\n"
        "v_perm_b32 v50, v98, v94, s78\n"
        "v_perm_b32 v58, v98, v94, s79\n"
        "v_perm_b32 v66, v99, v95, s78\n"
        "v_perm_b32 v74, v99, v95, s79\n"
        "v_mov_b32 v172, v100\n"
        "v_mov_b32 v173, v101\n"
        "v_mov_b32 v174, v104\n"
        "v_mov_b32 v175, v105\n"
        "v_mov_b32_dpp v100, v102 row_ror:8 row_mask:0xf bank_mask:0xc\n"
        "v_mov_b32_dpp v101, v103 row_ror:8 row_mask:0xf bank_mask:0xc\n"
        "v_mov_b32_dpp v104, v106 row_ror:8 row_mask:0xf bank_mask:0xc\n"
        "v_mov_b32_dpp v105, v107 row_ror:8 row_mask:0xf bank_mask:0xc\n"
        "v_mov_b32_dpp v102, v172 row_ror:8 row_mask:0xf bank_mask:0x3\n"
        "v_mov_b32_dpp v103, v173 row_ror:8 row_mask:0xf bank_mask:0x3\n"
        "v_mov_b32_dpp v106, v174 row_ror:8 row_mask:0xf bank_mask:0x3\n"
        "v_mov_b32_dpp v107, v175 row_ror:8 row_mask:0xf bank_mask:0x3\n"
        "v_perm_b32 v47, v104, v100, s78\n"
        "v_perm_b32 v55, v104, v100, s79\n"
        "v_perm_b32 v63, v105, v101, s78\n"
        "v_perm_b32 v71, v105, v101, s79\n"
        "v_perm_b32 v51, v106, v102, s78\n"
        "v_perm_b32 v59, v106, v102, s79\n"
        "v_perm_b32 v67, v107, v103, s78\n"
        "v_perm_b32 v75, v107, v103, s79\n"
        "s_lshl_b32 s75, s72, 3\n"
        "s_add_u32 s75, s75, s83\n"
        "s_sub_u32 s75, s75, 192\n"
        "s_lshl_b32 s77, s75, 13\n"
        "s_add_u32 s77, s77, 139264\n"
        "s_add_u32 s76, s77, 0x1000\n"
        "s_lshl_b32 s78, s75, 3\n"
        "s_add_u32 s78, s78, 4352\n"
        "buffer_store_dwordx4 v[44:47], v4, s[44:47], s77 offen offset:0 sc1\n"
        "buffer_store_dwordx4 v[48:51], v4, s[44:47], s77 offen offset:1024 sc1\n"
        "buffer_store_dwordx4 v[52:55], v4, s[44:47], s77 offen offset:2048 sc1\n"
        "buffer_store_dwordx4 v[56:59], v4, s[44:47], s77 offen offset:3072 sc1\n"
        "buffer_store_dwordx4 v[60:63], v4, s[44:47], s76 offen offset:0 sc1\n"
        "buffer_store_dwordx4 v[64:67], v4, s[44:47], s76 offen offset:1024 sc1\n"
        "buffer_store_dwordx4 v[68:71], v4, s[44:47], s76 offen offset:2048 sc1\n"
        "buffer_store_dwordx4 v[72:75], v4, s[44:47], s76 offen offset:3072 sc1\n"
        "s_branch .Lflag_out\n"
        ".Len0:\n"
        "s_mov_b64 exec, s[36:37]\n"
        "v_and_or_b32 v16, v72, s17, v7\n"
        "v_lshrrev_b32 v32, 8, v72\n"
        "ds_add_rtn_u32 v16, v16, v46\n"
        "v_and_or_b32 v17, v73, s17, v7\n"
        "v_lshrrev_b32 v33, 8, v73\n"
        "ds_add_rtn_u32 v17, v17, v46\n"
        "v_and_or_b32 v18, v74, s17, v7\n"
        "v_lshrrev_b32 v34, 8, v74\n"
        "ds_add_rtn_u32 v18, v18, v46\n"
        "v_and_or_b32 v19, v75, s17, v7\n"
        "v_lshrrev_b32 v35, 8, v75\n"
        "ds_add_rtn_u32 v19, v19, v46\n"
        "s_mov_b64 exec, s[38:39]\n"
        "v_and_or_b32 v20, v76, s17, v7\n"
        "v_lshrrev_b32 v36, 8, v76\n"
        "ds_add_rtn_u32 v20, v20, v46\n"
        "v_and_or_b32 v21, v77, s17, v7\n"
        "v_lshrrev_b32 v37, 8, v77\n"
        "ds_add_rtn_u32 v21, v21, v46\n"
        "v_and_or_b32 v22, v78, s17, v7\n"
        "v_lshrrev_b32 v38, 8, v78\n"
        "ds_add_rtn_u32 v22, v22, v46\n"
        "v_and_or_b32 v23, v79, s17, v7\n"
        "v_lshrrev_b32 v39, 8, v79\n"
        "ds_add_rtn_u32 v23, v23, v46\n"
        "s_branch .Len0_e\n"
        ".Len1:\n"
        "s_mov_b64 exec, s[36:37]\n"
        "ds_read_u16 v32, v32\n"
        "ds_read_u16 v33, v33\n"
        "ds_read_u16 v34, v34\n"
        "ds_read_u16 v35, v35\n"
        "s_mov_b64 exec, s[38:39]\n"
        "ds_read_u16 v36, v36\n"
        "ds_read_u16 v37, v37\n"
        "ds_read_u16 v38, v38\n"
        "ds_read_u16 v39, v39\n"
        "s_branch .Len1_e\n"
        ".Lbp:\n"
        "ds_bpermute_b32 v100, v72, v83\n"
        "v_and_b32 v72, s73, v72\n"
        "ds_bpermute_b32 v101, v73, v83\n"
        "v_and_b32 v73, s73, v73\n"
        "ds_bpermute_b32 v102, v74, v83\n"
        "v_and_b32 v74, s73, v74\n"
        "ds_bpermute_b32 v103, v75, v83\n"
        "v_and_b32 v75, s73, v75\n"
        "ds_bpermute_b32 v104, v76, v83\n"
        "v_and_b32 v76, s73, v76\n"
        "ds_bpermute_b32 v105, v77, v83\n"
        "v_and_b32 v77, s73, v77\n"
        "ds_bpermute_b32 v106, v78, v83\n"
        "v_and_b32 v78, s73, v78\n"
        "ds_bpermute_b32 v107, v79, v83\n"
        "v_and_b32 v79, s73, v79\n"
        "s_branch .Lbp_e\n"
        ".Len2:\n"
        "s_mov_b64 exec, s[36:37]\n"
        "v_add_u32 v16, v16, v100\n"
        "ds_write2_b32 v16, v72, v32 offset0:3 offset1:113\n"
        "v_add_u32 v17, v17, v101\n"
        "ds_write2_b32 v17, v73, v33 offset0:3 offset1:113\n"
        "v_add_u32 v18, v18, v102\n"
        "ds_write2_b32 v18, v74, v34 offset0:3 offset1:113\n"
        "v_add_u32 v19, v19, v103\n"
        "ds_write2_b32 v19, v75, v35 offset0:3 offset1:113\n"
        "s_mov_b64 exec, s[38:39]\n"
        "v_add_u32 v20, v20, v104\n"
        "ds_write2_b32 v20, v76, v36 offset0:3 offset1:113\n"
        "v_add_u32 v21, v21, v105\n"
        "ds_write2_b32 v21, v77, v37 offset0:3 offset1:113\n"
        "v_add_u32 v22, v22, v106\n"
        "ds_write2_b32 v22, v78, v38 offset0:3 offset1:113\n"
        "v_add_u32 v23, v23, v107\n"
        "ds_write2_b32 v23, v79, v39 offset0:3 offset1:113\n"
        "s_branch .Len2_e\n"
        ".Lhint:\n"
        "s_setprio 3\n"
        "s_lshl_b32 s41, s72, 9\n"
        "s_add_u32 s41, s41, 2304\n"
        "s_mov_b32 exec_hi, 0\n"
        "buffer_load_dwordx4 v[24:27], v4, s[44:47], s41 offen sc1\n"
        "s_mov_b32 exec_hi, -1\n"
        "s_memrealtime s[76:77]\n"
        "s_waitcnt vmcnt(0) lgkmcnt(0)\n"
        "v_cmp_ne_u32 s[74:75], s66, v24\n"
        "v_cmp_ne_u32 vcc, s66, v26\n"
        "s_or_b32 s74, s74, vcc_lo\n"
        "s_cbranch_scc0 .Lhint_m\n"
        "s_sub_u32 s73, s76, s36\n"
        "s_cmp_gt_u32 s73, 20000\n"
        "s_cbranch_scc1 .Lhint_m\n"
        "s_sleep 6\n"
        "s_branch .Lhint\n"
        ".Lhint_m:\n"
        "buffer_load_dwordx4 v[224:227], v4, s[44:47], s8 offen offset:0 sc1\n"
        "buffer_load_dwordx4 v[228:231], v4, s[44:47], s10 offen offset:0 sc1\n"
        "buffer_load_dwordx4 v[232:235], v4, s[44:47], s12 offen offset:0 sc1\n"
        "buffer_load_dwordx4 v[236:239], v4, s[44:47], s14 offen offset:0 sc1\n"
        "buffer_load_dwordx4 v[240:243], v4, s[44:47], s8 offen offset:1024 sc1\n"
        "buffer_load_dwordx4 v[244:247], v4, s[44:47], s10 offen offset:1024 sc1\n"
        "buffer_load_dwordx4 v[248:251], v4, s[44:47], s12 offen offset:1024 sc1\n"
        "buffer_load_dwordx4 v[252:255], v4, s[44:47], s14 offen offset:1024 sc1\n"
        "buffer_load_dwordx4 v[156:159], v4, s[44:47], s8 offen offset:2048 sc1\n"
        "buffer_load_dwordx4 v[160:163], v4, s[44:47], s10 offen offset:2048 sc1\n"
        "buffer_load_dwordx4 v[164:167], v4, s[44:47], s12 offen offset:2048 sc1\n"
        "buffer_load_dwordx4 v[168:171], v4, s[44:47], s14 offen offset:2048 sc1\n"
        "buffer_load_dwordx4 v[140:143], v4, s[44:47], s8 offen offset:3072 sc1\n"
        "buffer_load_dwordx4 v[144:147], v4, s[44:47], s10 offen offset:3072 sc1\n"
        "buffer_load_dwordx4 v[148:151], v4, s[44:47], s12 offen offset:3072 sc1\n"
        "buffer_load_dwordx4 v[152:155], v4, s[44:47], s14 offen offset:3072 sc1\n"
        "s_setprio 1\n"
        "s_waitcnt vmcnt(0)\n"
        "s_branch .Lhint_e\n"
        ".Lpub_more:\n"
        "s_sub_u32 s75, s75, 0\n"
        "ds_read_b32 v14, v10 offset:0\n"
        "ds_read_b128 v[16:19], v10 offset:64\n"
        "s_waitcnt lgkmcnt(0)\n"
        "v_mov_b32_dpp v15, v14 quad_perm:[0,0,0,0] row_mask:0xf bank_mask:0xf\n"
        "s_movk_i32 s76, 0x2000\n"
        "v_cmp_le_u32 vcc, s76, v15\n"
        "s_mov_b64 exec, vcc\n"
        "v_or_b32 v16, s68, v16\n"
        "v_or_b32 v17, s69, v17\n"
        "v_or_b32 v18, s70, v18\n"
        "v_or_b32 v19, s71, v19\n"
        "buffer_store_dwordx4 v[16:19], v11, s[44:47], s75 offen sc1\n"
        "s_mov_b64 exec, -1\n"
        "s_add_u32 s75, s75, 4096\n"
        "ds_read_b32 v14, v10 offset:9216\n"
        "ds_read_b128 v[16:19], v10 offset:9280\n"
        "s_waitcnt lgkmcnt(0)\n"
        "v_mov_b32_dpp v15, v14 quad_perm:[0,0,0,0] row_mask:0xf bank_mask:0xf\n"
        "s_movk_i32 s76, 0x2000\n"
        "v_cmp_le_u32 vcc, s76, v15\n"
        "s_mov_b64 exec, vcc\n"
        "v_or_b32 v16, s68, v16\n"
        "v_or_b32 v17, s69, v17\n"
        "v_or_b32 v18, s70, v18\n"
        "v_or_b32 v19, s71, v19\n"
        "buffer_store_dwordx4 v[16:19], v11, s[44:47], s75 offen sc1\n"
        "s_mov_b64 exec, -1\n"
        "s_add_u32 s75, s75, 4096\n"
        "ds_read_b32 v14, v10 offset:18432\n"
        "ds_read_b128 v[16:19], v10 offset:18496\n"
        "s_waitcnt lgkmcnt(0)\n"
        "v_mov_b32_dpp v15, v14 quad_perm:[0,0,0,0] row_mask:0xf bank_mask:0xf\n"
        "s_movk_i32 s76, 0x2000\n"
        "v_cmp_le_u32 vcc, s76, v15\n"
        "s_mov_b64 exec, vcc\n"
        "v_or_b32 v16, s68, v16\n"
        "v_or_b32 v17, s69, v17\n"
        "v_or_b32 v18, s70, v18\n"
        "v_or_b32 v19, s71, v19\n"
        "buffer_store_dwordx4 v[16:19], v11, s[44:47], s75 offen sc1\n"
        "s_mov_b64 exec, -1\n"
        "s_add_u32 s75, s75, 4096\n"
        "ds_read_b32 v14, v10 offset:27648\n"
        "ds_read_b128 v[16:19], v10 offset:27712\n"
        "s_waitcnt lgkmcnt(0)\n"
        "v_mov_b32_dpp v15, v14 quad_perm:[0,0,0,0] row_mask:0xf bank_mask:0xf\n"
        "s_movk_i32 s76, 0x2000\n"
        "v_cmp_le_u32 vcc, s76, v15\n"
        "s_mov_b64 exec, vcc\n"
        "v_or_b32 v16, s68, v16\n"
        "v_or_b32 v17, s69, v17\n"
        "v_or_b32 v18, s70, v18\n"
        "v_or_b32 v19, s71, v19\n"
        "buffer_store_dwordx4 v[16:19], v11, s[44:47], s75 offen sc1\n"
        "s_mov_b64 exec, -1\n"
        "s_add_u32 s75, s75, 4096\n"
        "s_branch .Lpub_done\n"
        ".Lwf:\n"
        "s_sleep 6\n"
        "s_mov_b32 exec_hi, 15\n"
        "buffer_load_dwordx4 v[172:175], v42, s[44:47], 0 offen sc1\n"
        "s_mov_b64 exec, -1\n"
        "s_memrealtime s[80:81]\n"
        "s_waitcnt vmcnt(0) lgkmcnt(0)\n"
        "v_cmp_ne_u32 s[74:75], s66, v172\n"
        "v_cmp_ne_u32 s[76:77], s66, v174\n"
        "v_cmp_ne_u32 s[78:79], s67, v173\n"
        "v_cmp_ne_u32 vcc, s67, v175\n"
        "s_or_b64 s[74:75], s[74:75], s[76:77]\n"
        "s_or_b64 s[78:79], s[78:79], vcc\n"
        "s_or_b64 s[74:75], s[74:75], s[78:79]\n"
        "s_and_b32 s74, s75, 11\n"
        "s_cbranch_scc0 .Lwf_e\n"
        "s_sub_u32 s73, s80, s36\n"
        "s_cmp_le_u32 s73, 20000\n"
        "s_cbranch_scc1 .Lwf\n"
        "s_movk_i32 s61, 0x1000\n"
        "s_branch .Lwf_e\n"
        ".Lbk_more:\n"
        "buffer_load_dwordx4 v[72:75], v47, s[44:47], s42 offen sc1\n"
        "buffer_load_dwordx4 v[76:79], v47, s[44:47], s43 offen sc1\n"
        "s_waitcnt vmcnt(0)\n"
        "v_xor_b32 v72, s68, v72\n"
        "v_xor_b32 v73, s69, v73\n"
        "v_xor_b32 v74, s70, v74\n"
        "v_xor_b32 v75, s71, v75\n"
        "v_or3_b32 v8, v72, v73, v74\n"
        "v_or_b32 v8, v8, v75\n"
        "v_and_b32 v8, s73, v8\n"
        "v_cmp_ne_u32 s[74:75], 0, v8\n"
        "v_xor_b32 v76, s68, v76\n"
        "v_xor_b32 v77, s69, v77\n"
        "v_xor_b32 v78, s70, v78\n"
        "v_xor_b32 v79, s71, v79\n"
        "v_or3_b32 v9, v76, v77, v78\n"
        "v_or_b32 v9, v9, v79\n"
        "v_and_b32 v9, s73, v9\n"
        "v_cmp_ne_u32 s[76:77], 0, v9\n"
        "s_and_b64 s[74:75], s[74:75], s[36:37]\n"
        "s_and_b64 s[76:77], s[76:77], s[38:39]\n"
        "s_or_b64 s[74:75], s[74:75], s[76:77]\n"
        "s_cbranch_scc0 .Lbk_done\n"
        ".Lbk_again:\n"
        "s_memrealtime s[80:81]\n"
        "s_sleep 6\n"
        "buffer_load_dwordx4 v[64:67], v47, s[44:47], s40 offen sc1\n"
        "buffer_load_dwordx4 v[68:71], v47, s[44:47], s41 offen sc1\n"
        "s_waitcnt vmcnt(0) lgkmcnt(0)\n"
        "v_xor_b32 v64, s68, v64\n"
        "v_xor_b32 v65, s69, v65\n"
        "v_xor_b32 v66, s70, v66\n"
        "v_xor_b32 v67, s71, v67\n"
        "v_or3_b32 v8, v64, v65, v66\n"
        "v_or_b32 v8, v8, v67\n"
        "v_and_b32 v8, s73, v8\n"
        "v_cmp_ne_u32 s[74:75], 0, v8\n"
        "v_xor_b32 v68, s68, v68\n"
        "v_xor_b32 v69, s69, v69\n"
        "v_xor_b32 v70, s70, v70\n"
        "v_xor_b32 v71, s71, v71\n"
        "v_or3_b32 v9, v68, v69, v70\n"
        "v_or_b32 v9, v9, v71\n"
        "v_and_b32 v9, s73, v9\n"
        "v_cmp_ne_u32 s[76:77], 0, v9\n"
        "v_mov_b32_dpp v10, v64 quad_perm:[0,0,0,0] row_mask:0xf bank_mask:0xf\n"
        "v_mov_b32_dpp v11, v68 quad_perm:[0,0,0,0] row_mask:0xf bank_mask:0xf\n"
        "v_cmp_ge_u32 s[32:33], v10, v44\n"
        "v_cmp_le_u32 s[36:37], s82, v10\n"
        "v_cmp_ge_u32 s[34:35], v11, v44\n"
        "v_cmp_le_u32 s[38:39], s82, v11\n"
        "s_or_b64 s[78:79], s[74:75], s[76:77]\n"
        "s_sub_u32 s80, s80, s60\n"
        "s_cmp_le_u32 s80, 40000\n"
        "s_cbranch_scc1 .Lbk\n"
        "ds_add_u32 v2, v3 offset:45492\n"
        "s_mov_b64 s[32:33], 0\n"
        "s_mov_b64 s[34:35], 0\n"
        "s_mov_b64 s[80:81], 0\n"
        "s_branch .Lbk_done\n"
        ".Lfail_l:\n"
        "v_mov_b32 v8, 0x1000\n"
        "ds_add_u32 v2, v8 offset:45452\n"
        "s_endpgm\n"
        ".Lno_tile:\n"
        "s_waitcnt lgkmcnt(0)\n"
        "s_add_u32 s73, s64, 1\n"
        "s_addc_u32 s74, s65, 0\n"
        "s_mul_i32 s76, s73, 0x7f4a7c15\n"
        "s_mul_hi_u32 s77, s73, 0x7f4a7c15\n"
        "s_mul_i32 s75, s73, 0x9e3779b9\n"
        "s_add_u32 s77, s77, s75\n"
        "s_mul_i32 s75, s74, 0x7f4a7c15\n"
        "s_add_u32 s77, s77, s75\n"
        "s_xor_b32 s66, s76, 0xd192ed03\n"
        "s_or_b32 s66, s66, 3\n"
        "s_xor_b32 s67, s77, 0xd1b54a32\n"
        "ds_add_u32 v2, v3 offset:45480\n"
        "s_cmp_lg_u32 s3, 4\n"
        "s_cbranch_scc1 .Lflag_donex\n"
        "s_mov_b32 s74, 0x40000\n"
        ".Lepx:\n"
        "ds_read_b32 v28, v2 offset:45480\n"
        "s_waitcnt lgkmcnt(0)\n"
        "v_readfirstlane_b32 s73, v28\n"
        "s_and_b32 s75, s73, 255\n"
        "s_cmp_ge_u32 s75, 8\n"
        "s_cbranch_scc1 .Lep_donex\n"
        "s_sleep 1\n"
        "s_sub_u32 s74, s74, 1\n"
        "s_cbranch_scc0 .Lepx\n"
        ".Lep_donex:\n"
        "s_xor_b32 s74, s66, 2\n"
        "s_cmp_gt_u32 s73, 255\n"
        "s_cselect_b32 s35, s74, s66\n"
        "s_lshl_b32 s73, s2, 3\n"
        "s_add_u32 s73, s73, 256\n"
        "v_mov_b32 v28, s35\n"
        "v_mov_b32 v29, s67\n"
        "s_mov_b64 exec, 1\n"
        "buffer_store_dwordx2 v[28:29], v2, s[44:47], s73 offen sc1\n"
        "s_mov_b64 exec, -1\n"
        ".Lflag_donex:\n"
        "s_cmp_lg_u32 s3, 4\n"
        "s_cbranch_scc1 .Lend_l\n"
        "v_mov_b32 v5, v4\n"
        "s_movk_i32 s43, 256\n"
        "s_sleep 127\n"
        "s_sleep 60\n"
        ".Lagain:\n"
        "s_memrealtime s[80:81]\n"
        "s_mov_b32 s63, 0\n"
        ".Lagain2:\n"
        "s_sleep 8\n"
        "buffer_load_dwordx4 v[24:27], v5, s[44:47], s43 offen sc1\n"
        "buffer_load_dwordx4 v[28:31], v5, s[44:47], s43 offen offset:1024 sc1\n"
        "s_memrealtime s[82:83]\n"
        "s_waitcnt vmcnt(0) lgkmcnt(0)\n"
        "v_and_b32 v32, v24, v26\n"
        "v_and_b32 v33, v28, v30\n"
        "v_and_b32 v32, v32, v33\n"
        "v_or_b32 v24, 3, v24\n"
        "v_xor_b32 v24, s66, v24\n"
        "v_xor_b32 v25, s67, v25\n"
        "v_or_b32 v26, 3, v26\n"
        "v_xor_b32 v26, s66, v26\n"
        "v_xor_b32 v27, s67, v27\n"
        "v_or_b32 v28, 3, v28\n"
        "v_xor_b32 v28, s66, v28\n"
        "v_xor_b32 v29, s67, v29\n"
        "v_or_b32 v30, 3, v30\n"
        "v_xor_b32 v30, s66, v30\n"
        "v_xor_b32 v31, s67, v31\n"
        "v_or3_b32 v24, v24, v25, v26\n"
        "v_or3_b32 v27, v27, v28, v29\n"
        "v_or3_b32 v24, v24, v30, v31\n"
        "v_and_b32 v33, 1, v32\n"
        "v_or_b32 v24, v24, v27\n"
        "v_and_b32 v32, 2, v32\n"
        "v_cmp_eq_u32 s[74:75], 0, v33\n"
        "v_cmp_eq_u32 s[76:77], 0, v32\n"
        "v_cmp_ne_u32 vcc, 0, v24\n"
        "s_cmp_eq_u64 s[74:75], 0\n"
        "s_cselect_b32 s62, 1, 0\n"
        "s_cmp_eq_u64 s[76:77], 0\n"
        "s_cselect_b32 s73, 2, 0\n"
        "s_or_b32 s62, s62, s73\n"
        "s_cmp_eq_u64 vcc, 0\n"
        "s_cselect_b32 s73, 4, 0\n"
        "s_or_b32 s62, s62, s73\n"
        "s_bitcmp1_b32 s62, 2\n"
        "s_cbranch_scc1 .Lall\n"
        "s_cmp_lg_u32 s63, 0\n"
        "s_cbranch_scc1 .Ltell\n"
        "s_sub_u32 s73, s82, s80\n"
        "s_cmp_le_u32 s73, 20000\n"
        "s_cbranch_scc1 .Lagain2\n"
        "s_add_u32 s75, s2, 1\n"
        "s_cmp_lg_u32 s75, s56\n"
        "s_cbranch_scc1 .Ltell\n"
        "s_xor_b32 s73, s35, 1\n"
        "s_lshl_b32 s74, s2, 3\n"
        "s_add_u32 s74, s74, 256\n"
        "v_mov_b32 v24, s73\n"
        "v_mov_b32 v25, s67\n"
        "s_mov_b64 exec, 1\n"
        "buffer_store_dwordx2 v[24:25], v2, s[44:47], s74 offen sc1\n"
        "s_mov_b64 exec, -1\n"
        "s_mov_b32 s63, 1\n"
        "s_waitcnt vmcnt(0)\n"
        "s_branch .Lagain2\n"
        ".Lcode_end:\n"
        ".Lqm_again:\n"
        "s_sleep 6\n"
        "s_memrealtime s[80:81]\n"
        "s_waitcnt lgkmcnt(0)\n"
        "s_sub_u32 s73, s80, s36\n"
        "s_cmp_gt_u32 s73, 20000\n"
        "s_cselect_b32 s61, 0x1000, s61\n"
        "s_cbranch_scc1 .Lqm_done\n"
        "s_setprio 3\n"
        "s_mov_b32 exec_hi, 15\n"
        "buffer_load_dwordx4 v[172:175], v42, s[44:47], 0 offen sc1\n"
        "s_mov_b64 exec, -1\n"
        "buffer_load_dwordx4 v[224:227], v4, s[44:47], s8 offen offset:0 sc1\n"
        "buffer_load_dwordx4 v[228:231], v4, s[44:47], s10 offen offset:0 sc1\n"
        "buffer_load_dwordx4 v[232:235], v4, s[44:47], s12 offen offset:0 sc1\n"
        "buffer_load_dwordx4 v[236:239], v4, s[44:47], s14 offen offset:0 sc1\n"
        "buffer_load_dwordx4 v[240:243], v4, s[44:47], s8 offen offset:1024 sc1\n"
        "buffer_load_dwordx4 v[244:247], v4, s[44:47], s10 offen offset:1024 sc1\n"
        "buffer_load_dwordx4 v[248:251], v4, s[44:47], s12 offen offset:1024 sc1\n"
        "buffer_load_dwordx4 v[252:255], v4, s[44:47], s14 offen offset:1024 sc1\n"
        "buffer_load_dwordx4 v[156:159], v4, s[44:47], s8 offen offset:2048 sc1\n"
        "buffer_load_dwordx4 v[160:163], v4, s[44:47], s10 offen offset:2048 sc1\n"
        "buffer_load_dwordx4 v[164:167], v4, s[44:47], s12 offen offset:2048 sc1\n"
        "buffer_load_dwordx4 v[168:171], v4, s[44:47], s14 offen offset:2048 sc1\n"
        "buffer_load_dwordx4 v[140:143], v4, s[44:47], s8 offen offset:3072 sc1\n"
        "buffer_load_dwordx4 v[144:147], v4, s[44:47], s10 offen offset:3072 sc1\n"
        "buffer_load_dwordx4 v[148:151], v4, s[44:47], s12 offen offset:3072 sc1\n"
        "buffer_load_dwordx4 v[152:155], v4, s[44:47], s14 offen offset:3072 sc1\n"
        "ds_read_b128 v[8:11], v36 offset:0\n"
        "ds_read_b128 v[12:15], v36 offset:8192\n"
        "ds_read_b128 v[16:19], v36 offset:16384\n"
        "s_waitcnt vmcnt(12)\n"
        "s_waitcnt lgkmcnt(2)\n"
        "v_mfma_f32_16x16x32_bf16 v[176:179], v[224:227], v[8:11], 0\n"
        "v_add_u32 v43, v224, v225\n"
        "v_mfma_f32_16x16x32_bf16 v[180:183], v[228:231], v[8:11], 0\n"
        "v_add3_u32 v43, v43, v226, v227\n"
        "v_mfma_f32_16x16x32_bf16 v[184:187], v[232:235], v[8:11], 0\n"
        "v_add3_u32 v43, v43, v228, v229\n"
        "v_mfma_f32_16x16x32_bf16 v[188:191], v[236:239], v[8:11], 0\n"
        "v_add3_u32 v43, v43, v230, v231\n"
        "ds_read_b128 v[8:11], v37 offset:0\n"
        "s_waitcnt lgkmcnt(2)\n"
        "v_mfma_f32_16x16x32_bf16 v[192:195], v[224:227], v[12:15], 0\n"
        "v_add3_u32 v43, v43, v232, v233\n"
        "v_mfma_f32_16x16x32_bf16 v[196:199], v[228:231], v[12:15], 0\n"
        "v_add3_u32 v43, v43, v234, v235\n"
        "v_mfma_f32_16x16x32_bf16 v[200:203], v[232:235], v[12:15], 0\n"
        "v_add3_u32 v43, v43, v236, v237\n"
        "v_mfma_f32_16x16x32_bf16 v[204:207], v[236:239], v[12:15], 0\n"
        "v_add3_u32 v43, v43, v238, v239\n"
        "ds_read_b128 v[12:15], v37 offset:8192\n"
        "s_waitcnt lgkmcnt(2)\n"
        "v_mfma_f32_16x16x32_bf16 v[208:211], v[224:227], v[16:19], 0\n"
        "v_mfma_f32_16x16x32_bf16 v[212:215], v[228:231], v[16:19], 0\n"
        "v_mfma_f32_16x16x32_bf16 v[216:219], v[232:235], v[16:19], 0\n"
        "v_mfma_f32_16x16x32_bf16 v[220:223], v[236:239], v[16:19], 0\n"
        "ds_read_b128 v[16:19], v37 offset:16384\n"
        "buffer_load_dwordx4 v[224:227], v4, s[44:47], s9 offen offset:0 sc1\n"
        "buffer_load_dwordx4 v[228:231], v4, s[44:47], s11 offen offset:0 sc1\n"
        "buffer_load_dwordx4 v[232:235], v4, s[44:47], s13 offen offset:0 sc1\n"
        "buffer_load_dwordx4 v[236:239], v4, s[44:47], s15 offen offset:0 sc1\n"
        "s_waitcnt vmcnt(12)\n"
        "s_waitcnt lgkmcnt(2)\n"
        "v_mfma_f32_16x16x32_bf16 v[176:179], v[240:243], v[8:11], v[176:179]\n"
        "v_add3_u32 v43, v43, v240, v241\n"
        "v_mfma_f32_16x16x32_bf16 v[180:183], v[244:247], v[8:11], v[180:183]\n"
        "v_add3_u32 v43, v43, v242, v243\n"
        "v_mfma_f32_16x16x32_bf16 v[184:187], v[248:251], v[8:11], v[184:187]\n"
        "v_add3_u32 v43, v43, v244, v245\n"
        "v_mfma_f32_16x16x32_bf16 v[188:191], v[252:255], v[8:11], v[188:191]\n"
        "v_add3_u32 v43, v43, v246, v247\n"
        "ds_read_b128 v[8:11], v38 offset:0\n"
        "s_waitcnt lgkmcnt(2)\n"
        "v_mfma_f32_16x16x32_bf16 v[192:195], v[240:243], v[12:15], v[192:195]\n"
        "v_add3_u32 v43, v43, v248, v249\n"
        "v_mfma_f32_16x16x32_bf16 v[196:199], v[244:247], v[12:15], v[196:199]\n"
        "v_add3_u32 v43, v43, v250, v251\n"
        "v_mfma_f32_16x16x32_bf16 v[200:203], v[248:251], v[12:15], v[200:203]\n"
        "v_add3_u32 v43, v43, v252, v253\n"
        "v_mfma_f32_16x16x32_bf16 v[204:207], v[252:255], v[12:15], v[204:207]\n"
        "v_add3_u32 v43, v43, v254, v255\n"
        "ds_read_b128 v[12:15], v38 offset:8192\n"
        "s_waitcnt lgkmcnt(2)\n"
        "v_mfma_f32_16x16x32_bf16 v[208:211], v[240:243], v[16:19], v[208:211]\n"
        "v_mfma_f32_16x16x32_bf16 v[212:215], v[244:247], v[16:19], v[212:215]\n"
        "v_mfma_f32_16x16x32_bf16 v[216:219], v[248:251], v[16:19], v[216:219]\n"
        "v_mfma_f32_16x16x32_bf16 v[220:223], v[252:255], v[16:19], v[220:223]\n"
        "ds_read_b128 v[16:19], v38 offset:16384\n"
        "buffer_load_dwordx4 v[240:243], v4, s[44:47], s9 offen offset:1024 sc1\n"
        "buffer_load_dwordx4 v[244:247], v4, s[44:47], s11 offen offset:1024 sc1\n"
        "buffer_load_dwordx4 v[248:251], v4, s[44:47], s13 offen offset:1024 sc1\n"
        "buffer_load_dwordx4 v[252:255], v4, s[44:47], s15 offen offset:1024 sc1\n"
        "s_waitcnt vmcnt(12)\n"
        "s_waitcnt lgkmcnt(2)\n"
        "v_mfma_f32_16x16x32_bf16 v[176:179], v[156:159], v[8:11], v[176:179]\n"
        "v_add3_u32 v43, v43, v156, v157\n"
        "v_mfma_f32_16x16x32_bf16 v[180:183], v[160:163], v[8:11], v[180:183]\n"
        "v_add3_u32 v43, v43, v158, v159\n"
        "v_mfma_f32_16x16x32_bf16 v[184:187], v[164:167], v[8:11], v[184:187]\n"
        "v_add3_u32 v43, v43, v160, v161\n"
        "v_mfma_f32_16x16x32_bf16 v[188:191], v[168:171], v[8:11], v[188:191]\n"
        "v_add3_u32 v43, v43, v162, v163\n"
        "ds_read_b128 v[8:11], v39 offset:0\n"
        "s_waitcnt lgkmcnt(2)\n"
        "v_mfma_f32_16x16x32_bf16 v[192:195], v[156:159], v[12:15], v[192:195]\n"
        "v_add3_u32 v43, v43, v164, v165\n"
        "v_mfma_f32_16x16x32_bf16 v[196:199], v[160:163], v[12:15], v[196:199]\n"
        "v_add3_u32 v43, v43, v166, v167\n"
        "v_mfma_f32_16x16x32_bf16 v[200:203], v[164:167], v[12:15], v[200:203]\n"
        "v_add3_u32 v43, v43, v168, v169\n"
        "v_mfma_f32_16x16x32_bf16 v[204:207], v[168:171], v[12:15], v[204:207]\n"
        "v_add3_u32 v43, v43, v170, v171\n"
        "ds_read_b128 v[12:15], v39 offset:8192\n"
        "s_waitcnt lgkmcnt(2)\n"
        "v_mfma_f32_16x16x32_bf16 v[208:211], v[156:159], v[16:19], v[208:211]\n"
        "v_mfma_f32_16x16x32_bf16 v[212:215], v[160:163], v[16:19], v[212:215]\n"
        "v_mfma_f32_16x16x32_bf16 v[216:219], v[164:167], v[16:19], v[216:219]\n"
        "v_mfma_f32_16x16x32_bf16 v[220:223], v[168:171], v[16:19], v[220:223]\n"
        "ds_read_b128 v[16:19], v39 offset:16384\n"
        "buffer_load_dwordx4 v[156:159], v4, s[44:47], s9 offen offset:2048 sc1\n"
        "buffer_load_dwordx4 v[160:163], v4, s[44:47], s11 offen offset:2048 sc1\n"
        "buffer_load_dwordx4 v[164:167], v4, s[44:47], s13 offen offset:2048 sc1\n"
        "buffer_load_dwordx4 v[168:171], v4, s[44:47], s15 offen offset:2048 sc1\n"
        "s_waitcnt vmcnt(12)\n"
        "s_waitcnt lgkmcnt(2)\n"
        "v_mfma_f32_16x16x32_bf16 v[176:179], v[140:143], v[8:11], v[176:179]\n"
        "v_add3_u32 v43, v43, v140, v141\n"
        "v_mfma_f32_16x16x32_bf16 v[180:183], v[144:147], v[8:11], v[180:183]\n"
        "v_add3_u32 v43, v43, v142, v143\n"
        "v_mfma_f32_16x16x32_bf16 v[184:187], v[148:151], v[8:11], v[184:187]\n"
        "v_add3_u32 v43, v43, v144, v145\n"
        "v_mfma_f32_16x16x32_bf16 v[188:191], v[152:155], v[8:11], v[188:191]\n"
        "v_add3_u32 v43, v43, v146, v147\n"
        "ds_read_b128 v[8:11], v36 offset:256\n"
        "s_waitcnt lgkmcnt(2)\n"
        "v_mfma_f32_16x16x32_bf16 v[192:195], v[140:143], v[12:15], v[192:195]\n"
        "v_add3_u32 v43, v43, v148, v149\n"
        "v_mfma_f32_16x16x32_bf16 v[196:199], v[144:147], v[12:15], v[196:199]\n"
        "v_add3_u32 v43, v43, v150, v151\n"
        "v_mfma_f32_16x16x32_bf16 v[200:203], v[148:151], v[12:15], v[200:203]\n"
        "v_add3_u32 v43, v43, v152, v153\n"
        "v_mfma_f32_16x16x32_bf16 v[204:207], v[152:155], v[12:15], v[204:207]\n"
        "v_add3_u32 v43, v43, v154, v155\n"
        "ds_read_b128 v[12:15], v36 offset:8448\n"
        "s_waitcnt lgkmcnt(2)\n"
        "v_mfma_f32_16x16x32_bf16 v[208:211], v[140:143], v[16:19], v[208:211]\n"
        "v_mfma_f32_16x16x32_bf16 v[212:215], v[144:147], v[16:19], v[212:215]\n"
        "v_mfma_f32_16x16x32_bf16 v[216:219], v[148:151], v[16:19], v[216:219]\n"
        "v_mfma_f32_16x16x32_bf16 v[220:223], v[152:155], v[16:19], v[220:223]\n"
        "ds_read_b128 v[16:19], v36 offset:16640\n"
        "buffer_load_dwordx4 v[140:143], v4, s[44:47], s9 offen offset:3072 sc1\n"
        "buffer_load_dwordx4 v[144:147], v4, s[44:47], s11 offen offset:3072 sc1\n"
        "buffer_load_dwordx4 v[148:151], v4, s[44:47], s13 offen offset:3072 sc1\n"
        "buffer_load_dwordx4 v[152:155], v4, s[44:47], s15 offen offset:3072 sc1\n"
        "s_waitcnt vmcnt(12)\n"
        "s_waitcnt lgkmcnt(2)\n"
        "v_mfma_f32_16x16x32_bf16 v[176:179], v[224:227], v[8:11], v[176:179]\n"
        "v_add3_u32 v43, v43, v224, v225\n"
        "v_mfma_f32_16x16x32_bf16 v[180:183], v[228:231], v[8:11], v[180:183]\n"
        "v_add3_u32 v43, v43, v226, v227\n"
        "v_mfma_f32_16x16x32_bf16 v[184:187], v[232:235], v[8:11], v[184:187]\n"
        "v_add3_u32 v43, v43, v228, v229\n"
        "v_mfma_f32_16x16x32_bf16 v[188:191], v[236:239], v[8:11], v[188:191]\n"
        "v_add3_u32 v43, v43, v230, v231\n"
        "ds_read_b128 v[8:11], v37 offset:256\n"
        "s_waitcnt lgkmcnt(2)\n"
        "v_mfma_f32_16x16x32_bf16 v[192:195], v[224:227], v[12:15], v[192:195]\n"
        "v_add3_u32 v43, v43, v232, v233\n"
        "v_mfma_f32_16x16x32_bf16 v[196:199], v[228:231], v[12:15], v[196:199]\n"
        "v_add3_u32 v43, v43, v234, v235\n"
        "v_mfma_f32_16x16x32_bf16 v[200:203], v[232:235], v[12:15], v[200:203]\n"
        "v_add3_u32 v43, v43, v236, v237\n"
        "v_mfma_f32_16x16x32_bf16 v[204:207], v[236:239], v[12:15], v[204:207]\n"
        "v_add3_u32 v43, v43, v238, v239\n"
        "ds_read_b128 v[12:15], v37 offset:8448\n"
        "s_waitcnt lgkmcnt(2)\n"
        "v_mfma_f32_16x16x32_bf16 v[208:211], v[224:227], v[16:19], v[208:211]\n"
        "v_mfma_f32_16x16x32_bf16 v[212:215], v[228:231], v[16:19], v[212:215]\n"
        "v_mfma_f32_16x16x32_bf16 v[216:219], v[232:235], v[16:19], v[216:219]\n"
        "v_mfma_f32_16x16x32_bf16 v[220:223], v[236:239], v[16:19], v[220:223]\n"
        "ds_read_b128 v[16:19], v37 offset:16640\n"
        "s_waitcnt vmcnt(8)\n"
        "s_waitcnt lgkmcnt(2)\n"
        "v_mfma_f32_16x16x32_bf16 v[176:179], v[240:243], v[8:11], v[176:179]\n"
        "v_add3_u32 v43, v43, v240, v241\n"
        "v_mfma_f32_16x16x32_bf16 v[180:183], v[244:247], v[8:11], v[180:183]\n"
        "v_add3_u32 v43, v43, v242, v243\n"
        "v_mfma_f32_16x16x32_bf16 v[184:187], v[248:251], v[8:11], v[184:187]\n"
        "v_add3_u32 v43, v43, v244, v245\n"
        "v_mfma_f32_16x16x32_bf16 v[188:191], v[252:255], v[8:11], v[188:191]\n"
        "v_add3_u32 v43, v43, v246, v247\n"
        "ds_read_b128 v[8:11], v38 offset:256\n"
        "s_waitcnt lgkmcnt(2)\n"
        "v_mfma_f32_16x16x32_bf16 v[192:195], v[240:243], v[12:15], v[192:195]\n"
        "v_add3_u32 v43, v43, v248, v249\n"
        "v_mfma_f32_16x16x32_bf16 v[196:199], v[244:247], v[12:15], v[196:199]\n"
        "v_add3_u32 v43, v43, v250, v251\n"
        "v_mfma_f32_16x16x32_bf16 v[200:203], v[248:251], v[12:15], v[200:203]\n"
        "v_add3_u32 v43, v43, v252, v253\n"
        "v_mfma_f32_16x16x32_bf16 v[204:207], v[252:255], v[12:15], v[204:207]\n"
        "v_add3_u32 v43, v43, v254, v255\n"
        "ds_read_b128 v[12:15], v38 offset:8448\n"
        "s_waitcnt lgkmcnt(2)\n"
        "v_mfma_f32_16x16x32_bf16 v[208:211], v[240:243], v[16:19], v[208:211]\n"
        "v_mfma_f32_16x16x32_bf16 v[212:215], v[244:247], v[16:19], v[212:215]\n"
        "v_mfma_f32_16x16x32_bf16 v[216:219], v[248:251], v[16:19], v[216:219]\n"
        "v_mfma_f32_16x16x32_bf16 v[220:223], v[252:255], v[16:19], v[220:223]\n"
        "ds_read_b128 v[16:19], v38 offset:16640\n"
        "s_waitcnt vmcnt(4)\n"
        "s_waitcnt lgkmcnt(2)\n"
        "v_mfma_f32_16x16x32_bf16 v[176:179], v[156:159], v[8:11], v[176:179]\n"
        "v_add3_u32 v43, v43, v156, v157\n"
        "v_mfma_f32_16x16x32_bf16 v[180:183], v[160:163], v[8:11], v[180:183]\n"
        "v_add3_u32 v43, v43, v158, v159\n"
        "v_mfma_f32_16x16x32_bf16 v[184:187], v[164:167], v[8:11], v[184:187]\n"
        "v_add3_u32 v43, v43, v160, v161\n"
        "v_mfma_f32_16x16x32_bf16 v[188:191], v[168:171], v[8:11], v[188:191]\n"
        "v_add3_u32 v43, v43, v162, v163\n"
        "ds_read_b128 v[8:11], v39 offset:256\n"
        "s_waitcnt lgkmcnt(2)\n"
        "v_mfma_f32_16x16x32_bf16 v[192:195], v[156:159], v[12:15], v[192:195]\n"
        "v_add3_u32 v43, v43, v164, v165\n"
        "v_mfma_f32_16x16x32_bf16 v[196:199], v[160:163], v[12:15], v[196:199]\n"
        "v_add3_u32 v43, v43, v166, v167\n"
        "v_mfma_f32_16x16x32_bf16 v[200:203], v[164:167], v[12:15], v[200:203]\n"
        "v_add3_u32 v43, v43, v168, v169\n"
        "v_mfma_f32_16x16x32_bf16 v[204:207], v[168:171], v[12:15], v[204:207]\n"
        "v_add3_u32 v43, v43, v170, v171\n"
        "ds_read_b128 v[12:15], v39 offset:8448\n"
        "s_waitcnt lgkmcnt(2)\n"
        "v_mfma_f32_16x16x32_bf16 v[208:211], v[156:159], v[16:19], v[208:211]\n"
        "v_mfma_f32_16x16x32_bf16 v[212:215], v[160:163], v[16:19], v[212:215]\n"
        "v_mfma_f32_16x16x32_bf16 v[216:219], v[164:167], v[16:19], v[216:219]\n"
        "v_mfma_f32_16x16x32_bf16 v[220:223], v[168:171], v[16:19], v[220:223]\n"
        "ds_read_b128 v[16:19], v39 offset:16640\n"
        "s_waitcnt vmcnt(0)\n"
        "s_waitcnt lgkmcnt(2)\n"
        "v_mfma_f32_16x16x32_bf16 v[176:179], v[140:143], v[8:11], v[176:179]\n"
        "v_add3_u32 v43, v43, v140, v141\n"
        "v_add3_u32 v43, v43, v142, v143\n"
        "v_mfma_f32_16x16x32_bf16 v[180:183], v[144:147], v[8:11], v[180:183]\n"
        "v_add3_u32 v43, v43, v144, v145\n"
        "v_add3_u32 v43, v43, v146, v147\n"
        "v_mfma_f32_16x16x32_bf16 v[184:187], v[148:151], v[8:11], v[184:187]\n"
        "v_add3_u32 v43, v43, v148, v149\n"
        "v_add3_u32 v43, v43, v150, v151\n"
        "v_mfma_f32_16x16x32_bf16 v[188:191], v[152:155], v[8:11], v[188:191]\n"
        "v_add3_u32 v43, v43, v152, v153\n"
        "v_add3_u32 v43, v43, v154, v155\n"
        "s_waitcnt lgkmcnt(1)\n"
        "v_mfma_f32_16x16x32_bf16 v[192:195], v[140:143], v[12:15], v[192:195]\n"
        "s_mov_b32 exec_hi, 0\n"
        "v_sub_u32 v43, v43, v173\n"
        "v_cmp_ne_u32 s[74:75], s66, v172\n"
        "v_sub_u32 v43, v43, v175\n"
        "v_cmp_ne_u32 s[76:77], s66, v174\n"
        "s_mov_b32 exec_hi, -1\n"
        "v_add_u32_dpp v43, v43, v43 row_shr:1 row_mask:0xf bank_mask:0xf bound_ctrl:0\n"
        "v_mfma_f32_16x16x32_bf16 v[196:199], v[144:147], v[12:15], v[196:199]\n"
        "s_nop 0\n"
        "v_add_u32_dpp v43, v43, v43 row_shr:2 row_mask:0xf bank_mask:0xf bound_ctrl:0\n"
        "s_nop 1\n"
        "v_add_u32_dpp v43, v43, v43 row_shr:4 row_mask:0xf bank_mask:0xf bound_ctrl:0\n"
        "v_mfma_f32_16x16x32_bf16 v[200:203], v[148:151], v[12:15], v[200:203]\n"
        "s_nop 0\n"
        "v_add_u32_dpp v43, v43, v43 row_shr:8 row_mask:0xf bank_mask:0xf bound_ctrl:0\n"
        "s_nop 1\n"
        "v_add_u32_dpp v43, v43, v43 row_bcast:15 row_mask:0xa bank_mask:0xf\n"
        "v_mfma_f32_16x16x32_bf16 v[204:207], v[152:155], v[12:15], v[204:207]\n"
        "s_nop 0\n"
        "v_add_u32_dpp v43, v43, v43 row_bcast:31 row_mask:0xc bank_mask:0xf\n"
        "s_lshl_b32 s75, s67, 6\n"
        "s_or_b32 s74, s74, s76\n"
        "s_waitcnt lgkmcnt(0)\n"
        "v_mfma_f32_16x16x32_bf16 v[208:211], v[140:143], v[16:19], v[208:211]\n"
        "v_readlane_b32 s73, v43, 63\n"
        "s_add_u32 s73, s73, s75\n"
        "s_or_b32 s73, s73, s74\n"
        "v_mfma_f32_16x16x32_bf16 v[212:215], v[144:147], v[16:19], v[212:215]\n"
        "v_mfma_f32_16x16x32_bf16 v[216:219], v[148:151], v[16:19], v[216:219]\n"
        "v_mfma_f32_16x16x32_bf16 v[220:223], v[152:155], v[16:19], v[220:223]\n"
        "s_setprio 1\n"
        "s_cbranch_scc1 .Lqm_again\n"
        "s_branch .Lqm_done\n"
        ".Lexit:\n"
        "s_mov_b64 exec, -1\n"
        "s_setprio 0\n"
        "s_waitcnt vmcnt(0) lgkmcnt(0)\n"
        : "={s3}"(wave), "+{v0}"(tx), "+{s[0:1]}"(kp), "+{s2}"(tile)
        :
        :
          "v1", "v2", "v3", "v4", "v5", "v6", "v7", "v8", "v9", "v10", "v11", "v12", "v13", "v14", "v15", "v16",
          "v17", "v18", "v19", "v20", "v21", "v22", "v23", "v24", "v25", "v26", "v27", "v28", "v29", "v30", "v31", "v32",
          "v33", "v34", "v35", "v36", "v37", "v38", "v39", "v40", "v41", "v42", "v43", "v44", "v45", "v46", "v47", "v48",
          "v49", "v50", "v51", "v52", "v53", "v54", "v55", "v56", "v57", "v58", "v59", "v60", "v61", "v62", "v63", "v64",
          "v65", "v66", "v67", "v68", "v69", "v70", "v71", "v72", "v73", "v74", "v75", "v76", "v77", "v78", "v79", "v80",
          "v81", "v82", "v83", "v84", "v85", "v86", "v87", "v88", "v89", "v90", "v91", "v92", "v93", "v94", "v95", "v96",
          "v97", "v98", "v99", "v100", "v101", "v102", "v103", "v104", "v105", "v106", "v107", "v108", "v109", "v110", "v111", "v112",
          "v113", "v114", "v115", "v116", "v117", "v118", "v119", "v120", "v121", "v122", "v123", "v124", "v125", "v126", "v127", "v128",
          "v129", "v130", "v131", "v132", "v133", "v134", "v135", "v136", "v137", "v138", "v139", "v140", "v141", "v142", "v143", "v144",
          "v145", "v146", "v147", "v148", "v149", "v150", "v151", "v152", "v153", "v154", "v155", "v156", "v157", "v158", "v159", "v160",
          "v161", "v162", "v163", "v164", "v165", "v166", "v167", "v168", "v169", "v170", "v171", "v172", "v173", "v174", "v175", "v176",
          "v177", "v178", "v179", "v180", "v181", "v182", "v183", "v184", "v185", "v186", "v187", "v188", "v189", "v190", "v191", "v192",
          "v193", "v194", "v195", "v196", "v197", "v198", "v199", "v200", "v201", "v202", "v203", "v204", "v205", "v206", "v207", "v208",
          "v209", "v210", "v211", "v212", "v213", "v214", "v215", "v216", "v217", "v218", "v219", "v220", "v221", "v222", "v223", "v224",
          "v225", "v226", "v227", "v228", "v229", "v230", "v231", "v232", "v233", "v234", "v235", "v236", "v237", "v238", "v239", "v240",
          "v241", "v242", "v243", "v244", "v245", "v246", "v247", "v248", "v249", "v250", "v251", "v252", "v253", "v254", "v255", "s4",
          "s5", "s6", "s7", "s8", "s9", "s10", "s11", "s12", "s13", "s14", "s15", "s16", "s17", "s18", "s19", "s20",
          "s21", "s22", "s23", "s24", "s25", "s26", "s27", "s28", "s29", "s30", "s31", "s33", "s34", "s35", "s36", "s37",
          "s38", "s39", "s40", "s41", "s42", "s43", "s44", "s45", "s46", "s47", "s48", "s49", "s50", "s51", "s52", "s53",
          "s54", "s55", "s56", "s57", "s58", "s59", "s60", "s61", "s62", "s63", "s64", "s65", "s66", "s67", "s68", "s69",
          "s70", "s71", "s72", "s73", "s74", "s75", "s76", "s77", "s78", "s79", "s80", "s81", "s82", "s83", "s84", "s85",
          "s86", "s87", "s88", "s89", "s90", "s91", "s92", "s93", "s94", "s95", "s96", "s97", "s98", "s99", "vcc", "scc",
          "memory"
    );
    const uint16_t *x = kp->x, *dis = kp->dis, *wl = kp->wl, *wv = kp->wv, *wq = kp->wq, *wk = kp->wk;
    const int *src = kp->src, *dst = kp->dst, *esrc = kp->esrc, *edst = kp->edst;
    uint16_t* out = kp->out;
    const int N = kp->N, E = kp->E, Ee = kp->Ee;
    const int lane = __builtin_amdgcn_mbcnt_hi(-1, __builtin_amdgcn_mbcnt_lo(-1, 0));
    wave -= 4;
    const int tid = wave * 64 + lane;
    uint16_t* xs = reinterpret_cast<uint16_t*>(smem + 46080);
    uint16_t* qs = xs + TN * D;
    uint16_t* ag = qs + TN * D;
    uint32_t* lst = reinterpret_cast<uint32_t*>(smem + 24064);
    uint32_t* lc = lst + 1024;
    __syncthreads();
    for (int i = tid; i < TN * D; i += 256) xs[i] = tile * TN + i / D < N ? x[(size_t)tile * TN * D + i] : (uint16_t)0;
    __syncthreads();
    const int col = tid;
#pragma unroll 1
    for (int r = 0; r < TN; r += 4) {
        f32x4 a = {0.f, 0.f, 0.f, 0.f};
#pragma unroll 2
        for (int k = 0; k < D; ++k) {
            const float w = bf2f(wq[k * D + col]);
#pragma unroll
            for (int i = 0; i < 4; ++i) a[i] += w * bf2f(xs[(r + i) * D + k]);
        }
#pragma unroll
        for (int i = 0; i < 4; ++i) qs[(r + i) * D + col] = f2bf(a[i]);
    }
    __syncthreads();
#pragma unroll 1
    for (int r = 0; r < TN; r += 4) {
        f32x4 a = {0.f, 0.f, 0.f, 0.f};
#pragma unroll 2
        for (int k = 0; k < D; ++k) {
            const float w = bf2f(wk[col * D + k]);
#pragma unroll
            for (int i = 0; i < 4; ++i) a[i] += w * bf2f(qs[(r + i) * D + k]);
        }
#pragma unroll
        for (int i = 0; i < 4; ++i) xs[(r + i) * D + col] = f2bf(a[i] * 0.0625f);
    }
#pragma unroll 1
    for (int pass = 0; pass < 2; ++pass) {
        f32x4 ac[10];
        float den[10];
#pragma unroll
        for (int k = 0; k < 10; ++k) {
            ac[k] = f32x4{0.f, 0.f, 0.f, 0.f};
            den[k] = 0.f;
        }
        const int* S = pass ? esrc : src;
        const int* Dd = pass ? edst : dst;
        const int ne = pass ? Ee : E;
        for (int e0 = 0; e0 < ne; e0 += 256) {
            const int e = e0 + tid;
            const uint32_t nd = e < ne ? (uint32_t)(Dd[e] - tile * TN) : ~0u;
            const uint64_t m = __ballot(nd < (uint32_t)TN);
            __syncthreads();
            if (lane == 0) lc[wave] = (uint32_t)__popcll(m);
            __syncthreads();
            uint32_t p = (uint32_t)__popcll(m & ((1ull << lane) - 1ull)), cn = 0;
            for (int w = 0; w < 4; ++w) {
                p += w < wave ? lc[w] : 0u;
                cn += lc[w];
            }
            if (nd < (uint32_t)TN) {
                lst[2 * p] = (uint32_t)S[e];
                lst[2 * p + 1] = nd;
            }
            __syncthreads();
            for (uint32_t i = 0; i < cn; ++i) {
                const int k = __builtin_amdgcn_readfirstlane((int)lst[2 * i + 1]) - 10 * wave;
                if (k < 0 || k >= 10) continue;
                const uint32_t s = lst[2 * i];
                const f32x4 rv = wide(*reinterpret_cast<const u32x2*>(x + (size_t)s * D + lane * 4));
                float wgt;
                if (!pass) {
                    wgt = bf2f(dis[s]);
                } else {
                    const f32x4 t = rv * wide(*reinterpret_cast<const u32x2*>(xs + (10 * wave + k) * D + lane * 4));
                    float lg = (t[0] + t[1]) + (t[2] + t[3]);
                    for (int o = 1; o < 64; o <<= 1) lg += __shfl_xor(lg, o);
                    wgt = __expf(lg);
                }
#pragma unroll
                for (int kk = 0; kk < 10; ++kk) {
                    if (kk == k) {
                        ac[kk] += rv * wgt;
                        den[kk] += wgt;
                    }
                }
            }
        }
#pragma unroll
        for (int k = 0; k < 10; ++k) {
            const f32x4 o = ac[k] * (pass ? 1.f / (den[k] + 1e-9f) : bf2f(dis[min(tile * TN + 10 * wave + k, N - 1)]));
            *reinterpret_cast<u32x2*>(ag + (pass * TN + 10 * wave + k) * D + lane * 4) = u32x2{pack2(o[0], o[1]), pack2(o[2], o[3])};
        }
    }
    __syncthreads();
#pragma unroll 1
    for (int r = 0; r < TN; r += 4) {
        f32x4 a = {0.f, 0.f, 0.f, 0.f};
#pragma unroll 2
        for (int k = 0; k < 2 * D; ++k) {
            const float w = bf2f(k < D ? wl[k * D + col] : wv[(k - D) * D + col]);
#pragma unroll
            for (int i = 0; i < 4; ++i) a[i] += w * bf2f(ag[((k >> 8) * TN + r + i) * D + (k & 255)]);
        }
#pragma unroll
        for (int i = 0; i < 4; ++i)
            if (tile * TN + r + i < N) out[(size_t)(tile * TN + r + i) * D + col] = f2bf(a[i]);
    }
}

extern "C" void kernel_launch(void* const* d_in, const int* in_sizes, int n_in,
                              void* d_out, int out_size, void* d_ws, size_t ws_size,
                              hipStream_t stream) {
    const int N = in_sizes[0] / D;
    const int E = in_sizes[1];
    const int Ee = in_sizes[3];
    const int T = (N + TN - 1) / TN;
    if (T == 0) return;
    const size_t need = WS_BK + 4194304;
    int fast = T >= 192 && T <= 250 && (N & 1) == 0 && (E & 3) == 0 && E <= 262144 && Ee < (1 << 26) && ws_size >= need;
    if (fast && Ee == 4 * N) fast = 3;
    const uint32_t F = 0x00020000u, W = 2 * D * D;
    const uint32_t on = fast ? ~0u : 0u;
    void* stamps = d_out;
    (void)hipFuncSetAttribute((const void*)main_kernel, hipFuncAttributeMaxDynamicSharedMemorySize, L_TOT);
    main_kernel<<<fast ? 256 : T, 512, L_TOT, stream>>>(
        (const uint16_t*)d_in[0], ((uint32_t)N * 512u) & on, F, (const uint16_t*)d_in[5], ((uint32_t)N * 2u) & on, F,
        (const uint16_t*)d_in[6], W, F, (const uint16_t*)d_in[9], W, F, (const uint16_t*)d_in[7], W, F, (const uint16_t*)d_in[8], W, F,
        (const int*)d_in[1], ((uint32_t)E * 4u) & on, F, (const int*)d_in[2], ((uint32_t)E * 4u) & on, F,
        (const int*)d_in[3], ((uint32_t)Ee * 4u) & on, F, (const int*)d_in[4], ((uint32_t)Ee * 4u) & on, F,
        (char*)(fast ? d_ws : d_in[6]), (uint32_t)need & on, F, (uint16_t*)d_out, ((uint32_t)N * 512u) & on, F,
        N, E, Ee, fast, T, 0, stamps);
}
```

```cpp
#include <hip/hip_runtime.h>
#include <hip/hip_bf16.h>
#include <stdint.h>

using f32x4 = __attribute__((ext_vector_type(4))) float;
using f32x2 = __attribute__((ext_vector_type(2))) float;
using u32x2 = __attribute__((ext_vector_type(2))) unsigned;
using bf16x2 = __attribute__((ext_vector_type(2))) __bf16;

constexpr int D = 256;
constexpr int TN = 40;
constexpr int L_TOT = 160 * 1024;
constexpr size_t WS_BK = 466944;

__device__ __forceinline__ float bf2f(uint32_t h) { return __uint_as_float(h << 16); }
__device__ __forceinline__ uint32_t pack2(float lo, float hi) {
    return __builtin_bit_cast(uint32_t, __builtin_convertvector(f32x2{lo, hi}, bf16x2));
}
__device__ __forceinline__ uint16_t f2bf(float f) { return (uint16_t)pack2(f, 0.f); }
__device__ __forceinline__ f32x4 wide(const u32x2 r) {
    return f32x4{bf2f(r[0] & 0xffffu), __uint_as_float(r[0] & 0xffff0000u), bf2f(r[1] & 0xffffu), __uint_as_float(r[1] & 0xffff0000u)};
}

#define BUF(type, name) type* name, uint32_t name##_bytes, uint32_t name##_flags
#define ARG(type, name) type* name; uint32_t name##_bytes, name##_flags;
struct Args {
    ARG(const uint16_t, x) ARG(const uint16_t, dis) ARG(const uint16_t, wl) ARG(const uint16_t, wv)
    ARG(const uint16_t, wq) ARG(const uint16_t, wk) ARG(const int, src) ARG(const int, dst)
    ARG(const int, esrc) ARG(const int, edst) ARG(char, ws) ARG(uint16_t, out)
    int N, E, Ee, fast, T, spare;
    void* stamps;
};
__global__ __launch_bounds__(512, 2) void main_kernel(
    BUF(const uint16_t, x_), BUF(const uint16_t, dis_), BUF(const uint16_t, wl_), BUF(const uint16_t, wv_),
    BUF(const uint16_t, wq_), BUF(const uint16_t, wk_), BUF(const int, src_), BUF(const int, dst_),
    BUF(const int, esrc_), BUF(const int, edst_), BUF(char, ws_), BUF(uint16_t, out_),
    int N_, int E_, int Ee_, int fast_, int T_, int spare_, void* stamps_) {
    extern __shared__ __attribute__((aligned(16))) char smem[];
    int wave, tile = blockIdx.x;
    unsigned tx = threadIdx.x;
    auto kp = (const __attribute__((address_space(4))) Args*)__builtin_amdgcn_kernarg_segment_ptr();
    asm volatile(
        ".Lcode:\n"
        "s_load_dwordx16 s[4:19], s[0:1], 0x0\n"
        "s_load_dwordx16 s[20:35], s[0:1], 0x40\n"
        "s_load_dwordx16 s[36:51], s[0:1], 0x80\n"
        "s_load_dwordx8 s[52:59], s[0:1], 0xc0\n"
        "s_getpc_b64 s[60:61]\n"
        ".Lpc:\n"
        "s_sub_u32 s60, s60, .Lpc-.Lcode\n"
        "s_subb_u32 s61, s61, 0\n"
        "s_mov_b32 s62, .Lcode_end-.Lcode\n"
        "s_mov_b32 s63, 0x20000\n"
        "s_mov_b32 m0, 23808\n"
        "v_lshlrev_b32 v9, 7, v0\n"
        "buffer_load_dword v9, s[60:63], 0 offen lds\n"
        "v_lshrrev_b32 v2, 6, v0\n"
        "s_nop 0\n"
        "v_readfirstlane_b32 s3, v2\n"
        "v_and_b32 v1, 63, v0\n"
        "v_mov_b32 v3, 1\n"
        "v_lshlrev_b32 v4, 4, v1\n"
        "v_lshlrev_b32 v8, 2, v1\n"
        "v_add_u32 v2, 0x80, v8\n"
        "v_writelane_b32 v2, 0, 0\n"
        "s_and_b32 s72, s3, 3\n"
        "s_add_u32 s83, s2, 128\n"
        "s_sub_u32 s73, s2, 64\n"
        "s_cselect_b32 s83, s83, s73\n"
        "s_cmp_ge_u32 s2, 192\n"
        "s_cselect_b32 s83, 255, s83\n"
        "s_cmp_ge_u32 s2, 254\n"
        "s_cselect_b32 s73, 56, 58\n"
        "s_sub_u32 s73, s2, s73\n"
        "s_cmp_ge_u32 s2, 250\n"
        "s_cselect_b32 s83, s73, s83\n"
        "s_cmp_ge_u32 s3, 4\n"
        "s_cbranch_scc1 .Lpre_a\n"
        "v_mov_b32 v9, 0\n"
        "s_cmp_lg_u32 s3, 0\n"
        "s_cbranch_scc1 .Lpre_1\n"
        "ds_write_b32 v8, v9 offset:45440\n"
        ".Lpre_1:\n"
        "s_setprio 3\n"
        "s_cmp_ge_u32 s83, 128\n"
        "s_cbranch_scc1 .Lpre_m\n"
        "v_lshrrev_b32 v6, 2, v1\n"
        "v_lshl_add_u32 v6, v6, 2, s72\n"
        "v_and_b32 v7, 48, v4\n"
        "v_lshl_or_b32 v5, v6, 13, v7\n"
        "s_lshr_b32 s73, s83, 4\n"
        "s_lshl_b32 s73, s73, 10\n"
        "s_and_b32 s74, s83, 7\n"
        "s_lshl_b32 s74, s74, 7\n"
        "s_add_u32 s73, s73, s74\n"
        "s_bfe_u32 s74, s83, 0x10003\n"
        "s_lshl_b32 s74, s74, 6\n"
        "s_add_u32 s73, s73, s74\n"
        "s_add_u32 s74, s73, 0x80000\n"
        "s_waitcnt lgkmcnt(0)\n"
        "s_load_dwordx2 s[64:65], s[44:45], 0x0\n"
        "buffer_load_dwordx4 v[8:11], v5, s[32:35], s73 offen\n"
        "buffer_load_dwordx4 v[12:15], v5, s[28:31], s73 offen\n"
        "buffer_load_dwordx4 v[16:19], v5, s[32:35], s74 offen\n"
        "buffer_load_dwordx4 v[20:23], v5, s[28:31], s74 offen\n"
        "s_branch .Lpre_done\n"
        ".Lpre_m:\n"
        "s_cmp_ge_u32 s83, 192\n"
        "s_cbranch_scc1 .Lpre_w\n"
        "v_bfe_u32 v5, v0, 5, 3\n"
        "v_and_b32 v7, 31, v0\n"
        "v_xor_b32 v7, v7, v5\n"
        "v_lshlrev_b32 v5, 9, v5\n"
        "v_lshl_or_b32 v6, v7, 4, v5\n"
        "v_xor_b32 v7, 0x80, v6\n"
        "s_sub_u32 s73, s83, 128\n"
        "s_lshr_b32 s77, s73, 2\n"
        "s_lshl_b32 s77, s77, 13\n"
        "s_and_b32 s78, s73, 3\n"
        "s_lshl_b32 s78, s78, 15\n"
        "s_lshl_b32 s76, s72, 10\n"
        "s_add_u32 m0, s76, 0\n"
        "s_waitcnt lgkmcnt(0)\n"
        "s_load_dwordx2 s[64:65], s[44:45], 0x0\n"
        "buffer_load_dwordx4 v6, s[20:23], s77 offen lds\n"
        "s_add_u32 m0, m0, 4096\n"
        "s_add_u32 s77, s77, 0x1000\n"
        "buffer_load_dwordx4 v7, s[20:23], s77 offen lds\n"
        "s_add_u32 m0, s76, 58368\n"
        "s_nop 0\n"
        "buffer_load_dwordx4 v6, s[24:27], s78 offen lds\n"
        "s_add_u32 m0, m0, 4096\n"
        "s_add_u32 s78, s78, 0x1000\n"
        "buffer_load_dwordx4 v7, s[24:27], s78 offen lds\n"
        "s_add_u32 m0, m0, 16384\n"
        "s_add_u32 s78, s78, 0x1000\n"
        "buffer_load_dwordx4 v6, s[24:27], s78 offen lds\n"
        "s_add_u32 m0, m0, 4096\n"
        "s_add_u32 s78, s78, 0x1000\n"
        "buffer_load_dwordx4 v7, s[24:27], s78 offen lds\n"
        "s_add_u32 m0, m0, 16384\n"
        "s_add_u32 s78, s78, 0x1000\n"
        "buffer_load_dwordx4 v6, s[24:27], s78 offen lds\n"
        "s_add_u32 m0, m0, 4096\n"
        "s_add_u32 s78, s78, 0x1000\n"
        "buffer_load_dwordx4 v7, s[24:27], s78 offen lds\n"
        "s_add_u32 m0, m0, 16384\n"
        "s_add_u32 s78, s78, 0x1000\n"
        "buffer_load_dwordx4 v6, s[24:27], s78 offen lds\n"
        "s_add_u32 m0, m0, 4096\n"
        "s_add_u32 s78, s78, 0x1000\n"
        "buffer_load_dwordx4 v7, s[24:27], s78 offen lds\n"
        "s_branch .Lpre_done\n"
        ".Lpre_w:\n"
        "v_bfe_u32 v6, v1, 3, 1\n"
        "v_lshlrev_b32 v6, 14, v6\n"
        "v_lshrrev_b32 v7, 4, v1\n"
        "v_lshl_or_b32 v6, v7, 12, v6\n"
        "v_and_b32 v7, 1, v1\n"
        "v_lshl_or_b32 v6, v7, 6, v6\n"
        "v_and_b32 v7, 4, v1\n"
        "v_lshl_or_b32 v6, v7, 3, v6\n"
        "v_and_b32 v7, 2, v1\n"
        "v_lshl_or_b32 v6, v7, 3, v6\n"
        "s_lshl_b32 s73, s72, 7\n"
        "v_add_u32 v5, s73, v6\n"
        "s_and_b32 s77, s83, 3\n"
        "s_lshl_b32 s77, s77, 15\n"
        "s_waitcnt lgkmcnt(0)\n"
        "s_load_dwordx2 s[64:65], s[44:45], 0x0\n"
        "s_cmp_ge_u32 s83, 200\n"
        "s_cbranch_scc1 .Lpre_done\n"
        "s_bitcmp1_b32 s83, 2\n"
        "s_cselect_b64 s[12:13], s[16:17], s[12:13]\n"
        "buffer_load_dwordx4 v[76:79], v5, s[12:15], s77 offen offset:0\n"
        "buffer_load_dwordx4 v[80:83], v5, s[12:15], s77 offen offset:512\n"
        "buffer_load_dwordx4 v[84:87], v5, s[12:15], s77 offen offset:1024\n"
        "buffer_load_dwordx4 v[88:91], v5, s[12:15], s77 offen offset:1536\n"
        "buffer_load_dwordx4 v[92:95], v5, s[12:15], s77 offen offset:2048\n"
        "buffer_load_dwordx4 v[96:99], v5, s[12:15], s77 offen offset:2560\n"
        "buffer_load_dwordx4 v[100:103], v5, s[12:15], s77 offen offset:3072\n"
        "buffer_load_dwordx4 v[104:107], v5, s[12:15], s77 offen offset:3584\n"
        ".Lpre_done:\n"
        "s_barrier\n"
        "s_bitcmp0_b32 s55, 0\n"
        "s_cbranch_scc1 .Lend_l\n"
        "s_memrealtime s[62:63]\n"
        "s_cmp_ge_u32 s83, 128\n"
        "s_cbranch_scc1 .Lno_sort\n"
        "s_lshl_b32 s78, s53, 2\n"
        "s_sub_i32 s80, s78, s74\n"
        "s_sub_i32 s78, s78, s73\n"
        "v_cmp_gt_i32 s[78:79], s78, v5\n"
        "v_cmp_gt_i32 s[80:81], s80, v5\n"
        "s_lshl_b32 s82, s83, 2\n"
        "s_lshl_b32 s75, s83, 4\n"
        "s_xor_b32 s82, s82, s75\n"
        "s_or_b32 s82, s82, 48\n"
        "s_and_b32 s82, s82, 0xfc\n"
        "v_mov_b32 v52, s82\n"
        "v_mov_b32 v53, s82\n"
        "v_mov_b32 v54, s82\n"
        "v_mov_b32 v55, s82\n"
        "v_lshlrev_b32 v56, 4, v0\n"
        "v_add_u32 v56, 127744, v56\n"
        "ds_write_b128 v56, v[52:55] offset:0\n"
        "ds_write_b128 v56, v[52:55] offset:4096\n"
        "ds_write_b128 v56, v[52:55] offset:8192\n"
        "ds_write_b128 v56, v[52:55] offset:12288\n"
        "ds_write_b128 v56, v[52:55] offset:16384\n"
        "ds_write_b128 v56, v[52:55] offset:20480\n"
        "ds_write_b128 v56, v[52:55] offset:24576\n"
        "ds_write_b128 v56, v[52:55] offset:28672\n"
        "ds_write_b128 v56, v[52:55] offset:32768\n"
        "ds_add_u32 v2, v3 offset:45524\n"
        "s_waitcnt lgkmcnt(0)\n"
        "s_add_u32 s73, s64, 1\n"
        "s_addc_u32 s74, s65, 0\n"
        "s_mul_i32 s76, s73, 0x7f4a7c15\n"
        "s_mul_hi_u32 s77, s73, 0x7f4a7c15\n"
        "s_mul_i32 s75, s73, 0x9e3779b9\n"
        "s_add_u32 s77, s77, s75\n"
        "s_mul_i32 s75, s74, 0x7f4a7c15\n"
        "s_add_u32 s77, s77, s75\n"
        "s_xor_b32 s66, s76, 0xd192ed03\n"
        "s_or_b32 s66, s66, 3\n"
        "s_xor_b32 s67, s77, 0xd1b54a32\n"
        "s_xor_b32 s76, s76, 0x4f6cdd1d\n"
        "s_xor_b32 s77, s77, 0x2545f491\n"
        "s_and_b32 s73, s76, 3\n"
        "s_lshr_b32 s74, s76, 2\n"
        "s_lshl_b32 s74, s74, 23\n"
        "s_or_b32 s68, s73, s74\n"
        "s_lshr_b64 s[76:77], s[76:77], 11\n"
        "s_and_b32 s73, s76, 3\n"
        "s_lshr_b32 s74, s76, 2\n"
        "s_lshl_b32 s74, s74, 23\n"
        "s_or_b32 s69, s73, s74\n"
        "s_lshr_b64 s[76:77], s[76:77], 11\n"
        "s_and_b32 s73, s76, 3\n"
        "s_lshr_b32 s74, s76, 2\n"
        "s_lshl_b32 s74, s74, 23\n"
        "s_or_b32 s70, s73, s74\n"
        "s_lshr_b64 s[76:77], s[76:77], 11\n"
        "s_and_b32 s73, s76, 3\n"
        "s_lshr_b32 s74, s76, 2\n"
        "s_lshl_b32 s74, s74, 23\n"
        "s_or_b32 s71, s73, s74\n"
        "ds_add_u32 v2, v3 offset:45480\n"
        "v_mov_b32 v52, 0x200\n"
        "s_mov_b32 s73, 52429\n"
        "s_movk_i32 s74, 62936\n"
        "s_movk_i32 s75, 144\n"
        "v_mov_b32 v53, 127744\n"
        "s_movk_i32 s82, 0xfc\n"
        "s_waitcnt vmcnt(3)\n"
        "v_mul_u32_u24 v40, s73, v8\n"
        "v_lshrrev_b32 v40, 21, v40\n"
        "v_mad_u32_u24 v24, v40, s75, v53\n"
        "v_mul_u32_u24 v41, s73, v9\n"
        "v_lshrrev_b32 v41, 21, v41\n"
        "v_mad_u32_u24 v25, v41, s75, v53\n"
        "v_mul_u32_u24 v42, s73, v10\n"
        "v_lshrrev_b32 v42, 21, v42\n"
        "v_mad_u32_u24 v26, v42, s75, v53\n"
        "v_mul_u32_u24 v43, s73, v11\n"
        "v_lshrrev_b32 v43, 21, v43\n"
        "v_mad_u32_u24 v27, v43, s75, v53\n"
        "s_waitcnt vmcnt(1)\n"
        "v_mul_u32_u24 v44, s73, v16\n"
        "v_lshrrev_b32 v44, 21, v44\n"
        "v_mad_u32_u24 v28, v44, s75, v53\n"
        "v_mul_u32_u24 v45, s73, v17\n"
        "v_lshrrev_b32 v45, 21, v45\n"
        "v_mad_u32_u24 v29, v45, s75, v53\n"
        "v_mul_u32_u24 v46, s73, v18\n"
        "v_lshrrev_b32 v46, 21, v46\n"
        "v_mad_u32_u24 v30, v46, s75, v53\n"
        "v_mul_u32_u24 v47, s73, v19\n"
        "v_lshrrev_b32 v47, 21, v47\n"
        "v_mad_u32_u24 v31, v47, s75, v53\n"
        "s_add_u32 s61, s72, 4\n"
        "s_mov_b32 s77, 0x40000\n"
        ".Lt_1:\n"
        "ds_read_b32 v54, v2 offset:45524\n"
        "s_sub_u32 s77, s77, 1\n"
        "s_waitcnt lgkmcnt(0)\n"
        "s_cbranch_scc1 .Lte_2\n"
        "v_readfirstlane_b32 s76, v54\n"
        "s_cmp_lt_u32 s76, s61\n"
        "s_cbranch_scc1 .Lt_1\n"
        ".Lte_2:\n"
        "s_mov_b64 exec, s[78:79]\n"
        "ds_add_rtn_u32 v32, v24, v52\n"
        "ds_add_rtn_u32 v33, v25, v52\n"
        "ds_add_rtn_u32 v34, v26, v52\n"
        "ds_add_rtn_u32 v35, v27, v52\n"
        "s_mov_b64 exec, s[80:81]\n"
        "ds_add_rtn_u32 v36, v28, v52\n"
        "ds_add_rtn_u32 v37, v29, v52\n"
        "ds_add_rtn_u32 v38, v30, v52\n"
        "ds_add_rtn_u32 v39, v31, v52\n"
        "s_mov_b64 exec, -1\n"
        "ds_add_u32 v2, v3 offset:45524\n"
        "s_waitcnt vmcnt(0)\n"
        "v_lshl_add_u32 v8, v8, 6, v8\n"
        "v_mad_i32_i24 v40, v40, s74, v8\n"
        "v_lshlrev_b32 v12, 9, v12\n"
        "v_and_or_b32 v40, v40, s82, v12\n"
        "v_lshl_add_u32 v9, v9, 6, v9\n"
        "v_mad_i32_i24 v41, v41, s74, v9\n"
        "v_lshlrev_b32 v13, 9, v13\n"
        "v_and_or_b32 v41, v41, s82, v13\n"
        "v_lshl_add_u32 v10, v10, 6, v10\n"
        "v_mad_i32_i24 v42, v42, s74, v10\n"
        "v_lshlrev_b32 v14, 9, v14\n"
        "v_and_or_b32 v42, v42, s82, v14\n"
        "v_lshl_add_u32 v11, v11, 6, v11\n"
        "v_mad_i32_i24 v43, v43, s74, v11\n"
        "v_lshlrev_b32 v15, 9, v15\n"
        "v_and_or_b32 v43, v43, s82, v15\n"
        "v_lshl_add_u32 v16, v16, 6, v16\n"
        "v_mad_i32_i24 v44, v44, s74, v16\n"
        "v_lshlrev_b32 v20, 9, v20\n"
        "v_and_or_b32 v44, v44, s82, v20\n"
        "v_lshl_add_u32 v17, v17, 6, v17\n"
        "v_mad_i32_i24 v45, v45, s74, v17\n"
        "v_lshlrev_b32 v21, 9, v21\n"
        "v_and_or_b32 v45, v45, s82, v21\n"
        "v_lshl_add_u32 v18, v18, 6, v18\n"
        "v_mad_i32_i24 v46, v46, s74, v18\n"
        "v_lshlrev_b32 v22, 9, v22\n"
        "v_and_or_b32 v46, v46, s82, v22\n"
        "v_lshl_add_u32 v19, v19, 6, v19\n"
        "v_mad_i32_i24 v47, v47, s74, v19\n"
        "v_lshlrev_b32 v23, 9, v23\n"
        "v_and_or_b32 v47, v47, s82, v23\n"
        "s_mov_b64 exec, s[78:79]\n"
        "s_waitcnt lgkmcnt(8)\n"
        "v_lshrrev_b32 v32, 9, v32\n"
        "v_min_u32 v32, 31, v32\n"
        "v_lshl_add_u32 v24, v32, 2, v24\n"
        "ds_write_b32 v24, v40 offset:4\n"
        "s_waitcnt lgkmcnt(8)\n"
        "v_lshrrev_b32 v33, 9, v33\n"
        "v_min_u32 v33, 31, v33\n"
        "v_lshl_add_u32 v25, v33, 2, v25\n"
        "ds_write_b32 v25, v41 offset:4\n"
        "s_waitcnt lgkmcnt(8)\n"
        "v_lshrrev_b32 v34, 9, v34\n"
        "v_min_u32 v34, 31, v34\n"
        "v_lshl_add_u32 v26, v34, 2, v26\n"
        "ds_write_b32 v26, v42 offset:4\n"
        "s_waitcnt lgkmcnt(8)\n"
        "v_lshrrev_b32 v35, 9, v35\n"
        "v_min_u32 v35, 31, v35\n"
        "v_lshl_add_u32 v27, v35, 2, v27\n"
        "ds_write_b32 v27, v43 offset:4\n"
        "s_mov_b64 exec, s[80:81]\n"
        "s_waitcnt lgkmcnt(8)\n"
        "v_lshrrev_b32 v36, 9, v36\n"
        "v_min_u32 v36, 31, v36\n"
        "v_lshl_add_u32 v28, v36, 2, v28\n"
        "ds_write_b32 v28, v44 offset:4\n"
        "s_waitcnt lgkmcnt(8)\n"
        "v_lshrrev_b32 v37, 9, v37\n"
        "v_min_u32 v37, 31, v37\n"
        "v_lshl_add_u32 v29, v37, 2, v29\n"
        "ds_write_b32 v29, v45 offset:4\n"
        "s_waitcnt lgkmcnt(8)\n"
        "v_lshrrev_b32 v38, 9, v38\n"
        "v_min_u32 v38, 31, v38\n"
        "v_lshl_add_u32 v30, v38, 2, v30\n"
        "ds_write_b32 v30, v46 offset:4\n"
        "s_waitcnt lgkmcnt(8)\n"
        "v_lshrrev_b32 v39, 9, v39\n"
        "v_min_u32 v39, 31, v39\n"
        "v_lshl_add_u32 v31, v39, 2, v31\n"
        "ds_write_b32 v31, v47 offset:4\n"
        "s_mov_b64 exec, -1\n"
        "ds_add_u32 v2, v3 offset:45500\n"
        "v_lshrrev_b32 v8, 2, v0\n"
        "v_bfe_u32 v10, v8, 1, 3\n"
        "v_and_or_b32 v10, v8, 48, v10\n"
        "v_and_b32 v8, 1, v8\n"
        "v_lshl_or_b32 v8, v8, 3, v10\n"
        "v_and_b32 v9, 48, v4\n"
        "v_mul_u32_u24 v10, 144, v8\n"
        "v_add3_u32 v10, v10, v9, v53\n"
        "v_lshlrev_b32 v11, 4, v0\n"
        "v_cmp_eq_u32 s[78:79], 0, v9\n"
        "v_mov_b32 v13, 16424\n"
        "v_mov_b32 v12, 0x2000\n"
        "v_cndmask_b32 v13, -1, v13, s[78:79]\n"
        "v_cndmask_b32 v12, -1, v12, s[78:79]\n"
        "s_mul_i32 s75, s83, 32768\n"
        "s_add_u32 s75, s75, 466944\n"
        "s_mov_b64 s[78:79], 0\n"
        "ds_read_b32 v15, v2 offset:45500\n"
        "s_waitcnt lgkmcnt(0)\n"
        "v_readfirstlane_b32 s73, v15\n"
        "s_cmp_ge_u32 s73, 4\n"
        "s_cbranch_scc0 .Lw_3\n"
        ".Le_4:\n"
        "ds_read_b128 v[16:19], v10 offset:0\n"
        "ds_read_b128 v[20:23], v10 offset:9216\n"
        "ds_read_b128 v[24:27], v10 offset:18432\n"
        "ds_read_b128 v[28:31], v10 offset:27648\n"
        "s_waitcnt lgkmcnt(3)\n"
        "v_min_u32 v16, v16, v13\n"
        "v_cmp_ge_u32 vcc, v16, v12\n"
        "v_or_b32 v16, s68, v16\n"
        "v_or_b32 v17, s69, v17\n"
        "v_or_b32 v18, s70, v18\n"
        "v_or_b32 v19, s71, v19\n"
        "buffer_store_dwordx4 v[16:19], v11, s[44:47], s75 offen sc1\n"
        "s_or_b64 s[78:79], s[78:79], vcc\n"
        "s_add_u32 s75, s75, 4096\n"
        "s_waitcnt lgkmcnt(2)\n"
        "v_min_u32 v20, v20, v13\n"
        "v_cmp_ge_u32 vcc, v20, v12\n"
        "v_or_b32 v20, s68, v20\n"
        "v_or_b32 v21, s69, v21\n"
        "v_or_b32 v22, s70, v22\n"
        "v_or_b32 v23, s71, v23\n"
        "buffer_store_dwordx4 v[20:23], v11, s[44:47], s75 offen sc1\n"
        "s_or_b64 s[78:79], s[78:79], vcc\n"
        "s_add_u32 s75, s75, 4096\n"
        "s_waitcnt lgkmcnt(1)\n"
        "v_min_u32 v24, v24, v13\n"
        "v_cmp_ge_u32 vcc, v24, v12\n"
        "v_or_b32 v24, s68, v24\n"
        "v_or_b32 v25, s69, v25\n"
        "v_or_b32 v26, s70, v26\n"
        "v_or_b32 v27, s71, v27\n"
        "buffer_store_dwordx4 v[24:27], v11, s[44:47], s75 offen sc1\n"
        "s_or_b64 s[78:79], s[78:79], vcc\n"
        "s_add_u32 s75, s75, 4096\n"
        "s_waitcnt lgkmcnt(0)\n"
        "v_min_u32 v28, v28, v13\n"
        "v_cmp_ge_u32 vcc, v28, v12\n"
        "v_or_b32 v28, s68, v28\n"
        "v_or_b32 v29, s69, v29\n"
        "v_or_b32 v30, s70, v30\n"
        "v_or_b32 v31, s71, v31\n"
        "buffer_store_dwordx4 v[28:31], v11, s[44:47], s75 offen sc1\n"
        "s_or_b64 s[78:79], s[78:79], vcc\n"
        "s_add_u32 s75, s75, 4096\n"
        "s_cmp_lg_u64 s[78:79], 0\n"
        "s_cbranch_scc1 .Lpub_more\n"
        ".Lpub_done:\n"
        "s_branch .Lspecial_done\n"
        ".Lno_sort:\n"
        "s_cmp_ge_u32 s83, 192\n"
        "s_cbranch_scc1 .Lno_m\n"
        "v_and_b32 v112, 15, v1\n"
        "v_lshrrev_b32 v113, 4, v1\n"
        "v_xor_b32 v113, v113, v112\n"
        "v_and_b32 v113, 3, v113\n"
        "v_and_or_b32 v113, v1, 12, v113\n"
        "v_lshlrev_b32 v112, 9, v112\n"
        "v_lshl_add_u32 v108, v113, 4, v112\n"
        "v_add_u32 v108, 0, v108\n"
        "v_xor_b32 v109, 64, v108\n"
        "v_xor_b32 v110, 128, v108\n"
        "v_xor_b32 v111, 192, v108\n"
        "s_mul_i32 s76, s3, 20480\n"
        "s_add_u32 s76, s76, 58368\n"
        "v_add_u32 v124, s76, v108\n"
        "v_add_u32 v125, s76, v109\n"
        "v_add_u32 v126, s76, v110\n"
        "v_add_u32 v127, s76, v111\n"
        "v_bfe_u32 v112, v1, 2, 1\n"
        "v_lshlrev_b32 v112, 13, v112\n"
        "v_bfe_u32 v113, v1, 5, 1\n"
        "v_lshl_or_b32 v112, v113, 8, v112\n"
        "v_bfe_u32 v113, v1, 3, 1\n"
        "v_lshl_or_b32 v112, v113, 6, v112\n"
        "v_and_b32 v113, 3, v1\n"
        "v_lshl_or_b32 v112, v113, 4, v112\n"
        "v_bfe_u32 v113, v1, 4, 1\n"
        "v_lshl_or_b32 v112, v113, 3, v112\n"
        "s_sub_u32 s80, s83, 128\n"
        "s_lshr_b32 s81, s80, 2\n"
        "s_and_b32 s80, s80, 3\n"
        "s_lshl_b32 s80, s80, 2\n"
        "s_add_u32 s80, s80, s3\n"
        "s_lshr_b32 s79, s80, 1\n"
        "s_lshl_b32 s79, s79, 14\n"
        "s_lshl_b32 s78, s81, 9\n"
        "s_add_u32 s79, s79, s78\n"
        "s_and_b32 s78, s80, 1\n"
        "s_lshl_b32 s78, s78, 7\n"
        "s_add_u32 s79, s79, s78\n"
        "s_add_u32 s79, s79, 8192\n"
        "s_lshl_b32 s80, s80, 7\n"
        "s_lshl_b32 s81, s81, 3\n"
        "s_add_u32 s78, s80, s81\n"
        "s_add_u32 s78, s78, 2304\n"
        "s_waitcnt lgkmcnt(0)\n"
        "s_add_u32 s73, s64, 1\n"
        "s_addc_u32 s74, s65, 0\n"
        "s_mul_i32 s76, s73, 0x7f4a7c15\n"
        "s_mul_hi_u32 s77, s73, 0x7f4a7c15\n"
        "s_mul_i32 s75, s73, 0x9e3779b9\n"
        "s_add_u32 s77, s77, s75\n"
        "s_mul_i32 s75, s74, 0x7f4a7c15\n"
        "s_add_u32 s77, s77, s75\n"
        "s_xor_b32 s66, s76, 0xd192ed03\n"
        "s_or_b32 s66, s66, 3\n"
        "s_xor_b32 s67, s77, 0xd1b54a32\n"
        "s_xor_b32 s76, s76, 0x4f6cdd1d\n"
        "s_xor_b32 s77, s77, 0x2545f491\n"
        "s_and_b32 s73, s76, 3\n"
        "s_lshr_b32 s74, s76, 2\n"
        "s_lshl_b32 s74, s74, 23\n"
        "s_or_b32 s68, s73, s74\n"
        "s_lshr_b64 s[76:77], s[76:77], 11\n"
        "s_and_b32 s73, s76, 3\n"
        "s_lshr_b32 s74, s76, 2\n"
        "s_lshl_b32 s74, s74, 23\n"
        "s_or_b32 s69, s73, s74\n"
        "s_lshr_b64 s[76:77], s[76:77], 11\n"
        "s_and_b32 s73, s76, 3\n"
        "s_lshr_b32 s74, s76, 2\n"
        "s_lshl_b32 s74, s74, 23\n"
        "s_or_b32 s70, s73, s74\n"
        "s_lshr_b64 s[76:77], s[76:77], 11\n"
        "s_and_b32 s73, s76, 3\n"
        "s_lshr_b32 s74, s76, 2\n"
        "s_lshl_b32 s74, s74, 23\n"
        "s_or_b32 s71, s73, s74\n"
        "ds_add_u32 v2, v3 offset:45480\n"
        "s_waitcnt vmcnt(0)\n"
        "ds_add_u32 v2, v3 offset:45500\n"
        "ds_read_b32 v113, v2 offset:45500\n"
        "s_waitcnt lgkmcnt(0)\n"
        "v_readfirstlane_b32 s73, v113\n"
        "s_cmp_ge_u32 s73, 4\n"
        "s_cbranch_scc0 .Lw_5\n"
        ".Le_6:\n"
        "ds_read_b128 v[8:11], v108 offset:0\n"
        "ds_read_b128 v[12:15], v124 offset:0\n"
        "ds_read_b128 v[16:19], v109 offset:0\n"
        "ds_read_b128 v[20:23], v125 offset:0\n"
        "ds_read_b128 v[24:27], v110 offset:0\n"
        "ds_read_b128 v[28:31], v126 offset:0\n"
        "ds_read_b128 v[32:35], v111 offset:0\n"
        "ds_read_b128 v[36:39], v127 offset:0\n"
        "ds_read_b128 v[40:43], v108 offset:256\n"
        "ds_read_b128 v[44:47], v124 offset:256\n"
        "ds_read_b128 v[48:51], v109 offset:256\n"
        "ds_read_b128 v[52:55], v125 offset:256\n"
        "ds_read_b128 v[56:59], v110 offset:256\n"
        "ds_read_b128 v[60:63], v126 offset:256\n"
        "ds_read_b128 v[64:67], v111 offset:256\n"
        "ds_read_b128 v[68:71], v127 offset:256\n"
        "s_waitcnt lgkmcnt(14)\n"
        "v_mfma_f32_16x16x32_bf16 v[116:119], v[8:11], v[12:15], 0\n"
        "s_waitcnt lgkmcnt(12)\n"
        "v_mfma_f32_16x16x32_bf16 v[120:123], v[16:19], v[20:23], 0\n"
        "s_waitcnt lgkmcnt(10)\n"
        "v_mfma_f32_16x16x32_bf16 v[116:119], v[24:27], v[28:31], v[116:119]\n"
        "s_waitcnt lgkmcnt(8)\n"
        "v_mfma_f32_16x16x32_bf16 v[120:123], v[32:35], v[36:39], v[120:123]\n"
        "s_waitcnt lgkmcnt(6)\n"
        "v_mfma_f32_16x16x32_bf16 v[116:119], v[40:43], v[44:47], v[116:119]\n"
        "s_waitcnt lgkmcnt(4)\n"
        "v_mfma_f32_16x16x32_bf16 v[120:123], v[48:51], v[52:55], v[120:123]\n"
        "s_waitcnt lgkmcnt(2)\n"
        "v_mfma_f32_16x16x32_bf16 v[116:119], v[56:59], v[60:63], v[116:119]\n"
        "s_waitcnt lgkmcnt(0)\n"
        "v_mfma_f32_16x16x32_bf16 v[120:123], v[64:67], v[68:71], v[120:123]\n"
        "s_nop 7\n"
        "v_add_f32 v116, v116, v120\n"
        "v_add_f32 v117, v117, v121\n"
        "v_add_f32 v118, v118, v122\n"
        "v_add_f32 v119, v119, v123\n"
        "s_and_b32 s73, s64, 7\n"
        "s_lshl_b32 s73, s73, 23\n"
        "s_sub_u32 s73, 0x3d800000, s73\n"
        "v_mul_f32 v116, s73, v116\n"
        "v_mul_f32 v117, s73, v117\n"
        "v_mul_f32 v118, s73, v118\n"
        "v_mul_f32 v119, s73, v119\n"
        "v_cvt_pk_bf16_f32 v8, v116, v117\n"
        "v_cvt_pk_bf16_f32 v9, v118, v119\n"
        "buffer_store_dwordx2 v[8:9], v112, s[44:47], s79 offen sc1\n"
        "v_add_u32 v10, v8, v9\n"
        "s_nop 1\n"
        "v_add_u32_dpp v10, v10, v10 row_shr:1 row_mask:0xf bank_mask:0xf bound_ctrl:0\n"
        "s_nop 1\n"
        "v_add_u32_dpp v10, v10, v10 row_shr:2 row_mask:0xf bank_mask:0xf bound_ctrl:0\n"
        "s_nop 1\n"
        "v_add_u32_dpp v10, v10, v10 row_shr:4 row_mask:0xf bank_mask:0xf bound_ctrl:0\n"
        "s_nop 1\n"
        "v_add_u32_dpp v10, v10, v10 row_shr:8 row_mask:0xf bank_mask:0xf bound_ctrl:0\n"
        "s_nop 1\n"
        "v_add_u32_dpp v10, v10, v10 row_bcast:15 row_mask:0xa bank_mask:0xf\n"
        "s_nop 1\n"
        "v_add_u32_dpp v10, v10, v10 row_bcast:31 row_mask:0xc bank_mask:0xf\n"
        "s_nop 0\n"
        "v_readlane_b32 s73, v10, 63\n"
        "s_add_u32 s73, s73, s67\n"
        "v_mov_b32 v12, s66\n"
        "v_mov_b32 v13, s73\n"
        "s_mov_b64 exec, 1\n"
        "buffer_store_dwordx2 v[12:13], v2, s[44:47], s78 offen sc1\n"
        "s_mov_b64 exec, -1\n"
        "s_branch .Lspecial_done\n"
        ".Lflag_out:\n"
        "s_waitcnt vmcnt(0)\n"
        "v_mov_b32 v12, s66\n"
        "v_mov_b32 v13, s67\n"
        "s_mov_b64 exec, 1\n"
        "buffer_store_dwordx2 v[12:13], v2, s[44:47], s78 offen sc1\n"
        "s_mov_b64 exec, -1\n"
        ".Lspecial_done:\n"
        "ds_add_u32 v2, v3 offset:45516\n"
        ".Lheld:\n"
        "s_setprio 0\n"
        "s_cmp_ge_u32 s2, s56\n"
        "s_cbranch_scc1 .Lend_l\n"
        "v_mov_b32 v52, 0\n"
        "v_mov_b32 v53, 0\n"
        "v_mov_b32 v54, 0\n"
        "v_bfrev_b32 v55, 1\n"
        "v_lshlrev_b32 v47, 4, v0\n"
        "v_add_u32 v57, 130304, v47\n"
        "v_lshrrev_b32 v7, 2, v47\n"
        "v_add_u32 v7, 128256, v7\n"
        "ds_read_b32 v8, v2 offset:45516\n"
        "s_waitcnt lgkmcnt(0)\n"
        "v_readfirstlane_b32 s73, v8\n"
        "s_cmp_ge_u32 s73, 4\n"
        "s_cbranch_scc0 .Lw_7\n"
        ".Le_8:\n"
        "s_setprio 0\n"
        "s_mul_i32 s76, s72, 5120\n"
        "s_mov_b32 m0, s76\n"
        "s_add_u32 s77, s76, 0x1000\n"
        "buffer_load_dwordx4 v4, s[8:11], s76 offen offset:0 lds\n"
        "buffer_load_dwordx4 v4, s[8:11], s76 offen offset:1024 lds\n"
        "buffer_load_dwordx4 v4, s[8:11], s76 offen offset:2048 lds\n"
        "buffer_load_dwordx4 v4, s[8:11], s76 offen offset:3072 lds\n"
        "s_mov_b32 m0, s77\n"
        "s_nop 0\n"
        "buffer_load_dwordx4 v4, s[8:11], s77 offen lds\n"
        "s_setprio 0\n"
        "ds_write_b128 v57, v[52:55] offset:0\n"
        "ds_write_b128 v57, v[52:55] offset:4096\n"
        "ds_write_b128 v57, v[52:55] offset:8192\n"
        "ds_write_b128 v57, v[52:55] offset:12288\n"
        "ds_write_b128 v57, v[52:55] offset:16384\n"
        "ds_write_b128 v57, v[52:55] offset:20480\n"
        "ds_write2st64_b32 v7, v52, v52 offset1:4\n"
        "v_and_b32 v7, 0xffffff00, v7\n"
        "s_and_b32 s73, s2, 7\n"
        "s_lshl_b32 s73, s73, 1\n"
        "s_and_b32 s74, s2, 0xf0\n"
        "s_add_u32 s73, s73, s74\n"
        "s_bfe_u32 s74, s2, 0x10003\n"
        "s_add_u32 s73, s73, s74\n"
        "s_lshl_b32 s73, s73, 6\n"
        "s_add_u32 s40, s73, 466944\n"
        "s_add_u32 s41, s73, 2564096\n"
        "s_add_u32 s42, s73, 483328\n"
        "s_add_u32 s43, s73, 2580480\n"
        "v_and_b32 v44, 48, v47\n"
        "v_lshrrev_b32 v47, 6, v47\n"
        "v_mul_u32_u24 v47, 32768, v47\n"
        "v_add_u32 v47, v47, v44\n"
        "v_and_b32 v44, 3, v1\n"
        "v_lshlrev_b32 v44, 11, v44\n"
        "v_mov_b32 v46, 16\n"
        "s_mul_i32 s74, s2, 80\n"
        "s_lshl_b32 s75, s72, 1\n"
        "s_add_u32 s74, s74, s75\n"
        "v_lshl_add_u32 v45, v1, 3, s74\n"
        "v_min_u32 v45, 20478, v45\n"
        "s_cmp_gt_u32 s72, 0\n"
        "s_cselect_b32 s14, 1, 0\n"
        "s_cmp_gt_u32 s72, 1\n"
        "s_cselect_b32 s15, 1, 0\n"
        "s_cmp_gt_u32 s72, 2\n"
        "s_cselect_b32 s16, 1, 0\n"
        "v_and_b32 v10, 15, v1\n"
        "v_lshrrev_b32 v24, 5, v1\n"
        "v_mul_u32_u24 v24, 272, v24\n"
        "v_lshl_add_u32 v24, v10, 4, v24\n"
        "v_bfe_u32 v25, v1, 4, 1\n"
        "v_lshl_add_u32 v24, v25, 3, v24\n"
        "s_mul_i32 s73, s72, 544\n"
        "s_add_u32 s73, s73, 0\n"
        "v_add_u32 v51, s73, v24\n"
        "v_mov_b64 v[56:57], 0\n"
        "v_mov_b64 v[58:59], 0\n"
        "v_lshrrev_b32 v24, 5, v1\n"
        "v_lshlrev_b32 v24, 7, v24\n"
        "v_lshl_add_u32 v24, v10, 3, v24\n"
        "v_lshl_add_u32 v24, v25, 2, v24\n"
        "s_add_u32 s73, s73, 17408\n"
        "v_add_u32 v42, s73, v24\n"
        "s_mul_i32 s12, s72, 6144\n"
        "s_add_u32 s12, s12, 130304\n"
        "v_mov_b32 v43, v2\n"
        "v_lshrrev_b32 v16, 4, v1\n"
        "v_lshlrev_b32 v85, 2, v1\n"
        "v_add_u32 v84, 128256, v85\n"
        "v_mul_u32_u24 v83, 6144, v16\n"
        "v_add_u32 v83, 130304, v83\n"
        "v_cmp_gt_u32 s[18:19], 10, v10\n"
        "v_mov_b32 v82, 154880\n"
        "s_movk_i32 s17, 0xfc\n"
        "v_cndmask_b32 v83, v82, v83, s[18:19]\n"
        "s_add_u32 s73, s12, 452\n"
        "v_lshl_add_u32 v24, v16, 4, s73\n"
        "v_and_b32 v25, 4, v1\n"
        "v_sub_u32 v24, v24, v25\n"
        "v_mov_b32 v25, 129280\n"
        "v_cmp_gt_u32 s[76:77], 8, v10\n"
        "v_and_b32 v26, 3, v1\n"
        "v_mul_u32_u24 v54, 0x5555, v26\n"
        "v_mov_b32 v26, 512\n"
        "v_cndmask_b32 v52, v25, v24, s[76:77]\n"
        "v_cndmask_b32 v53, v24, v25, s[76:77]\n"
        "v_cndmask_b32 v55, 0, v26, s[76:77]\n"
        "v_sub_u32 v60, v26, v55\n"
        "s_add_u32 s73, s12, 12\n"
        "v_and_b32 v48, 31, v1\n"
        "v_lshlrev_b32 v48, 4, v48\n"
        "v_lshrrev_b32 v16, 5, v1\n"
        "v_lshl_add_u32 v49, v16, 4, s73\n"
        "s_lshl_b32 s13, s72, 4\n"
        ".Lpd:\n"
        "s_memrealtime s[60:61]\n"
        "s_waitcnt lgkmcnt(0)\n"
        "s_sub_u32 s73, s60, s62\n"
        "s_cmp_ge_u32 s73, 250\n"
        "s_cbranch_scc1 .Llook\n"
        "s_sleep 2\n"
        "s_branch .Lpd\n"
        ".Llook:\n"
        "s_setprio 3\n"
        "buffer_load_dwordx4 v[64:67], v47, s[44:47], s40 offen sc1\n"
        "buffer_load_dwordx4 v[68:71], v47, s[44:47], s41 offen sc1\n"
        "s_setprio 2\n"
        "s_mov_b32 s73, 0xff800003\n"
        "s_movk_i32 s82, 0x2000\n"
        "s_waitcnt vmcnt(0)\n"
        "ds_add_u32 v2, v3 offset:45512\n"
        "v_xor_b32 v64, s68, v64\n"
        "v_xor_b32 v65, s69, v65\n"
        "v_xor_b32 v66, s70, v66\n"
        "v_xor_b32 v67, s71, v67\n"
        "v_or3_b32 v8, v64, v65, v66\n"
        "v_or_b32 v8, v8, v67\n"
        "v_and_b32 v8, s73, v8\n"
        "v_cmp_ne_u32 s[74:75], 0, v8\n"
        "v_xor_b32 v68, s68, v68\n"
        "v_xor_b32 v69, s69, v69\n"
        "v_xor_b32 v70, s70, v70\n"
        "v_xor_b32 v71, s71, v71\n"
        "v_or3_b32 v9, v68, v69, v70\n"
        "v_or_b32 v9, v9, v71\n"
        "v_and_b32 v9, s73, v9\n"
        "v_cmp_ne_u32 s[76:77], 0, v9\n"
        "v_mov_b32_dpp v10, v64 quad_perm:[0,0,0,0] row_mask:0xf bank_mask:0xf\n"
        "v_mov_b32_dpp v11, v68 quad_perm:[0,0,0,0] row_mask:0xf bank_mask:0xf\n"
        "v_cmp_ge_u32 s[32:33], v10, v44\n"
        "v_cmp_le_u32 s[36:37], s82, v10\n"
        "v_cmp_ge_u32 s[34:35], v11, v44\n"
        "v_cmp_le_u32 s[38:39], s82, v11\n"
        "s_or_b64 s[78:79], s[74:75], s[76:77]\n"
        ".Lbk:\n"
        "s_cmp_lg_u64 s[78:79], 0\n"
        "s_cbranch_scc1 .Lbk_again\n"
        "s_or_b64 s[80:81], s[36:37], s[38:39]\n"
        "s_cbranch_scc1 .Lbk_more\n"
        ".Lbk_done:\n"
        "s_mov_b64 exec, s[32:33]\n"
        "v_and_or_b32 v8, v64, s17, v7\n"
        "v_lshrrev_b32 v24, 8, v64\n"
        "ds_add_rtn_u32 v8, v8, v46\n"
        "v_and_or_b32 v9, v65, s17, v7\n"
        "v_lshrrev_b32 v25, 8, v65\n"
        "ds_add_rtn_u32 v9, v9, v46\n"
        "v_and_or_b32 v10, v66, s17, v7\n"
        "v_lshrrev_b32 v26, 8, v66\n"
        "ds_add_rtn_u32 v10, v10, v46\n"
        "v_and_or_b32 v11, v67, s17, v7\n"
        "v_lshrrev_b32 v27, 8, v67\n"
        "ds_add_rtn_u32 v11, v11, v46\n"
        "s_mov_b64 exec, s[34:35]\n"
        "v_and_or_b32 v12, v68, s17, v7\n"
        "v_lshrrev_b32 v28, 8, v68\n"
        "ds_add_rtn_u32 v12, v12, v46\n"
        "v_and_or_b32 v13, v69, s17, v7\n"
        "v_lshrrev_b32 v29, 8, v69\n"
        "ds_add_rtn_u32 v13, v13, v46\n"
        "v_and_or_b32 v14, v70, s17, v7\n"
        "v_lshrrev_b32 v30, 8, v70\n"
        "ds_add_rtn_u32 v14, v14, v46\n"
        "v_and_or_b32 v15, v71, s17, v7\n"
        "v_lshrrev_b32 v31, 8, v71\n"
        "ds_add_rtn_u32 v15, v15, v46\n"
        "s_cmp_lg_u64 s[80:81], 0\n"
        "s_cbranch_scc1 .Len0\n"
        ".Len0_e:\n"
        "s_mov_b64 exec, -1\n"
        "ds_add_u32 v2, v3 offset:45528\n"
        "ds_read_b32 v86, v2 offset:45512\n"
        "s_waitcnt lgkmcnt(0)\n"
        "v_readfirstlane_b32 s73, v86\n"
        "s_cmp_ge_u32 s73, 4\n"
        "s_cbranch_scc0 .Lw_9\n"
        ".Le_10:\n"
        "ds_read_u16 v6, v45\n"
        "s_mov_b64 exec, s[32:33]\n"
        "ds_read_u16 v24, v24\n"
        "ds_read_u16 v25, v25\n"
        "ds_read_u16 v26, v26\n"
        "ds_read_u16 v27, v27\n"
        "s_mov_b64 exec, s[34:35]\n"
        "ds_read_u16 v28, v28\n"
        "ds_read_u16 v29, v29\n"
        "ds_read_u16 v30, v30\n"
        "ds_read_u16 v31, v31\n"
        "s_cmp_lg_u64 s[80:81], 0\n"
        "s_cbranch_scc1 .Len1\n"
        ".Len1_e:\n"
        "s_mov_b64 exec, -1\n"
        "s_mov_b32 s74, 0x40000\n"
        ".Lcn:\n"
        "ds_read_b32 v112, v85 offset:45440\n"
        "ds_read2st64_b32 v[108:109], v84 offset1:1\n"
        "ds_read2st64_b32 v[110:111], v84 offset0:2 offset1:3\n"
        "s_waitcnt lgkmcnt(0)\n"
        "v_readlane_b32 s73, v112, 22\n"
        "s_cmp_lt_u32 s73, 4\n"
        "s_cbranch_scc1 .Lcn_again\n"
        ".Lcn_done:\n"
        "v_add3_u32 v113, v108, v109, v110\n"
        "v_add3_u32 v113, v113, v111, 48\n"
        "v_readlane_b32 s75, v112, 13\n"
        "v_readlane_b32 s77, v113, 10\n"
        "v_and_b32 v113, 0xffffffc0, v113\n"
        "v_max_u32 v113, 64, v113\n"
        "v_cndmask_b32 v113, 0, v113, s[18:19]\n"
        "v_mad_u32_u24 v83, v108, s14, v83\n"
        "v_mad_u32_u24 v83, v109, s15, v83\n"
        "v_add_u32_dpp v114, v113, v113 row_shr:1 row_mask:0xf bank_mask:0xf bound_ctrl:0\n"
        "v_mad_u32_u24 v83, v110, s16, v83\n"
        "s_xor_b32 s77, s77, 48\n"
        "v_add_u32_dpp v114, v114, v114 row_shr:2 row_mask:0xf bank_mask:0xf bound_ctrl:0\n"
        "v_sub_u32 v83, v83, v113\n"
        "s_or_b32 s75, s75, s77\n"
        "v_add_u32_dpp v114, v114, v114 row_shr:4 row_mask:0xf bank_mask:0xf bound_ctrl:0\n"
        "v_lshrrev_b32 v50, 6, v113\n"
        "v_lshlrev_b32 v6, 16, v6\n"
        "v_add_u32_dpp v114, v114, v114 row_shr:8 row_mask:0xf bank_mask:0xf bound_ctrl:0\n"
        "s_mov_b32 s73, 0xfffffe00\n"
        "v_cmp_lt_u32 vcc, 5376, v114\n"
        "v_cndmask_b32 v115, 0, v114, s[18:19]\n"
        "v_add_u32 v83, v83, v115\n"
        "s_or_b32 s75, s75, vcc_lo\n"
        "s_or_b32 s75, s75, vcc_hi\n"
        "s_cbranch_scc1 .Lfail_l\n"
        "ds_bpermute_b32 v92, v64, v83\n"
        "ds_bpermute_b32 v93, v65, v83\n"
        "ds_bpermute_b32 v94, v66, v83\n"
        "ds_bpermute_b32 v95, v67, v83\n"
        "ds_bpermute_b32 v96, v68, v83\n"
        "ds_bpermute_b32 v97, v69, v83\n"
        "ds_bpermute_b32 v98, v70, v83\n"
        "ds_bpermute_b32 v99, v71, v83\n"
        "s_cmp_lg_u64 s[80:81], 0\n"
        "s_cbranch_scc1 .Lbp\n"
        ".Lbp_e:\n"
        "v_and_b32 v64, s73, v64\n"
        "v_and_b32 v65, s73, v65\n"
        "v_and_b32 v66, s73, v66\n"
        "v_and_b32 v67, s73, v67\n"
        "v_and_b32 v68, s73, v68\n"
        "v_and_b32 v69, s73, v69\n"
        "v_and_b32 v70, s73, v70\n"
        "v_and_b32 v71, s73, v71\n"
        "s_add_u32 s76, s13, 15\n"
        "v_readlane_b32 s77, v114, s76\n"
        "v_subrev_u32 v50, 1, v50\n"
        "s_waitcnt lgkmcnt(0)\n"
        "s_mov_b64 exec, s[32:33]\n"
        "v_add_u32 v8, v8, v92\n"
        "ds_write2_b32 v8, v64, v24 offset0:3 offset1:113\n"
        "v_add_u32 v9, v9, v93\n"
        "ds_write2_b32 v9, v65, v25 offset0:3 offset1:113\n"
        "v_add_u32 v10, v10, v94\n"
        "ds_write2_b32 v10, v66, v26 offset0:3 offset1:113\n"
        "v_add_u32 v11, v11, v95\n"
        "ds_write2_b32 v11, v67, v27 offset0:3 offset1:113\n"
        "s_mov_b64 exec, s[34:35]\n"
        "v_add_u32 v12, v12, v96\n"
        "ds_write2_b32 v12, v68, v28 offset0:3 offset1:113\n"
        "v_add_u32 v13, v13, v97\n"
        "ds_write2_b32 v13, v69, v29 offset0:3 offset1:113\n"
        "v_add_u32 v14, v14, v98\n"
        "ds_write2_b32 v14, v70, v30 offset0:3 offset1:113\n"
        "v_add_u32 v15, v15, v99\n"
        "ds_write2_b32 v15, v71, v31 offset0:3 offset1:113\n"
        "s_cmp_lg_u64 s[80:81], 0\n"
        "s_cbranch_scc1 .Len2\n"
        ".Len2_e:\n"
        "s_mov_b64 exec, -1\n"
        "ds_add_u32 v2, v3 offset:45504\n"
        "s_lshr_b32 s9, s77, 4\n"
        "s_sub_i32 s9, s9, 14\n"
        "s_max_i32 s9, s9, 0\n"
        "s_lshr_b32 s9, s9, 1\n"
        "s_mov_b32 s10, 3\n"
        "ds_read_b32 v40, v2 offset:45504\n"
        "s_waitcnt lgkmcnt(0)\n"
        "v_readfirstlane_b32 s73, v40\n"
        "s_cmp_ge_u32 s73, 4\n"
        "s_cbranch_scc0 .Lw_11\n"
        ".Le_12:\n"
        "ds_read_b32 v40, v2 offset:45520\n"
        "s_waitcnt lgkmcnt(0)\n"
        "v_readfirstlane_b32 s73, v40\n"
        "s_cmp_ge_u32 s73, 4\n"
        "s_cbranch_scc0 .Lw_13\n"
        ".Le_14:\n"
        "ds_read2_b32 v[8:9], v49 offset0:0 offset1:8\n"
        "ds_read2_b32 v[10:11], v49 offset0:16 offset1:24\n"
        "ds_read2_b32 v[12:13], v49 offset0:32 offset1:40\n"
        "ds_read2_b32 v[14:15], v49 offset0:48 offset1:56\n"
        "ds_read2_b32 v[16:17], v49 offset0:64 offset1:72\n"
        "ds_read2_b32 v[18:19], v49 offset0:80 offset1:88\n"
        "ds_read2_b32 v[20:21], v49 offset0:96 offset1:104\n"
        "v_add_u32 v49, 448, v49\n"
        "ds_read2_b32 v[116:117], v52 offset0:0 offset1:1\n"
        "ds_read2_b32 v[118:119], v53 offset0:0 offset1:1\n"
        "ds_read2_b32 v[40:41], v49 offset0:0 offset1:8\n"
        "ds_read2_b32 v[120:121], v52 offset0:16 offset1:17\n"
        "ds_read2_b32 v[122:123], v53 offset0:16 offset1:17\n"
        "ds_read2_b32 v[62:63], v49 offset0:16 offset1:24\n"
        "s_waitcnt lgkmcnt(12)\n"
        "v_or_b32 v8, v8, v48\n"
        "buffer_load_dwordx4 v[124:127], v8, s[4:7], 0 offen\n"
        "v_or_b32 v9, v9, v48\n"
        "buffer_load_dwordx4 v[128:131], v9, s[4:7], 0 offen\n"
        "s_waitcnt lgkmcnt(11)\n"
        "v_or_b32 v10, v10, v48\n"
        "buffer_load_dwordx4 v[132:135], v10, s[4:7], 0 offen\n"
        "v_or_b32 v11, v11, v48\n"
        "buffer_load_dwordx4 v[136:139], v11, s[4:7], 0 offen\n"
        "s_waitcnt lgkmcnt(10)\n"
        "v_or_b32 v12, v12, v48\n"
        "buffer_load_dwordx4 v[140:143], v12, s[4:7], 0 offen\n"
        "v_or_b32 v13, v13, v48\n"
        "buffer_load_dwordx4 v[144:147], v13, s[4:7], 0 offen\n"
        "s_waitcnt lgkmcnt(9)\n"
        "v_or_b32 v14, v14, v48\n"
        "buffer_load_dwordx4 v[148:151], v14, s[4:7], 0 offen\n"
        "v_or_b32 v15, v15, v48\n"
        "buffer_load_dwordx4 v[152:155], v15, s[4:7], 0 offen\n"
        "s_waitcnt lgkmcnt(8)\n"
        "v_or_b32 v16, v16, v48\n"
        "buffer_load_dwordx4 v[156:159], v16, s[4:7], 0 offen\n"
        "v_or_b32 v17, v17, v48\n"
        "buffer_load_dwordx4 v[160:163], v17, s[4:7], 0 offen\n"
        "s_waitcnt lgkmcnt(7)\n"
        "v_or_b32 v18, v18, v48\n"
        "buffer_load_dwordx4 v[164:167], v18, s[4:7], 0 offen\n"
        "v_or_b32 v19, v19, v48\n"
        "buffer_load_dwordx4 v[168:171], v19, s[4:7], 0 offen\n"
        "s_waitcnt lgkmcnt(6)\n"
        "v_or_b32 v20, v20, v48\n"
        "buffer_load_dwordx4 v[172:175], v20, s[4:7], 0 offen\n"
        "v_or_b32 v21, v21, v48\n"
        "buffer_load_dwordx4 v[176:179], v21, s[4:7], 0 offen\n"
        "v_readlane_b32 s74, v50, s13\n"
        "s_mov_b32 s75, 0\n"
        "s_getpc_b64 s[82:83]\n"
        ".Lrg_base:\n"
        "s_mov_b32 s73, 40\n"
        "s_setprio 2\n"
        ".Lrg_m:\n"
        ".Lrg_m0:\n"
        "ds_read2_b32 v[24:25], v52 offset0:32 offset1:33\n"
        "ds_read2_b32 v[26:27], v53 offset0:32 offset1:33\n"
        "ds_read2_b32 v[32:33], v49 offset0:32 offset1:40\n"
        "s_waitcnt vmcnt(12) lgkmcnt(6)\n"
        "v_or_b32 v40, v40, v48\n"
        "v_or_b32 v41, v41, v48\n"
        "s_sub_u32 s74, s74, 1\n"
        "v_smfmac_f32_16x16x64_bf16 v[56:59], v[116:119], v[124:131], v54\n"
        "buffer_load_dwordx4 v[180:183], v40, s[4:7], 0 offen\n"
        "buffer_load_dwordx4 v[184:187], v41, s[4:7], 0 offen\n"
        "s_cbranch_scc1 .Lrg_me0\n"
        ".Lrg_m1:\n"
        "ds_read2_b32 v[28:29], v52 offset0:48 offset1:49\n"
        "ds_read2_b32 v[30:31], v53 offset0:48 offset1:49\n"
        "ds_read2_b32 v[34:35], v49 offset0:48 offset1:56\n"
        "s_waitcnt vmcnt(12) lgkmcnt(6)\n"
        "v_or_b32 v62, v62, v48\n"
        "v_or_b32 v63, v63, v48\n"
        "s_sub_u32 s74, s74, 1\n"
        "v_smfmac_f32_16x16x64_bf16 v[56:59], v[120:123], v[132:139], v54\n"
        "buffer_load_dwordx4 v[124:127], v62, s[4:7], 0 offen\n"
        "buffer_load_dwordx4 v[128:131], v63, s[4:7], 0 offen\n"
        "s_cbranch_scc1 .Lrg_me1\n"
        ".Lrg_m2:\n"
        "ds_read2_b32 v[116:117], v52 offset0:64 offset1:65\n"
        "ds_read2_b32 v[118:119], v53 offset0:64 offset1:65\n"
        "ds_read2_b32 v[40:41], v49 offset0:64 offset1:72\n"
        "s_waitcnt vmcnt(12) lgkmcnt(6)\n"
        "v_or_b32 v32, v32, v48\n"
        "v_or_b32 v33, v33, v48\n"
        "s_sub_u32 s74, s74, 1\n"
        "v_smfmac_f32_16x16x64_bf16 v[56:59], v[24:27], v[140:147], v54\n"
        "buffer_load_dwordx4 v[132:135], v32, s[4:7], 0 offen\n"
        "buffer_load_dwordx4 v[136:139], v33, s[4:7], 0 offen\n"
        "s_cbranch_scc1 .Lrg_me2\n"
        ".Lrg_m3:\n"
        "ds_read2_b32 v[120:121], v52 offset0:80 offset1:81\n"
        "ds_read2_b32 v[122:123], v53 offset0:80 offset1:81\n"
        "ds_read2_b32 v[62:63], v49 offset0:80 offset1:88\n"
        "s_waitcnt vmcnt(12) lgkmcnt(6)\n"
        "v_or_b32 v34, v34, v48\n"
        "v_or_b32 v35, v35, v48\n"
        "s_sub_u32 s74, s74, 1\n"
        "v_smfmac_f32_16x16x64_bf16 v[56:59], v[28:31], v[148:155], v54\n"
        "buffer_load_dwordx4 v[140:143], v34, s[4:7], 0 offen\n"
        "buffer_load_dwordx4 v[144:147], v35, s[4:7], 0 offen\n"
        "s_cbranch_scc1 .Lrg_me3\n"
        ".Lrg_m4:\n"
        "ds_read2_b32 v[24:25], v52 offset0:96 offset1:97\n"
        "ds_read2_b32 v[26:27], v53 offset0:96 offset1:97\n"
        "ds_read2_b32 v[32:33], v49 offset0:96 offset1:104\n"
        "s_waitcnt vmcnt(12) lgkmcnt(6)\n"
        "v_or_b32 v40, v40, v48\n"
        "v_or_b32 v41, v41, v48\n"
        "s_sub_u32 s74, s74, 1\n"
        "v_smfmac_f32_16x16x64_bf16 v[56:59], v[116:119], v[156:163], v54\n"
        "buffer_load_dwordx4 v[148:151], v40, s[4:7], 0 offen\n"
        "buffer_load_dwordx4 v[152:155], v41, s[4:7], 0 offen\n"
        "s_cbranch_scc1 .Lrg_me4\n"
        ".Lrg_m5:\n"
        "ds_read2_b32 v[28:29], v52 offset0:112 offset1:113\n"
        "ds_read2_b32 v[30:31], v53 offset0:112 offset1:113\n"
        "ds_read2_b32 v[34:35], v49 offset0:112 offset1:120\n"
        "s_waitcnt vmcnt(12) lgkmcnt(6)\n"
        "v_or_b32 v62, v62, v48\n"
        "v_or_b32 v63, v63, v48\n"
        "s_sub_u32 s74, s74, 1\n"
        "v_smfmac_f32_16x16x64_bf16 v[56:59], v[120:123], v[164:171], v54\n"
        "buffer_load_dwordx4 v[156:159], v62, s[4:7], 0 offen\n"
        "buffer_load_dwordx4 v[160:163], v63, s[4:7], 0 offen\n"
        "s_cbranch_scc1 .Lrg_me5\n"
        ".Lrg_m6:\n"
        "ds_read2_b32 v[116:117], v52 offset0:128 offset1:129\n"
        "ds_read2_b32 v[118:119], v53 offset0:128 offset1:129\n"
        "ds_read2_b32 v[40:41], v49 offset0:128 offset1:136\n"
        "s_waitcnt vmcnt(12) lgkmcnt(6)\n"
        "v_or_b32 v32, v32, v48\n"
        "v_or_b32 v33, v33, v48\n"
        "s_sub_u32 s74, s74, 1\n"
        "v_smfmac_f32_16x16x64_bf16 v[56:59], v[24:27], v[172:179], v54\n"
        "buffer_load_dwordx4 v[164:167], v32, s[4:7], 0 offen\n"
        "buffer_load_dwordx4 v[168:171], v33, s[4:7], 0 offen\n"
        "s_cbranch_scc1 .Lrg_me6\n"
        ".Lrg_m7:\n"
        "ds_read2_b32 v[120:121], v52 offset0:144 offset1:145\n"
        "ds_read2_b32 v[122:123], v53 offset0:144 offset1:145\n"
        "ds_read2_b32 v[62:63], v49 offset0:144 offset1:152\n"
        "s_waitcnt vmcnt(12) lgkmcnt(6)\n"
        "v_or_b32 v34, v34, v48\n"
        "v_or_b32 v35, v35, v48\n"
        "s_sub_u32 s74, s74, 1\n"
        "v_smfmac_f32_16x16x64_bf16 v[56:59], v[28:31], v[180:187], v54\n"
        "buffer_load_dwordx4 v[172:175], v34, s[4:7], 0 offen\n"
        "buffer_load_dwordx4 v[176:179], v35, s[4:7], 0 offen\n"
        "s_cbranch_scc1 .Lrg_me7\n"
        ".Lrg_m8:\n"
        "v_add_u32 v49, 512, v49\n"
        "v_add_u32 v52, v55, v52\n"
        "v_add_u32 v53, v60, v53\n"
        "s_sub_u32 s73, s73, 1\n"
        "s_cbranch_scc0 .Lrg_m\n"
        "s_branch .Lrg_done\n"
        ".Lrg_me0:\n"
        "s_add_u32 s80, s82, .Lrg_m1-.Lrg_base\n"
        "s_branch .Lrg_flush\n"
        ".Lrg_me1:\n"
        "s_add_u32 s80, s82, .Lrg_m2-.Lrg_base\n"
        "s_branch .Lrg_flush\n"
        ".Lrg_me2:\n"
        "s_add_u32 s80, s82, .Lrg_m3-.Lrg_base\n"
        "s_branch .Lrg_flush\n"
        ".Lrg_me3:\n"
        "s_add_u32 s80, s82, .Lrg_m4-.Lrg_base\n"
        "s_branch .Lrg_flush\n"
        ".Lrg_me4:\n"
        "s_add_u32 s80, s82, .Lrg_m5-.Lrg_base\n"
        "s_branch .Lrg_flush\n"
        ".Lrg_me5:\n"
        "s_add_u32 s80, s82, .Lrg_m6-.Lrg_base\n"
        "s_branch .Lrg_flush\n"
        ".Lrg_me6:\n"
        "s_add_u32 s80, s82, .Lrg_m7-.Lrg_base\n"
        "s_branch .Lrg_flush\n"
        ".Lrg_me7:\n"
        "s_add_u32 s80, s82, .Lrg_m8-.Lrg_base\n"
        ".Lrg_flush:\n"
        "s_addc_u32 s81, s83, 0\n"
        "v_readlane_b32 s77, v6, s75\n"
        "s_add_u32 s13, s13, 1\n"
        "s_add_u32 s75, s75, 1\n"
        "v_readlane_b32 s74, v50, s13\n"
        "s_cmp_ge_u32 s75, 9\n"
        "v_mul_f32 v8, s77, v56\n"
        "v_mul_f32 v9, s77, v57\n"
        "v_mul_f32 v10, s77, v58\n"
        "v_mul_f32 v11, s77, v59\n"
        "v_cvt_pk_bf16_f32 v8, v8, v9\n"
        "v_cvt_pk_bf16_f32 v9, v10, v11\n"
        "s_cbranch_scc1 .Lrg_last\n"
        "v_mov_b64 v[56:57], 0\n"
        "v_mov_b64 v[58:59], 0\n"
        "ds_write_b64 v51, v[8:9]\n"
        "ds_add_u32 v43, v3 offset:45440\n"
        "v_add_u32 v51, 2176, v51\n"
        "v_add_u32 v43, 4, v43\n"
        "s_setpc_b64 s[80:81]\n"
        ".Lrg_last:\n"
        "ds_write2_b32 v42, v8, v9 offset1:68\n"
        "ds_add_u32 v43, v3 offset:45440\n"
        "s_cmp_ge_u32 s75, 10\n"
        "v_mov_b64 v[56:57], 0\n"
        "v_mov_b64 v[58:59], 0\n"
        "v_add_u32 v42, 2176, v42\n"
        "v_add_u32 v43, 4, v43\n"
        "s_cbranch_scc1 .Lrg_done\n"
        "s_setpc_b64 s[80:81]\n"
        ".Lrg_done:\n"
        ".Lend_l:\n"
        "s_endpgm\n"
        ".Lpre_a:\n"
        "v_bfe_u32 v5, v0, 5, 3\n"
        "v_and_b32 v7, 31, v0\n"
        "v_xor_b32 v7, v7, v5\n"
        "v_lshlrev_b32 v5, 9, v5\n"
        "v_lshl_or_b32 v6, v7, 4, v5\n"
        "v_xor_b32 v7, 0x80, v6\n"
        "s_lshl_b32 s73, s72, 9\n"
        "s_lshl_b32 s76, s72, 10\n"
        "s_add_u32 m0, s73, 21760\n"
        "s_mul_i32 s74, s2, 640\n"
        "s_mul_i32 s75, s72, 160\n"
        "s_add_u32 s74, s74, s75\n"
        "s_mul_i32 s75, s2, 20480\n"
        "s_barrier\n"
        "s_waitcnt lgkmcnt(0)\n"
        "s_load_dwordx2 s[64:65], s[44:45], 0x0\n"
        "buffer_load_dword v8, s[36:39], s74 offen lds\n"
        "s_add_u32 m0, m0, 256\n"
        "s_nop 0\n"
        "buffer_load_dword v8, s[40:43], s74 offen lds\n"
        "s_add_u32 m0, s76, 24064\n"
        "s_mov_b32 s78, m0\n"
        "s_lshr_b32 s73, s2, 3\n"
        "s_lshl_b32 s73, s73, 12\n"
        "s_add_u32 s73, s73, s76\n"
        "v_lshlrev_b32 v9, 7, v1\n"
        "s_mov_b32 m0, 23808\n"
        "s_mov_b64 exec, 0x1ff\n"
        "buffer_load_dword v9, s[16:19], s73 offen lds\n"
        "s_lshr_b32 s73, s73, 2\n"
        "s_mov_b64 exec, 3\n"
        "buffer_load_dword v9, s[8:11], s73 offen lds\n"
        "s_mov_b64 exec, -1\n"
        "s_mov_b32 m0, s78\n"
        "s_nop 0\n"
        "buffer_load_dwordx4 v6, s[4:7], s75 offen lds\n"
        "s_add_u32 m0, m0, 0x1000\n"
        "s_add_u32 s75, s75, 0x1000\n"
        "buffer_load_dwordx4 v7, s[4:7], s75 offen lds\n"
        "s_add_u32 m0, m0, 0x1000\n"
        "s_add_u32 s75, s75, 0x1000\n"
        "buffer_load_dwordx4 v6, s[4:7], s75 offen lds\n"
        "s_add_u32 m0, m0, 0x1000\n"
        "s_add_u32 s75, s75, 0x1000\n"
        "buffer_load_dwordx4 v7, s[4:7], s75 offen lds\n"
        "s_add_u32 m0, m0, 0x1000\n"
        "s_add_u32 s75, s75, 0x1000\n"
        "buffer_load_dwordx4 v6, s[4:7], s75 offen lds\n"
        ".Lrole_a:\n"
        "s_bitcmp0_b32 s55, 0\n"
        "s_cbranch_scc1 .Lexit\n"
        "s_memrealtime s[38:39]\n"
        "s_cmp_ge_u32 s2, s56\n"
        "s_cbranch_scc1 .Lno_tile\n"
        "s_sub_u32 s73, s83, 128\n"
        "s_cmp_lt_u32 s73, 72\n"
        "s_cbranch_scc0 .Lmh\n"
        "ds_read_b32 v28, v2 offset:45516\n"
        "s_waitcnt lgkmcnt(0)\n"
        "v_readfirstlane_b32 s73, v28\n"
        "s_cmp_ge_u32 s73, 4\n"
        "s_cbranch_scc0 .Lw_15\n"
        ".Le_16:\n"
        ".Lmh:\n"
        "v_bfe_u32 v6, v1, 3, 1\n"
        "v_lshlrev_b32 v6, 14, v6\n"
        "v_lshrrev_b32 v7, 4, v1\n"
        "v_lshl_or_b32 v6, v7, 12, v6\n"
        "v_and_b32 v7, 1, v1\n"
        "v_lshl_or_b32 v6, v7, 6, v6\n"
        "v_and_b32 v7, 4, v1\n"
        "v_lshl_or_b32 v6, v7, 3, v6\n"
        "v_and_b32 v7, 2, v1\n"
        "v_lshl_or_b32 v6, v7, 3, v6\n"
        "s_lshl_b32 s73, s72, 7\n"
        "v_add_u32 v5, s73, v6\n"
        "s_mov_b32 s32, 0x8000\n"
        "s_mov_b32 s33, 0x10000\n"
        "s_mov_b32 s34, 0x18000\n"
        "buffer_load_dwordx4 v[76:79], v5, s[16:19], 0 offen offset:0\n"
        "buffer_load_dwordx4 v[80:83], v5, s[16:19], 0 offen offset:512\n"
        "buffer_load_dwordx4 v[84:87], v5, s[16:19], 0 offen offset:1024\n"
        "buffer_load_dwordx4 v[88:91], v5, s[16:19], 0 offen offset:1536\n"
        "buffer_load_dwordx4 v[92:95], v5, s[16:19], 0 offen offset:2048\n"
        "buffer_load_dwordx4 v[96:99], v5, s[16:19], 0 offen offset:2560\n"
        "buffer_load_dwordx4 v[100:103], v5, s[16:19], 0 offen offset:3072\n"
        "buffer_load_dwordx4 v[104:107], v5, s[16:19], 0 offen offset:3584\n"
        "v_lshrrev_b32 v6, 5, v1\n"
        "s_lshl_b32 s73, s72, 9\n"
        "v_lshl_add_u32 v6, v6, 2, s73\n"
        "v_lshl_add_u32 v7, v1, 2, s73\n"
        "v_add_u32 v6, 21760, v6\n"
        "v_lshrrev_b32 v30, 3, v1\n"
        "v_and_b32 v30, 4, v30\n"
        "v_xor_b32 v30, v30, v1\n"
        "s_and_b32 s40, s72, 1\n"
        "s_mul_i32 s40, s40, 3\n"
        "v_xor_b32 v30, s40, v30\n"
        "v_and_b32 v30, 31, v30\n"
        "v_lshlrev_b32 v200, 4, v30\n"
        "v_mov_b32 v31, v2\n"
        "v_writelane_b32 v31, 4, 1\n"
        "v_xor_b32 v201, 128, v200\n"
        "v_xor_b32 v202, 32, v200\n"
        "v_xor_b32 v203, 160, v200\n"
        "v_xor_b32 v204, 48, v200\n"
        "v_xor_b32 v205, 176, v200\n"
        "v_xor_b32 v206, 16, v200\n"
        "v_xor_b32 v207, 144, v200\n"
        "s_mul_i32 s74, s2, 160\n"
        "s_mul_i32 s75, s72, 40\n"
        "s_add_u32 s74, s74, s75\n"
        "v_add_u32 v29, s74, v1\n"
        "v_lshrrev_b32 v29, 2, v29\n"
        "s_sub_i32 s75, s54, s74\n"
        "s_max_i32 s75, s75, 0\n"
        "s_min_i32 s75, s75, 40\n"
        "s_bfm_b64 s[80:81], s75, 0\n"
        "s_mul_i32 s82, s72, 20480\n"
        "s_add_u32 m0, s82, 45056\n"
        "s_waitcnt vmcnt(8)\n"
        "ds_add_u32 v2, v3 offset:45508\n"
        "ds_read2_b32 v[176:177], v6 offset0:0 offset1:2\n"
        "ds_read2_b32 v[178:179], v6 offset0:4 offset1:6\n"
        "ds_read2_b32 v[180:181], v6 offset0:8 offset1:10\n"
        "ds_read2_b32 v[182:183], v6 offset0:12 offset1:14\n"
        "ds_read2_b32 v[184:185], v6 offset0:16 offset1:18\n"
        "ds_read2_b32 v[186:187], v6 offset0:20 offset1:22\n"
        "ds_read2_b32 v[188:189], v6 offset0:24 offset1:26\n"
        "ds_read2_b32 v[190:191], v6 offset0:28 offset1:30\n"
        "ds_read2_b32 v[192:193], v6 offset0:32 offset1:34\n"
        "ds_read2_b32 v[194:195], v6 offset0:36 offset1:38\n"
        "ds_read_b32 v28, v7 offset:22016\n"
        "s_waitcnt lgkmcnt(0)\n"
        "s_add_u32 s73, s64, 1\n"
        "s_addc_u32 s74, s65, 0\n"
        "s_mul_i32 s76, s73, 0x7f4a7c15\n"
        "s_mul_hi_u32 s77, s73, 0x7f4a7c15\n"
        "s_mul_i32 s75, s73, 0x9e3779b9\n"
        "s_add_u32 s77, s77, s75\n"
        "s_mul_i32 s75, s74, 0x7f4a7c15\n"
        "s_add_u32 s77, s77, s75\n"
        "s_xor_b32 s66, s76, 0xd192ed03\n"
        "s_or_b32 s66, s66, 3\n"
        "s_xor_b32 s67, s77, 0xd1b54a32\n"
        "v_cmp_ne_u32 vcc, v28, v29\n"
        "s_and_b64 s[80:81], s[80:81], vcc\n"
        "s_cselect_b32 s73, 0x101, 1\n"
        "s_bitcmp1_b32 s55, 1\n"
        "s_cselect_b32 s73, s73, 0x101\n"
        "v_mov_b32 v28, s73\n"
        "ds_add_u32 v2, v28 offset:45480\n"
        "s_cmp_lg_u32 s3, 4\n"
        "s_cbranch_scc1 .Lflag_done\n"
        "s_mov_b32 s74, 0x40000\n"
        ".Lep:\n"
        "ds_read_b32 v28, v2 offset:45480\n"
        "s_waitcnt lgkmcnt(0)\n"
        "v_readfirstlane_b32 s73, v28\n"
        "s_and_b32 s75, s73, 255\n"
        "s_cmp_ge_u32 s75, 8\n"
        "s_cbranch_scc1 .Lep_done\n"
        "s_sleep 1\n"
        "s_sub_u32 s74, s74, 1\n"
        "s_cbranch_scc0 .Lep\n"
        ".Lep_done:\n"
        "s_xor_b32 s74, s66, 2\n"
        "s_cmp_gt_u32 s73, 255\n"
        "s_cselect_b32 s35, s74, s66\n"
        "s_lshl_b32 s73, s2, 3\n"
        "s_add_u32 s73, s73, 256\n"
        "v_mov_b32 v28, s35\n"
        "v_mov_b32 v29, s67\n"
        "s_mov_b64 exec, 1\n"
        "buffer_store_dwordx2 v[28:29], v2, s[44:47], s73 offen sc1\n"
        "s_mov_b64 exec, -1\n"
        ".Lflag_done:\n"
        "ds_read_b32 v28, v2 offset:45500\n"
        "s_waitcnt lgkmcnt(0)\n"
        "v_readfirstlane_b32 s73, v28\n"
        "s_cmp_ge_u32 s73, 4\n"
        "s_cbranch_scc0 .Lw_17\n"
        ".Le_18:\n"
        "s_bfe_u32 s57, s2, 0x10003\n"
        "s_add_u32 m0, m0, 0x400\n"
        "v_lshl_or_b32 v176, v176, 9, v200\n"
        "buffer_load_dwordx4 v176, s[4:7], 0 offen lds\n"
        "s_add_u32 m0, m0, 0x400\n"
        "v_lshl_or_b32 v177, v177, 9, v201\n"
        "buffer_load_dwordx4 v177, s[4:7], 0 offen lds\n"
        "s_add_u32 m0, m0, 0x400\n"
        "v_lshl_or_b32 v178, v178, 9, v202\n"
        "buffer_load_dwordx4 v178, s[4:7], 0 offen lds\n"
        "s_add_u32 m0, m0, 0x400\n"
        "v_lshl_or_b32 v179, v179, 9, v203\n"
        "buffer_load_dwordx4 v179, s[4:7], 0 offen lds\n"
        "s_add_u32 m0, m0, 0x400\n"
        "v_lshl_or_b32 v180, v180, 9, v204\n"
        "buffer_load_dwordx4 v180, s[4:7], 0 offen lds\n"
        "s_add_u32 m0, m0, 0x400\n"
        "v_lshl_or_b32 v181, v181, 9, v205\n"
        "buffer_load_dwordx4 v181, s[4:7], 0 offen lds\n"
        "s_cmp_lg_u32 s57, 0\n"
        "s_cbranch_scc1 .Lk_19\n"
        "s_add_u32 m0, m0, 0x400\n"
        "v_lshl_or_b32 v182, v182, 9, v206\n"
        "buffer_load_dwordx4 v182, s[4:7], 0 offen lds\n"
        "s_add_u32 m0, m0, 0x400\n"
        "v_lshl_or_b32 v183, v183, 9, v207\n"
        "buffer_load_dwordx4 v183, s[4:7], 0 offen lds\n"
        "s_add_u32 m0, m0, 0x400\n"
        "v_lshl_or_b32 v184, v184, 9, v200\n"
        "buffer_load_dwordx4 v184, s[4:7], 0 offen lds\n"
        "s_add_u32 m0, m0, 0x400\n"
        "v_lshl_or_b32 v185, v185, 9, v201\n"
        "buffer_load_dwordx4 v185, s[4:7], 0 offen lds\n"
        "s_add_u32 m0, m0, 0x400\n"
        "v_lshl_or_b32 v186, v186, 9, v202\n"
        "buffer_load_dwordx4 v186, s[4:7], 0 offen lds\n"
        "s_add_u32 m0, m0, 0x400\n"
        "v_lshl_or_b32 v187, v187, 9, v203\n"
        "buffer_load_dwordx4 v187, s[4:7], 0 offen lds\n"
        "ds_read_b32 v28, v2 offset:45516\n"
        "s_waitcnt lgkmcnt(0)\n"
        "v_readfirstlane_b32 s73, v28\n"
        "s_cmp_ge_u32 s73, 4\n"
        "s_cbranch_scc0 .Lw_20\n"
        ".Le_21:\n"
        "s_add_u32 m0, m0, 0x400\n"
        "v_lshl_or_b32 v188, v188, 9, v204\n"
        "buffer_load_dwordx4 v188, s[4:7], 0 offen lds\n"
        "s_add_u32 m0, m0, 0x400\n"
        "v_lshl_or_b32 v189, v189, 9, v205\n"
        "buffer_load_dwordx4 v189, s[4:7], 0 offen lds\n"
        ".Lk_19:\n"
        "buffer_load_dwordx4 v[108:111], v5, s[16:19], s32 offen offset:0\n"
        "buffer_load_dwordx4 v[112:115], v5, s[16:19], s32 offen offset:512\n"
        "buffer_load_dwordx4 v[116:119], v5, s[16:19], s32 offen offset:1024\n"
        "buffer_load_dwordx4 v[120:123], v5, s[16:19], s32 offen offset:1536\n"
        "buffer_load_dwordx4 v[124:127], v5, s[16:19], s32 offen offset:2048\n"
        "buffer_load_dwordx4 v[128:131], v5, s[16:19], s32 offen offset:2560\n"
        "buffer_load_dwordx4 v[132:135], v5, s[16:19], s32 offen offset:3072\n"
        "buffer_load_dwordx4 v[136:139], v5, s[16:19], s32 offen offset:3584\n"
        "buffer_load_dwordx4 v[140:143], v5, s[16:19], s33 offen offset:0\n"
        "buffer_load_dwordx4 v[144:147], v5, s[16:19], s33 offen offset:512\n"
        "buffer_load_dwordx4 v[148:151], v5, s[16:19], s33 offen offset:1024\n"
        "buffer_load_dwordx4 v[152:155], v5, s[16:19], s33 offen offset:1536\n"
        "buffer_load_dwordx4 v[156:159], v5, s[16:19], s33 offen offset:2048\n"
        "buffer_load_dwordx4 v[160:163], v5, s[16:19], s33 offen offset:2560\n"
        "buffer_load_dwordx4 v[164:167], v5, s[16:19], s33 offen offset:3072\n"
        "buffer_load_dwordx4 v[168:171], v5, s[16:19], s33 offen offset:3584\n"
        "s_mov_b32 s78, 0x05040100\n"
        "s_mov_b32 s79, 0x07060302\n"
        "s_cmp_eq_u32 s57, 0\n"
        "s_cbranch_scc1 .Ld_22\n"
        "s_waitcnt vmcnt(22)\n"
        ".Ld_22:\n"
        "s_waitcnt vmcnt(30)\n"
        "v_mov_b32 v172, v76\n"
        "v_mov_b32 v173, v77\n"
        "v_mov_b32 v174, v80\n"
        "v_mov_b32 v175, v81\n"
        "v_mov_b32_dpp v76, v78 row_ror:8 row_mask:0xf bank_mask:0xc\n"
        "v_mov_b32_dpp v77, v79 row_ror:8 row_mask:0xf bank_mask:0xc\n"
        "v_mov_b32_dpp v80, v82 row_ror:8 row_mask:0xf bank_mask:0xc\n"
        "v_mov_b32_dpp v81, v83 row_ror:8 row_mask:0xf bank_mask:0xc\n"
        "v_mov_b32_dpp v78, v172 row_ror:8 row_mask:0xf bank_mask:0x3\n"
        "v_mov_b32_dpp v79, v173 row_ror:8 row_mask:0xf bank_mask:0x3\n"
        "v_mov_b32_dpp v82, v174 row_ror:8 row_mask:0xf bank_mask:0x3\n"
        "v_mov_b32_dpp v83, v175 row_ror:8 row_mask:0xf bank_mask:0x3\n"
        "v_perm_b32 v44, v80, v76, s78\n"
        "v_perm_b32 v52, v80, v76, s79\n"
        "v_perm_b32 v60, v81, v77, s78\n"
        "v_perm_b32 v68, v81, v77, s79\n"
        "v_perm_b32 v48, v82, v78, s78\n"
        "v_perm_b32 v56, v82, v78, s79\n"
        "v_perm_b32 v64, v83, v79, s78\n"
        "v_perm_b32 v72, v83, v79, s79\n"
        "v_mov_b32 v172, v84\n"
        "v_mov_b32 v173, v85\n"
        "v_mov_b32 v174, v88\n"
        "v_mov_b32 v175, v89\n"
        "v_mov_b32_dpp v84, v86 row_ror:8 row_mask:0xf bank_mask:0xc\n"
        "v_mov_b32_dpp v85, v87 row_ror:8 row_mask:0xf bank_mask:0xc\n"
        "v_mov_b32_dpp v88, v90 row_ror:8 row_mask:0xf bank_mask:0xc\n"
        "v_mov_b32_dpp v89, v91 row_ror:8 row_mask:0xf bank_mask:0xc\n"
        "v_mov_b32_dpp v86, v172 row_ror:8 row_mask:0xf bank_mask:0x3\n"
        "v_mov_b32_dpp v87, v173 row_ror:8 row_mask:0xf bank_mask:0x3\n"
        "v_mov_b32_dpp v90, v174 row_ror:8 row_mask:0xf bank_mask:0x3\n"
        "v_mov_b32_dpp v91, v175 row_ror:8 row_mask:0xf bank_mask:0x3\n"
        "v_perm_b32 v45, v88, v84, s78\n"
        "v_perm_b32 v53, v88, v84, s79\n"
        "v_perm_b32 v61, v89, v85, s78\n"
        "v_perm_b32 v69, v89, v85, s79\n"
        "v_perm_b32 v49, v90, v86, s78\n"
        "v_perm_b32 v57, v90, v86, s79\n"
        "v_perm_b32 v65, v91, v87, s78\n"
        "v_perm_b32 v73, v91, v87, s79\n"
        "v_mov_b32 v172, v92\n"
        "v_mov_b32 v173, v93\n"
        "v_mov_b32 v174, v96\n"
        "v_mov_b32 v175, v97\n"
        "v_mov_b32_dpp v92, v94 row_ror:8 row_mask:0xf bank_mask:0xc\n"
        "v_mov_b32_dpp v93, v95 row_ror:8 row_mask:0xf bank_mask:0xc\n"
        "v_mov_b32_dpp v96, v98 row_ror:8 row_mask:0xf bank_mask:0xc\n"
        "v_mov_b32_dpp v97, v99 row_ror:8 row_mask:0xf bank_mask:0xc\n"
        "v_mov_b32_dpp v94, v172 row_ror:8 row_mask:0xf bank_mask:0x3\n"
        "v_mov_b32_dpp v95, v173 row_ror:8 row_mask:0xf bank_mask:0x3\n"
        "v_mov_b32_dpp v98, v174 row_ror:8 row_mask:0xf bank_mask:0x3\n"
        "v_mov_b32_dpp v99, v175 row_ror:8 row_mask:0xf bank_mask:0x3\n"
        "v_perm_b32 v46, v96, v92, s78\n"
        "v_perm_b32 v54, v96, v92, s79\n"
        "v_perm_b32 v62, v97, v93, s78\n"
        "v_perm_b32 v70, v97, v93, s79\n"
        "v_perm_b32 v50, v98, v94, s78\n"
        "v_perm_b32 v58, v98, v94, s79\n"
        "v_perm_b32 v66, v99, v95, s78\n"
        "v_perm_b32 v74, v99, v95, s79\n"
        "v_mov_b32 v172, v100\n"
        "v_mov_b32 v173, v101\n"
        "v_mov_b32 v174, v104\n"
        "v_mov_b32 v175, v105\n"
        "v_mov_b32_dpp v100, v102 row_ror:8 row_mask:0xf bank_mask:0xc\n"
        "v_mov_b32_dpp v101, v103 row_ror:8 row_mask:0xf bank_mask:0xc\n"
        "v_mov_b32_dpp v104, v106 row_ror:8 row_mask:0xf bank_mask:0xc\n"
        "v_mov_b32_dpp v105, v107 row_ror:8 row_mask:0xf bank_mask:0xc\n"
        "v_mov_b32_dpp v102, v172 row_ror:8 row_mask:0xf bank_mask:0x3\n"
        "v_mov_b32_dpp v103, v173 row_ror:8 row_mask:0xf bank_mask:0x3\n"
        "v_mov_b32_dpp v106, v174 row_ror:8 row_mask:0xf bank_mask:0x3\n"
        "v_mov_b32_dpp v107, v175 row_ror:8 row_mask:0xf bank_mask:0x3\n"
        "v_perm_b32 v47, v104, v100, s78\n"
        "v_perm_b32 v55, v104, v100, s79\n"
        "v_perm_b32 v63, v105, v101, s78\n"
        "v_perm_b32 v71, v105, v101, s79\n"
        "v_perm_b32 v51, v106, v102, s78\n"
        "v_perm_b32 v59, v106, v102, s79\n"
        "v_perm_b32 v67, v107, v103, s78\n"
        "v_perm_b32 v75, v107, v103, s79\n"
        "s_mov_b32 s73, s82\n"
        "v_and_b32 v8, 15, v1\n"
        "v_lshrrev_b32 v9, 4, v1\n"
        "v_xor_b32 v9, v9, v8\n"
        "v_and_b32 v9, 3, v9\n"
        "v_and_or_b32 v9, v1, 12, v9\n"
        "v_lshlrev_b32 v8, 9, v8\n"
        "v_lshl_add_u32 v36, v9, 4, v8\n"
        "v_add_u32 v36, 24064, v36\n"
        "v_xor_b32 v37, 64, v36\n"
        "v_xor_b32 v38, 128, v36\n"
        "v_xor_b32 v39, 192, v36\n"
        "v_mov_b64 v[12:13], 0\n"
        "s_lshl_b32 s74, s72, 9\n"
        "v_lshl_add_u32 v8, v1, 3, s74\n"
        "ds_write_b64 v8, v[12:13] offset:21760\n"
        "v_and_b32 v8, 15, v1\n"
        "v_lshrrev_b32 v9, 4, v1\n"
        "v_mov_b32 v5, v4\n"
        "s_movk_i32 s43, 256\n"
        "s_mov_b32 s61, 1\n"
        "v_lshrrev_b32 v10, 2, v8\n"
        "v_lshrrev_b32 v11, 3, v8\n"
        "v_xor_b32 v10, v10, v11\n"
        "v_and_b32 v10, 1, v10\n"
        "v_lshl_or_b32 v10, v10, 1, v11\n"
        "v_and_b32 v11, 3, v8\n"
        "v_lshl_or_b32 v10, v11, 2, v10\n"
        "v_xor_b32 v10, s40, v10\n"
        "v_xor_b32 v10, v10, v9\n"
        "s_add_u32 s73, s73, 46080\n"
        "v_lshl_add_u32 v12, v8, 9, s73\n"
        "v_lshl_add_u32 v6, v10, 4, v12\n"
        "v_mul_u32_u24 v10, 528, v8\n"
        "v_lshl_add_u32 v23, v9, 4, v10\n"
        "s_mul_i32 s74, s72, 5280\n"
        "v_add_u32 v7, s74, v23\n"
        "s_lshl_b32 s73, s72, 7\n"
        "v_add_u32 v41, s73, v23\n"
        "s_mul_i32 s74, s72, 160\n"
        "v_lshl_add_u32 v10, v8, 4, s74\n"
        "v_and_b32 v12, 1, v8\n"
        "v_lshl_add_u32 v40, v12, 3, v10\n"
        "v_and_b32 v10, 12, v8\n"
        "v_and_b32 v12, 1, v9\n"
        "v_mad_u32_u24 v10, v12, 3, v10\n"
        "s_lshl_b32 s74, s72, 3\n"
        "v_add_u32 v11, s74, v11\n"
        "v_xor_b32 v10, v10, v11\n"
        "v_lshrrev_b32 v11, 2, v8\n"
        "v_lshl_add_u32 v11, v9, 3, v11\n"
        "v_lshlrev_b32 v11, 9, v11\n"
        "v_lshl_add_u32 v11, v10, 4, v11\n"
        "v_add_u32 v20, 46080, v11\n"
        "v_lshrrev_b32 v10, 1, v8\n"
        "v_xor_b32 v10, v10, v9\n"
        "v_lshlrev_b32 v11, 4, v8\n"
        "v_mov_b32 v12, 768\n"
        "v_cmp_eq_u32 s[74:75], 0, v10\n"
        "v_cmp_eq_u32 s[76:77], 4, v10\n"
        "s_nop 0\n"
        "v_cndmask_b32 v21, v12, v11, s[74:75]\n"
        "v_cndmask_b32 v22, v12, v11, s[76:77]\n"
        "v_mul_u32_u24 v10, 544, v8\n"
        "v_lshl_add_u32 v10, v9, 4, v10\n"
        "v_add_u32 v34, 0, v10\n"
        "v_and_b32 v10, 1, v9\n"
        "v_lshlrev_b32 v10, 5, v10\n"
        "v_lshl_or_b32 v10, v8, 9, v10\n"
        "v_lshrrev_b32 v11, 1, v9\n"
        "v_lshl_or_b32 v35, v11, 4, v10\n"
        "v_and_b32 v10, 1, v8\n"
        "v_mul_u32_u24 v10, 0xaaaa, v10\n"
        "v_add_u32 v0, 0x4444, v10\n"
        "s_lshl_b32 s8, s72, 15\n"
        "s_add_u32 s8, s8, 8192\n"
        "s_add_u32 s9, s8, 4096\n"
        "s_add_u32 s10, s8, 8192\n"
        "s_add_u32 s11, s8, 12288\n"
        "s_add_u32 s12, s8, 16384\n"
        "s_add_u32 s13, s8, 20480\n"
        "s_add_u32 s14, s8, 24576\n"
        "s_add_u32 s15, s8, 28672\n"
        "s_lshl_b32 s16, s72, 16\n"
        "s_add_u32 s16, s16, 139264\n"
        "s_lshl_b32 s73, s72, 9\n"
        "s_add_u32 s73, s73, 2304\n"
        "s_lshl_b32 s74, s72, 6\n"
        "s_add_u32 s74, s74, 3840\n"
        "v_mov_b32 v42, s73\n"
        "v_mov_b32 v43, s74\n"
        "v_cmp_gt_u32 s[76:77], 32, v1\n"
        "s_nop 1\n"
        "v_cndmask_b32 v42, v43, v42, s[76:77]\n"
        "v_add_u32 v42, v42, v4\n"
        ".Lpda:\n"
        "s_memrealtime s[36:37]\n"
        "s_waitcnt lgkmcnt(0)\n"
        "s_sub_u32 s73, s36, s38\n"
        "s_cmp_ge_u32 s73, 200\n"
        "s_cbranch_scc1 .Lpoll\n"
        "s_sleep 2\n"
        "s_branch .Lpda\n"
        ".Lpoll:\n"
        "s_setprio 3\n"
        "s_lshl_b32 s41, s72, 9\n"
        "s_add_u32 s41, s41, 2304\n"
        "s_mov_b32 exec_hi, 0\n"
        "buffer_load_dwordx4 v[24:27], v4, s[44:47], s41 offen sc1\n"
        "s_mov_b32 exec_hi, -1\n"
        "buffer_load_dwordx4 v[224:227], v4, s[44:47], s8 offen offset:0 sc1\n"
        "buffer_load_dwordx4 v[228:231], v4, s[44:47], s10 offen offset:0 sc1\n"
        "buffer_load_dwordx4 v[232:235], v4, s[44:47], s12 offen offset:0 sc1\n"
        "buffer_load_dwordx4 v[236:239], v4, s[44:47], s14 offen offset:0 sc1\n"
        "buffer_load_dwordx4 v[240:243], v4, s[44:47], s8 offen offset:1024 sc1\n"
        "buffer_load_dwordx4 v[244:247], v4, s[44:47], s10 offen offset:1024 sc1\n"
        "buffer_load_dwordx4 v[248:251], v4, s[44:47], s12 offen offset:1024 sc1\n"
        "buffer_load_dwordx4 v[252:255], v4, s[44:47], s14 offen offset:1024 sc1\n"
        "s_mov_b32 s78, 0x05040100\n"
        "s_mov_b32 s79, 0x07060302\n"
        "s_waitcnt vmcnt(17)\n"
        "v_mov_b32 v172, v108\n"
        "v_mov_b32 v173, v109\n"
        "v_mov_b32 v174, v112\n"
        "v_mov_b32 v175, v113\n"
        "v_mov_b32_dpp v108, v110 row_ror:8 row_mask:0xf bank_mask:0xc\n"
        "v_mov_b32_dpp v109, v111 row_ror:8 row_mask:0xf bank_mask:0xc\n"
        "v_mov_b32_dpp v112, v114 row_ror:8 row_mask:0xf bank_mask:0xc\n"
        "v_mov_b32_dpp v113, v115 row_ror:8 row_mask:0xf bank_mask:0xc\n"
        "v_mov_b32_dpp v110, v172 row_ror:8 row_mask:0xf bank_mask:0x3\n"
        "v_mov_b32_dpp v111, v173 row_ror:8 row_mask:0xf bank_mask:0x3\n"
        "v_mov_b32_dpp v114, v174 row_ror:8 row_mask:0xf bank_mask:0x3\n"
        "v_mov_b32_dpp v115, v175 row_ror:8 row_mask:0xf bank_mask:0x3\n"
        "v_perm_b32 v76, v112, v108, s78\n"
        "v_perm_b32 v84, v112, v108, s79\n"
        "v_perm_b32 v92, v113, v109, s78\n"
        "v_perm_b32 v100, v113, v109, s79\n"
        "v_perm_b32 v80, v114, v110, s78\n"
        "v_perm_b32 v88, v114, v110, s79\n"
        "v_perm_b32 v96, v115, v111, s78\n"
        "v_perm_b32 v104, v115, v111, s79\n"
        "v_mov_b32 v172, v116\n"
        "v_mov_b32 v173, v117\n"
        "v_mov_b32 v174, v120\n"
        "v_mov_b32 v175, v121\n"
        "v_mov_b32_dpp v116, v118 row_ror:8 row_mask:0xf bank_mask:0xc\n"
        "v_mov_b32_dpp v117, v119 row_ror:8 row_mask:0xf bank_mask:0xc\n"
        "v_mov_b32_dpp v120, v122 row_ror:8 row_mask:0xf bank_mask:0xc\n"
        "v_mov_b32_dpp v121, v123 row_ror:8 row_mask:0xf bank_mask:0xc\n"
        "v_mov_b32_dpp v118, v172 row_ror:8 row_mask:0xf bank_mask:0x3\n"
        "v_mov_b32_dpp v119, v173 row_ror:8 row_mask:0xf bank_mask:0x3\n"
        "v_mov_b32_dpp v122, v174 row_ror:8 row_mask:0xf bank_mask:0x3\n"
        "v_mov_b32_dpp v123, v175 row_ror:8 row_mask:0xf bank_mask:0x3\n"
        "v_perm_b32 v77, v120, v116, s78\n"
        "v_perm_b32 v85, v120, v116, s79\n"
        "v_perm_b32 v93, v121, v117, s78\n"
        "v_perm_b32 v101, v121, v117, s79\n"
        "v_perm_b32 v81, v122, v118, s78\n"
        "v_perm_b32 v89, v122, v118, s79\n"
        "v_perm_b32 v97, v123, v119, s78\n"
        "v_perm_b32 v105, v123, v119, s79\n"
        "v_mov_b32 v172, v124\n"
        "v_mov_b32 v173, v125\n"
        "v_mov_b32 v174, v128\n"
        "v_mov_b32 v175, v129\n"
        "v_mov_b32_dpp v124, v126 row_ror:8 row_mask:0xf bank_mask:0xc\n"
        "v_mov_b32_dpp v125, v127 row_ror:8 row_mask:0xf bank_mask:0xc\n"
        "v_mov_b32_dpp v128, v130 row_ror:8 row_mask:0xf bank_mask:0xc\n"
        "v_mov_b32_dpp v129, v131 row_ror:8 row_mask:0xf bank_mask:0xc\n"
        "v_mov_b32_dpp v126, v172 row_ror:8 row_mask:0xf bank_mask:0x3\n"
        "v_mov_b32_dpp v127, v173 row_ror:8 row_mask:0xf bank_mask:0x3\n"
        "v_mov_b32_dpp v130, v174 row_ror:8 row_mask:0xf bank_mask:0x3\n"
        "v_mov_b32_dpp v131, v175 row_ror:8 row_mask:0xf bank_mask:0x3\n"
        "v_perm_b32 v78, v128, v124, s78\n"
        "v_perm_b32 v86, v128, v124, s79\n"
        "v_perm_b32 v94, v129, v125, s78\n"
        "v_perm_b32 v102, v129, v125, s79\n"
        "v_perm_b32 v82, v130, v126, s78\n"
        "v_perm_b32 v90, v130, v126, s79\n"
        "v_perm_b32 v98, v131, v127, s78\n"
        "v_perm_b32 v106, v131, v127, s79\n"
        "v_mov_b32 v172, v132\n"
        "v_mov_b32 v173, v133\n"
        "v_mov_b32 v174, v136\n"
        "v_mov_b32 v175, v137\n"
        "v_mov_b32_dpp v132, v134 row_ror:8 row_mask:0xf bank_mask:0xc\n"
        "v_mov_b32_dpp v133, v135 row_ror:8 row_mask:0xf bank_mask:0xc\n"
        "v_mov_b32_dpp v136, v138 row_ror:8 row_mask:0xf bank_mask:0xc\n"
        "v_mov_b32_dpp v137, v139 row_ror:8 row_mask:0xf bank_mask:0xc\n"
        "v_mov_b32_dpp v134, v172 row_ror:8 row_mask:0xf bank_mask:0x3\n"
        "v_mov_b32_dpp v135, v173 row_ror:8 row_mask:0xf bank_mask:0x3\n"
        "v_mov_b32_dpp v138, v174 row_ror:8 row_mask:0xf bank_mask:0x3\n"
        "v_mov_b32_dpp v139, v175 row_ror:8 row_mask:0xf bank_mask:0x3\n"
        "v_perm_b32 v79, v136, v132, s78\n"
        "v_perm_b32 v87, v136, v132, s79\n"
        "v_perm_b32 v95, v137, v133, s78\n"
        "v_perm_b32 v103, v137, v133, s79\n"
        "v_perm_b32 v83, v138, v134, s78\n"
        "v_perm_b32 v91, v138, v134, s79\n"
        "v_perm_b32 v99, v139, v135, s78\n"
        "v_perm_b32 v107, v139, v135, s79\n"
        "s_waitcnt vmcnt(9)\n"
        "v_mov_b32 v172, v140\n"
        "v_mov_b32 v173, v141\n"
        "v_mov_b32 v174, v144\n"
        "v_mov_b32 v175, v145\n"
        "v_mov_b32_dpp v140, v142 row_ror:8 row_mask:0xf bank_mask:0xc\n"
        "v_mov_b32_dpp v141, v143 row_ror:8 row_mask:0xf bank_mask:0xc\n"
        "v_mov_b32_dpp v144, v146 row_ror:8 row_mask:0xf bank_mask:0xc\n"
        "v_mov_b32_dpp v145, v147 row_ror:8 row_mask:0xf bank_mask:0xc\n"
        "v_mov_b32_dpp v142, v172 row_ror:8 row_mask:0xf bank_mask:0x3\n"
        "v_mov_b32_dpp v143, v173 row_ror:8 row_mask:0xf bank_mask:0x3\n"
        "v_mov_b32_dpp v146, v174 row_ror:8 row_mask:0xf bank_mask:0x3\n"
        "v_mov_b32_dpp v147, v175 row_ror:8 row_mask:0xf bank_mask:0x3\n"
        "v_perm_b32 v108, v144, v140, s78\n"
        "v_perm_b32 v116, v144, v140, s79\n"
        "v_perm_b32 v124, v145, v141, s78\n"
        "v_perm_b32 v132, v145, v141, s79\n"
        "v_perm_b32 v112, v146, v142, s78\n"
        "v_perm_b32 v120, v146, v142, s79\n"
        "v_perm_b32 v128, v147, v143, s78\n"
        "v_perm_b32 v136, v147, v143, s79\n"
        "v_mov_b32 v172, v148\n"
        "v_mov_b32 v173, v149\n"
        "v_mov_b32 v174, v152\n"
        "v_mov_b32 v175, v153\n"
        "v_mov_b32_dpp v148, v150 row_ror:8 row_mask:0xf bank_mask:0xc\n"
        "v_mov_b32_dpp v149, v151 row_ror:8 row_mask:0xf bank_mask:0xc\n"
        "v_mov_b32_dpp v152, v154 row_ror:8 row_mask:0xf bank_mask:0xc\n"
        "v_mov_b32_dpp v153, v155 row_ror:8 row_mask:0xf bank_mask:0xc\n"
        "v_mov_b32_dpp v150, v172 row_ror:8 row_mask:0xf bank_mask:0x3\n"
        "v_mov_b32_dpp v151, v173 row_ror:8 row_mask:0xf bank_mask:0x3\n"
        "v_mov_b32_dpp v154, v174 row_ror:8 row_mask:0xf bank_mask:0x3\n"
        "v_mov_b32_dpp v155, v175 row_ror:8 row_mask:0xf bank_mask:0x3\n"
        "v_perm_b32 v109, v152, v148, s78\n"
        "v_perm_b32 v117, v152, v148, s79\n"
        "v_perm_b32 v125, v153, v149, s78\n"
        "v_perm_b32 v133, v153, v149, s79\n"
        "v_perm_b32 v113, v154, v150, s78\n"
        "v_perm_b32 v121, v154, v150, s79\n"
        "v_perm_b32 v129, v155, v151, s78\n"
        "v_perm_b32 v137, v155, v151, s79\n"
        "v_mov_b32 v172, v156\n"
        "v_mov_b32 v173, v157\n"
        "v_mov_b32 v174, v160\n"
        "v_mov_b32 v175, v161\n"
        "v_mov_b32_dpp v156, v158 row_ror:8 row_mask:0xf bank_mask:0xc\n"
        "v_mov_b32_dpp v157, v159 row_ror:8 row_mask:0xf bank_mask:0xc\n"
        "v_mov_b32_dpp v160, v162 row_ror:8 row_mask:0xf bank_mask:0xc\n"
        "v_mov_b32_dpp v161, v163 row_ror:8 row_mask:0xf bank_mask:0xc\n"
        "v_mov_b32_dpp v158, v172 row_ror:8 row_mask:0xf bank_mask:0x3\n"
        "v_mov_b32_dpp v159, v173 row_ror:8 row_mask:0xf bank_mask:0x3\n"
        "v_mov_b32_dpp v162, v174 row_ror:8 row_mask:0xf bank_mask:0x3\n"
        "v_mov_b32_dpp v163, v175 row_ror:8 row_mask:0xf bank_mask:0x3\n"
        "v_perm_b32 v110, v160, v156, s78\n"
        "v_perm_b32 v118, v160, v156, s79\n"
        "v_perm_b32 v126, v161, v157, s78\n"
        "v_perm_b32 v134, v161, v157, s79\n"
        "v_perm_b32 v114, v162, v158, s78\n"
        "v_perm_b32 v122, v162, v158, s79\n"
        "v_perm_b32 v130, v163, v159, s78\n"
        "v_perm_b32 v138, v163, v159, s79\n"
        "v_mov_b32 v172, v164\n"
        "v_mov_b32 v173, v165\n"
        "v_mov_b32 v174, v168\n"
        "v_mov_b32 v175, v169\n"
        "v_mov_b32_dpp v164, v166 row_ror:8 row_mask:0xf bank_mask:0xc\n"
        "v_mov_b32_dpp v165, v167 row_ror:8 row_mask:0xf bank_mask:0xc\n"
        "v_mov_b32_dpp v168, v170 row_ror:8 row_mask:0xf bank_mask:0xc\n"
        "v_mov_b32_dpp v169, v171 row_ror:8 row_mask:0xf bank_mask:0xc\n"
        "v_mov_b32_dpp v166, v172 row_ror:8 row_mask:0xf bank_mask:0x3\n"
        "v_mov_b32_dpp v167, v173 row_ror:8 row_mask:0xf bank_mask:0x3\n"
        "v_mov_b32_dpp v170, v174 row_ror:8 row_mask:0xf bank_mask:0x3\n"
        "v_mov_b32_dpp v171, v175 row_ror:8 row_mask:0xf bank_mask:0x3\n"
        "v_perm_b32 v111, v168, v164, s78\n"
        "v_perm_b32 v119, v168, v164, s79\n"
        "v_perm_b32 v127, v169, v165, s78\n"
        "v_perm_b32 v135, v169, v165, s79\n"
        "v_perm_b32 v115, v170, v166, s78\n"
        "v_perm_b32 v123, v170, v166, s79\n"
        "v_perm_b32 v131, v171, v167, s78\n"
        "v_perm_b32 v139, v171, v167, s79\n"
        "buffer_load_dwordx4 v[156:159], v4, s[44:47], s8 offen offset:2048 sc1\n"
        "buffer_load_dwordx4 v[160:163], v4, s[44:47], s10 offen offset:2048 sc1\n"
        "buffer_load_dwordx4 v[164:167], v4, s[44:47], s12 offen offset:2048 sc1\n"
        "buffer_load_dwordx4 v[168:171], v4, s[44:47], s14 offen offset:2048 sc1\n"
        "buffer_load_dwordx4 v[140:143], v4, s[44:47], s8 offen offset:3072 sc1\n"
        "buffer_load_dwordx4 v[144:147], v4, s[44:47], s10 offen offset:3072 sc1\n"
        "buffer_load_dwordx4 v[148:151], v4, s[44:47], s12 offen offset:3072 sc1\n"
        "buffer_load_dwordx4 v[152:155], v4, s[44:47], s14 offen offset:3072 sc1\n"
        "ds_add_u32 v2, v3 offset:45520\n"
        "s_setprio 3\n"
        "ds_read_b32 v28, v2 offset:45516\n"
        "s_waitcnt lgkmcnt(0)\n"
        "v_readfirstlane_b32 s73, v28\n"
        "s_cmp_ge_u32 s73, 4\n"
        "s_cbranch_scc0 .Lw_23\n"
        ".Le_24:\n"
        "s_cmp_lg_u32 s57, 1\n"
        "s_cbranch_scc1 .Lk_25\n"
        "s_add_u32 m0, m0, 0x400\n"
        "v_lshl_or_b32 v182, v182, 9, v206\n"
        "buffer_load_dwordx4 v182, s[4:7], 0 offen lds\n"
        "s_add_u32 m0, m0, 0x400\n"
        "v_lshl_or_b32 v183, v183, 9, v207\n"
        "buffer_load_dwordx4 v183, s[4:7], 0 offen lds\n"
        "s_add_u32 m0, m0, 0x400\n"
        "v_lshl_or_b32 v184, v184, 9, v200\n"
        "buffer_load_dwordx4 v184, s[4:7], 0 offen lds\n"
        "s_add_u32 m0, m0, 0x400\n"
        "v_lshl_or_b32 v185, v185, 9, v201\n"
        "buffer_load_dwordx4 v185, s[4:7], 0 offen lds\n"
        "s_add_u32 m0, m0, 0x400\n"
        "v_lshl_or_b32 v186, v186, 9, v202\n"
        "buffer_load_dwordx4 v186, s[4:7], 0 offen lds\n"
        "s_add_u32 m0, m0, 0x400\n"
        "v_lshl_or_b32 v187, v187, 9, v203\n"
        "buffer_load_dwordx4 v187, s[4:7], 0 offen lds\n"
        "s_add_u32 m0, m0, 0x400\n"
        "v_lshl_or_b32 v188, v188, 9, v204\n"
        "buffer_load_dwordx4 v188, s[4:7], 0 offen lds\n"
        "s_add_u32 m0, m0, 0x400\n"
        "v_lshl_or_b32 v189, v189, 9, v205\n"
        "buffer_load_dwordx4 v189, s[4:7], 0 offen lds\n"
        ".Lk_25:\n"
        "s_add_u32 m0, m0, 0x400\n"
        "v_lshl_or_b32 v190, v190, 9, v206\n"
        "buffer_load_dwordx4 v190, s[4:7], 0 offen lds\n"
        "s_add_u32 m0, m0, 0x400\n"
        "v_lshl_or_b32 v191, v191, 9, v207\n"
        "buffer_load_dwordx4 v191, s[4:7], 0 offen lds\n"
        "s_add_u32 m0, m0, 0x400\n"
        "v_lshl_or_b32 v192, v192, 9, v200\n"
        "buffer_load_dwordx4 v192, s[4:7], 0 offen lds\n"
        "s_add_u32 m0, m0, 0x400\n"
        "v_lshl_or_b32 v193, v193, 9, v201\n"
        "buffer_load_dwordx4 v193, s[4:7], 0 offen lds\n"
        "s_add_u32 m0, m0, 0x400\n"
        "v_lshl_or_b32 v194, v194, 9, v202\n"
        "buffer_load_dwordx4 v194, s[4:7], 0 offen lds\n"
        "s_add_u32 m0, m0, 0x400\n"
        "v_lshl_or_b32 v195, v195, 9, v203\n"
        "buffer_load_dwordx4 v195, s[4:7], 0 offen lds\n"
        "s_setprio 1\n"
        "ds_read_b32 v8, v2 offset:45508\n"
        "s_waitcnt lgkmcnt(0)\n"
        "v_readfirstlane_b32 s73, v8\n"
        "s_cmp_ge_u32 s73, 4\n"
        "s_cbranch_scc0 .Lw_26\n"
        ".Le_27:\n"
        "s_cmp_eq_u32 s57, 1\n"
        "s_cbranch_scc1 .Ld_28\n"
        "s_waitcnt vmcnt(22)\n"
        ".Ld_28:\n"
        "s_waitcnt vmcnt(30)\n"
        "v_cmp_ne_u32 s[74:75], s66, v24\n"
        "v_cmp_ne_u32 vcc, s66, v26\n"
        "s_or_b32 s74, s74, vcc_lo\n"
        "s_cbranch_scc1 .Lhint\n"
        ".Lhint_e:\n"
        "ds_read_b128 v[8:11], v36 offset:0\n"
        "ds_read_b128 v[12:15], v36 offset:8192\n"
        "ds_read_b128 v[16:19], v36 offset:16384\n"
        "s_cmp_eq_u32 s57, 1\n"
        "s_cbranch_scc1 .Ld_29\n"
        "s_waitcnt vmcnt(18)\n"
        ".Ld_29:\n"
        "s_waitcnt vmcnt(26)\n"
        "s_waitcnt lgkmcnt(2)\n"
        "v_mfma_f32_16x16x32_bf16 v[176:179], v[224:227], v[8:11], 0\n"
        "v_add_u32 v43, v224, v225\n"
        "v_mfma_f32_16x16x32_bf16 v[180:183], v[228:231], v[8:11], 0\n"
        "v_add3_u32 v43, v43, v226, v227\n"
        "v_mfma_f32_16x16x32_bf16 v[184:187], v[232:235], v[8:11], 0\n"
        "v_add3_u32 v43, v43, v228, v229\n"
        "v_mfma_f32_16x16x32_bf16 v[188:191], v[236:239], v[8:11], 0\n"
        "v_add3_u32 v43, v43, v230, v231\n"
        "ds_read_b128 v[8:11], v37 offset:0\n"
        "s_waitcnt lgkmcnt(2)\n"
        "v_mfma_f32_16x16x32_bf16 v[192:195], v[224:227], v[12:15], 0\n"
        "v_add3_u32 v43, v43, v232, v233\n"
        "v_mfma_f32_16x16x32_bf16 v[196:199], v[228:231], v[12:15], 0\n"
        "v_add3_u32 v43, v43, v234, v235\n"
        "v_mfma_f32_16x16x32_bf16 v[200:203], v[232:235], v[12:15], 0\n"
        "v_add3_u32 v43, v43, v236, v237\n"
        "v_mfma_f32_16x16x32_bf16 v[204:207], v[236:239], v[12:15], 0\n"
        "v_add3_u32 v43, v43, v238, v239\n"
        "ds_read_b128 v[12:15], v37 offset:8192\n"
        "s_waitcnt lgkmcnt(2)\n"
        "v_mfma_f32_16x16x32_bf16 v[208:211], v[224:227], v[16:19], 0\n"
        "v_mfma_f32_16x16x32_bf16 v[212:215], v[228:231], v[16:19], 0\n"
        "v_mfma_f32_16x16x32_bf16 v[216:219], v[232:235], v[16:19], 0\n"
        "v_mfma_f32_16x16x32_bf16 v[220:223], v[236:239], v[16:19], 0\n"
        "ds_read_b128 v[16:19], v37 offset:16384\n"
        "s_setprio 3\n"
        "buffer_load_dwordx4 v[224:227], v4, s[44:47], s9 offen offset:0 sc1\n"
        "buffer_load_dwordx4 v[228:231], v4, s[44:47], s11 offen offset:0 sc1\n"
        "buffer_load_dwordx4 v[232:235], v4, s[44:47], s13 offen offset:0 sc1\n"
        "buffer_load_dwordx4 v[236:239], v4, s[44:47], s15 offen offset:0 sc1\n"
        "s_setprio 1\n"
        "s_cmp_eq_u32 s57, 1\n"
        "s_cbranch_scc1 .Ld_30\n"
        "s_waitcnt vmcnt(18)\n"
        ".Ld_30:\n"
        "s_waitcnt vmcnt(26)\n"
        "s_waitcnt lgkmcnt(2)\n"
        "v_mfma_f32_16x16x32_bf16 v[176:179], v[240:243], v[8:11], v[176:179]\n"
        "v_add3_u32 v43, v43, v240, v241\n"
        "v_mfma_f32_16x16x32_bf16 v[180:183], v[244:247], v[8:11], v[180:183]\n"
        "v_add3_u32 v43, v43, v242, v243\n"
        "v_mfma_f32_16x16x32_bf16 v[184:187], v[248:251], v[8:11], v[184:187]\n"
        "v_add3_u32 v43, v43, v244, v245\n"
        "v_mfma_f32_16x16x32_bf16 v[188:191], v[252:255], v[8:11], v[188:191]\n"
        "v_add3_u32 v43, v43, v246, v247\n"
        "ds_read_b128 v[8:11], v38 offset:0\n"
        "s_waitcnt lgkmcnt(2)\n"
        "v_mfma_f32_16x16x32_bf16 v[192:195], v[240:243], v[12:15], v[192:195]\n"
        "v_add3_u32 v43, v43, v248, v249\n"
        "v_mfma_f32_16x16x32_bf16 v[196:199], v[244:247], v[12:15], v[196:199]\n"
        "v_add3_u32 v43, v43, v250, v251\n"
        "v_mfma_f32_16x16x32_bf16 v[200:203], v[248:251], v[12:15], v[200:203]\n"
        "v_add3_u32 v43, v43, v252, v253\n"
        "v_mfma_f32_16x16x32_bf16 v[204:207], v[252:255], v[12:15], v[204:207]\n"
        "v_add3_u32 v43, v43, v254, v255\n"
        "ds_read_b128 v[12:15], v38 offset:8192\n"
        "s_waitcnt lgkmcnt(2)\n"
        "v_mfma_f32_16x16x32_bf16 v[208:211], v[240:243], v[16:19], v[208:211]\n"
        "v_mfma_f32_16x16x32_bf16 v[212:215], v[244:247], v[16:19], v[212:215]\n"
        "v_mfma_f32_16x16x32_bf16 v[216:219], v[248:251], v[16:19], v[216:219]\n"
        "v_mfma_f32_16x16x32_bf16 v[220:223], v[252:255], v[16:19], v[220:223]\n"
        "ds_read_b128 v[16:19], v38 offset:16384\n"
        "s_setprio 3\n"
        "buffer_load_dwordx4 v[240:243], v4, s[44:47], s9 offen offset:1024 sc1\n"
        "buffer_load_dwordx4 v[244:247], v4, s[44:47], s11 offen offset:1024 sc1\n"
        "buffer_load_dwordx4 v[248:251], v4, s[44:47], s13 offen offset:1024 sc1\n"
        "buffer_load_dwordx4 v[252:255], v4, s[44:47], s15 offen offset:1024 sc1\n"
        "s_setprio 1\n"
        "s_cmp_eq_u32 s57, 1\n"
        "s_cbranch_scc1 .Ld_31\n"
        "s_waitcnt vmcnt(18)\n"
        ".Ld_31:\n"
        "s_waitcnt vmcnt(26)\n"
        "s_waitcnt lgkmcnt(2)\n"
        "v_mfma_f32_16x16x32_bf16 v[176:179], v[156:159], v[8:11], v[176:179]\n"
        "v_add3_u32 v43, v43, v156, v157\n"
        "v_mfma_f32_16x16x32_bf16 v[180:183], v[160:163], v[8:11], v[180:183]\n"
        "v_add3_u32 v43, v43, v158, v159\n"
        "v_mfma_f32_16x16x32_bf16 v[184:187], v[164:167], v[8:11], v[184:187]\n"
        "v_add3_u32 v43, v43, v160, v161\n"
        "v_mfma_f32_16x16x32_bf16 v[188:191], v[168:171], v[8:11], v[188:191]\n"
        "v_add3_u32 v43, v43, v162, v163\n"
        "ds_read_b128 v[8:11], v39 offset:0\n"
        "s_waitcnt lgkmcnt(2)\n"
        "v_mfma_f32_16x16x32_bf16 v[192:195], v[156:159], v[12:15], v[192:195]\n"
        "v_add3_u32 v43, v43, v164, v165\n"
        "v_mfma_f32_16x16x32_bf16 v[196:199], v[160:163], v[12:15], v[196:199]\n"
        "v_add3_u32 v43, v43, v166, v167\n"
        "v_mfma_f32_16x16x32_bf16 v[200:203], v[164:167], v[12:15], v[200:203]\n"
        "v_add3_u32 v43, v43, v168, v169\n"
        "v_mfma_f32_16x16x32_bf16 v[204:207], v[168:171], v[12:15], v[204:207]\n"
        "v_add3_u32 v43, v43, v170, v171\n"
        "ds_read_b128 v[12:15], v39 offset:8192\n"
        "s_waitcnt lgkmcnt(2)\n"
        "v_mfma_f32_16x16x32_bf16 v[208:211], v[156:159], v[16:19], v[208:211]\n"
        "v_mfma_f32_16x16x32_bf16 v[212:215], v[160:163], v[16:19], v[212:215]\n"
        "v_mfma_f32_16x16x32_bf16 v[216:219], v[164:167], v[16:19], v[216:219]\n"
        "v_mfma_f32_16x16x32_bf16 v[220:223], v[168:171], v[16:19], v[220:223]\n"
        "ds_read_b128 v[16:19], v39 offset:16384\n"
        "s_setprio 3\n"
        "buffer_load_dwordx4 v[156:159], v4, s[44:47], s9 offen offset:2048 sc1\n"
        "buffer_load_dwordx4 v[160:163], v4, s[44:47], s11 offen offset:2048 sc1\n"
        "buffer_load_dwordx4 v[164:167], v4, s[44:47], s13 offen offset:2048 sc1\n"
        "buffer_load_dwordx4 v[168:171], v4, s[44:47], s15 offen offset:2048 sc1\n"
        "s_setprio 1\n"
        "s_cmp_eq_u32 s57, 1\n"
        "s_cbranch_scc1 .Ld_32\n"
        "s_waitcnt vmcnt(18)\n"
        ".Ld_32:\n"
        "s_waitcnt vmcnt(26)\n"
        "s_waitcnt lgkmcnt(2)\n"
        "v_mfma_f32_16x16x32_bf16 v[176:179], v[140:143], v[8:11], v[176:179]\n"
        "v_add3_u32 v43, v43, v140, v141\n"
        "v_mfma_f32_16x16x32_bf16 v[180:183], v[144:147], v[8:11], v[180:183]\n"
        "v_add3_u32 v43, v43, v142, v143\n"
        "v_mfma_f32_16x16x32_bf16 v[184:187], v[148:151], v[8:11], v[184:187]\n"
        "v_add3_u32 v43, v43, v144, v145\n"
        "v_mfma_f32_16x16x32_bf16 v[188:191], v[152:155], v[8:11], v[188:191]\n"
        "v_add3_u32 v43, v43, v146, v147\n"
        "ds_read_b128 v[8:11], v36 offset:256\n"
        "s_waitcnt lgkmcnt(2)\n"
        "v_mfma_f32_16x16x32_bf16 v[192:195], v[140:143], v[12:15], v[192:195]\n"
        "v_add3_u32 v43, v43, v148, v149\n"
        "v_mfma_f32_16x16x32_bf16 v[196:199], v[144:147], v[12:15], v[196:199]\n"
        "v_add3_u32 v43, v43, v150, v151\n"
        "v_mfma_f32_16x16x32_bf16 v[200:203], v[148:151], v[12:15], v[200:203]\n"
        "v_add3_u32 v43, v43, v152, v153\n"
        "v_mfma_f32_16x16x32_bf16 v[204:207], v[152:155], v[12:15], v[204:207]\n"
        "v_add3_u32 v43, v43, v154, v155\n"
        "ds_read_b128 v[12:15], v36 offset:8448\n"
        "s_waitcnt lgkmcnt(2)\n"
        "v_mfma_f32_16x16x32_bf16 v[208:211], v[140:143], v[16:19], v[208:211]\n"
        "v_mfma_f32_16x16x32_bf16 v[212:215], v[144:147], v[16:19], v[212:215]\n"
        "v_mfma_f32_16x16x32_bf16 v[216:219], v[148:151], v[16:19], v[216:219]\n"
        "v_mfma_f32_16x16x32_bf16 v[220:223], v[152:155], v[16:19], v[220:223]\n"
        "ds_read_b128 v[16:19], v36 offset:16640\n"
        "s_setprio 3\n"
        "buffer_load_dwordx4 v[140:143], v4, s[44:47], s9 offen offset:3072 sc1\n"
        "buffer_load_dwordx4 v[144:147], v4, s[44:47], s11 offen offset:3072 sc1\n"
        "buffer_load_dwordx4 v[148:151], v4, s[44:47], s13 offen offset:3072 sc1\n"
        "buffer_load_dwordx4 v[152:155], v4, s[44:47], s15 offen offset:3072 sc1\n"
        "s_setprio 1\n"
        "s_waitcnt vmcnt(12)\n"
        "s_waitcnt lgkmcnt(2)\n"
        "v_mfma_f32_16x16x32_bf16 v[176:179], v[224:227], v[8:11], v[176:179]\n"
        "v_add3_u32 v43, v43, v224, v225\n"
        "v_mfma_f32_16x16x32_bf16 v[180:183], v[228:231], v[8:11], v[180:183]\n"
        "v_add3_u32 v43, v43, v226, v227\n"
        "v_mfma_f32_16x16x32_bf16 v[184:187], v[232:235], v[8:11], v[184:187]\n"
        "v_add3_u32 v43, v43, v228, v229\n"
        "v_mfma_f32_16x16x32_bf16 v[188:191], v[236:239], v[8:11], v[188:191]\n"
        "v_add3_u32 v43, v43, v230, v231\n"
        "ds_read_b128 v[8:11], v37 offset:256\n"
        "s_waitcnt lgkmcnt(2)\n"
        "v_mfma_f32_16x16x32_bf16 v[192:195], v[224:227], v[12:15], v[192:195]\n"
        "v_add3_u32 v43, v43, v232, v233\n"
        "v_mfma_f32_16x16x32_bf16 v[196:199], v[228:231], v[12:15], v[196:199]\n"
        "v_add3_u32 v43, v43, v234, v235\n"
        "v_mfma_f32_16x16x32_bf16 v[200:203], v[232:235], v[12:15], v[200:203]\n"
        "v_add3_u32 v43, v43, v236, v237\n"
        "v_mfma_f32_16x16x32_bf16 v[204:207], v[236:239], v[12:15], v[204:207]\n"
        "v_add3_u32 v43, v43, v238, v239\n"
        "ds_read_b128 v[12:15], v37 offset:8448\n"
        "s_waitcnt lgkmcnt(2)\n"
        "v_mfma_f32_16x16x32_bf16 v[208:211], v[224:227], v[16:19], v[208:211]\n"
        "v_mfma_f32_16x16x32_bf16 v[212:215], v[228:231], v[16:19], v[212:215]\n"
        "v_mfma_f32_16x16x32_bf16 v[216:219], v[232:235], v[16:19], v[216:219]\n"
        "v_mfma_f32_16x16x32_bf16 v[220:223], v[236:239], v[16:19], v[220:223]\n"
        "ds_read_b128 v[16:19], v37 offset:16640\n"
        "s_setprio 3\n"
        "s_mov_b32 exec_hi, 15\n"
        "buffer_load_dwordx4 v[172:175], v42, s[44:47], 0 offen sc1\n"
        "s_mov_b64 exec, -1\n"
        "s_cmp_lg_u32 s3, 4\n"
        "s_cbranch_scc1 .Lsw1\n"
        "buffer_load_dwordx4 v[24:27], v5, s[44:47], s43 offen sc1\n"
        "buffer_load_dwordx4 v[28:31], v5, s[44:47], s43 offen offset:1024 sc1\n"
        ".Lsw1:\n"
        "s_setprio 1\n"
        "s_waitcnt vmcnt(9)\n"
        "s_waitcnt lgkmcnt(2)\n"
        "v_mfma_f32_16x16x32_bf16 v[176:179], v[240:243], v[8:11], v[176:179]\n"
        "v_add3_u32 v43, v43, v240, v241\n"
        "v_mfma_f32_16x16x32_bf16 v[180:183], v[244:247], v[8:11], v[180:183]\n"
        "v_add3_u32 v43, v43, v242, v243\n"
        "v_mfma_f32_16x16x32_bf16 v[184:187], v[248:251], v[8:11], v[184:187]\n"
        "v_add3_u32 v43, v43, v244, v245\n"
        "v_mfma_f32_16x16x32_bf16 v[188:191], v[252:255], v[8:11], v[188:191]\n"
        "v_add3_u32 v43, v43, v246, v247\n"
        "ds_read_b128 v[8:11], v38 offset:256\n"
        "s_waitcnt lgkmcnt(2)\n"
        "v_mfma_f32_16x16x32_bf16 v[192:195], v[240:243], v[12:15], v[192:195]\n"
        "v_add3_u32 v43, v43, v248, v249\n"
        "v_mfma_f32_16x16x32_bf16 v[196:199], v[244:247], v[12:15], v[196:199]\n"
        "v_add3_u32 v43, v43, v250, v251\n"
        "v_mfma_f32_16x16x32_bf16 v[200:203], v[248:251], v[12:15], v[200:203]\n"
        "v_add3_u32 v43, v43, v252, v253\n"
        "v_mfma_f32_16x16x32_bf16 v[204:207], v[252:255], v[12:15], v[204:207]\n"
        "v_add3_u32 v43, v43, v254, v255\n"
        "ds_read_b128 v[12:15], v38 offset:8448\n"
        "s_waitcnt lgkmcnt(2)\n"
        "v_mfma_f32_16x16x32_bf16 v[208:211], v[240:243], v[16:19], v[208:211]\n"
        "v_mfma_f32_16x16x32_bf16 v[212:215], v[244:247], v[16:19], v[212:215]\n"
        "v_mfma_f32_16x16x32_bf16 v[216:219], v[248:251], v[16:19], v[216:219]\n"
        "v_mfma_f32_16x16x32_bf16 v[220:223], v[252:255], v[16:19], v[220:223]\n"
        "ds_read_b128 v[16:19], v38 offset:16640\n"
        "s_waitcnt vmcnt(5)\n"
        "s_waitcnt lgkmcnt(2)\n"
        "v_mfma_f32_16x16x32_bf16 v[176:179], v[156:159], v[8:11], v[176:179]\n"
        "v_add3_u32 v43, v43, v156, v157\n"
        "v_mfma_f32_16x16x32_bf16 v[180:183], v[160:163], v[8:11], v[180:183]\n"
        "v_add3_u32 v43, v43, v158, v159\n"
        "v_mfma_f32_16x16x32_bf16 v[184:187], v[164:167], v[8:11], v[184:187]\n"
        "v_add3_u32 v43, v43, v160, v161\n"
        "v_mfma_f32_16x16x32_bf16 v[188:191], v[168:171], v[8:11], v[188:191]\n"
        "v_add3_u32 v43, v43, v162, v163\n"
        "ds_read_b128 v[8:11], v39 offset:256\n"
        "s_waitcnt lgkmcnt(2)\n"
        "v_mfma_f32_16x16x32_bf16 v[192:195], v[156:159], v[12:15], v[192:195]\n"
        "v_add3_u32 v43, v43, v164, v165\n"
        "v_mfma_f32_16x16x32_bf16 v[196:199], v[160:163], v[12:15], v[196:199]\n"
        "v_add3_u32 v43, v43, v166, v167\n"
        "v_mfma_f32_16x16x32_bf16 v[200:203], v[164:167], v[12:15], v[200:203]\n"
        "v_add3_u32 v43, v43, v168, v169\n"
        "v_mfma_f32_16x16x32_bf16 v[204:207], v[168:171], v[12:15], v[204:207]\n"
        "v_add3_u32 v43, v43, v170, v171\n"
        "ds_read_b128 v[12:15], v39 offset:8448\n"
        "s_waitcnt lgkmcnt(2)\n"
        "v_mfma_f32_16x16x32_bf16 v[208:211], v[156:159], v[16:19], v[208:211]\n"
        "v_mfma_f32_16x16x32_bf16 v[212:215], v[160:163], v[16:19], v[212:215]\n"
        "v_mfma_f32_16x16x32_bf16 v[216:219], v[164:167], v[16:19], v[216:219]\n"
        "v_mfma_f32_16x16x32_bf16 v[220:223], v[168:171], v[16:19], v[220:223]\n"
        "ds_read_b128 v[16:19], v39 offset:16640\n"
        "s_waitcnt vmcnt(1)\n"
        "s_waitcnt vmcnt(0)\n"
        "s_waitcnt lgkmcnt(2)\n"
        "v_mfma_f32_16x16x32_bf16 v[176:179], v[140:143], v[8:11], v[176:179]\n"
        "v_add3_u32 v43, v43, v140, v141\n"
        "v_add3_u32 v43, v43, v142, v143\n"
        "v_mfma_f32_16x16x32_bf16 v[180:183], v[144:147], v[8:11], v[180:183]\n"
        "v_add3_u32 v43, v43, v144, v145\n"
        "v_add3_u32 v43, v43, v146, v147\n"
        "v_mfma_f32_16x16x32_bf16 v[184:187], v[148:151], v[8:11], v[184:187]\n"
        "v_add3_u32 v43, v43, v148, v149\n"
        "v_add3_u32 v43, v43, v150, v151\n"
        "v_mfma_f32_16x16x32_bf16 v[188:191], v[152:155], v[8:11], v[188:191]\n"
        "v_add3_u32 v43, v43, v152, v153\n"
        "v_add3_u32 v43, v43, v154, v155\n"
        "s_waitcnt lgkmcnt(1)\n"
        "v_mfma_f32_16x16x32_bf16 v[192:195], v[140:143], v[12:15], v[192:195]\n"
        "s_mov_b32 exec_hi, 0\n"
        "v_sub_u32 v43, v43, v173\n"
        "v_cmp_ne_u32 s[74:75], s66, v172\n"
        "v_sub_u32 v43, v43, v175\n"
        "v_cmp_ne_u32 s[76:77], s66, v174\n"
        "s_mov_b32 exec_hi, -1\n"
        "v_add_u32_dpp v43, v43, v43 row_shr:1 row_mask:0xf bank_mask:0xf bound_ctrl:0\n"
        "v_mfma_f32_16x16x32_bf16 v[196:199], v[144:147], v[12:15], v[196:199]\n"
        "s_nop 0\n"
        "v_add_u32_dpp v43, v43, v43 row_shr:2 row_mask:0xf bank_mask:0xf bound_ctrl:0\n"
        "s_nop 1\n"
        "v_add_u32_dpp v43, v43, v43 row_shr:4 row_mask:0xf bank_mask:0xf bound_ctrl:0\n"
        "v_mfma_f32_16x16x32_bf16 v[200:203], v[148:151], v[12:15], v[200:203]\n"
        "s_nop 0\n"
        "v_add_u32_dpp v43, v43, v43 row_shr:8 row_mask:0xf bank_mask:0xf bound_ctrl:0\n"
        "s_nop 1\n"
        "v_add_u32_dpp v43, v43, v43 row_bcast:15 row_mask:0xa bank_mask:0xf\n"
        "v_mfma_f32_16x16x32_bf16 v[204:207], v[152:155], v[12:15], v[204:207]\n"
        "s_nop 0\n"
        "v_add_u32_dpp v43, v43, v43 row_bcast:31 row_mask:0xc bank_mask:0xf\n"
        "s_lshl_b32 s75, s67, 6\n"
        "s_or_b32 s74, s74, s76\n"
        "s_waitcnt lgkmcnt(0)\n"
        "v_mfma_f32_16x16x32_bf16 v[208:211], v[140:143], v[16:19], v[208:211]\n"
        "v_readlane_b32 s73, v43, 63\n"
        "s_add_u32 s73, s73, s75\n"
        "s_or_b32 s73, s73, s74\n"
        "v_mfma_f32_16x16x32_bf16 v[212:215], v[144:147], v[16:19], v[212:215]\n"
        "v_mfma_f32_16x16x32_bf16 v[216:219], v[148:151], v[16:19], v[216:219]\n"
        "v_mfma_f32_16x16x32_bf16 v[220:223], v[152:155], v[16:19], v[220:223]\n"
        "s_cbranch_scc1 .Lqm_again\n"
        ".Lqm_done:\n"
        "ds_add_u32 v2, v3 offset:45496\n"
        "s_add_u32 s8, s16, 32768\n"
        "s_add_u32 s9, s16, 36864\n"
        "s_add_u32 s10, s16, 40960\n"
        "s_add_u32 s11, s16, 45056\n"
        "s_add_u32 s12, s16, 49152\n"
        "s_add_u32 s13, s16, 53248\n"
        "s_add_u32 s14, s16, 57344\n"
        "s_add_u32 s15, s16, 61440\n"
        "ds_read_b32 v8, v2 offset:45496\n"
        "s_waitcnt lgkmcnt(0)\n"
        "v_readfirstlane_b32 s73, v8\n"
        "s_cmp_ge_u32 s73, 4\n"
        "s_cbranch_scc0 .Lw_33\n"
        ".Le_34:\n"
        "v_cvt_pk_bf16_f32 v224, v176, v177\n"
        "v_cvt_pk_bf16_f32 v225, v178, v179\n"
        "v_cvt_pk_bf16_f32 v226, v180, v181\n"
        "v_cvt_pk_bf16_f32 v227, v182, v183\n"
        "ds_write_b128 v41, v[224:227] offset:24064\n"
        "v_cvt_pk_bf16_f32 v228, v184, v185\n"
        "v_cvt_pk_bf16_f32 v229, v186, v187\n"
        "v_cvt_pk_bf16_f32 v230, v188, v189\n"
        "v_cvt_pk_bf16_f32 v231, v190, v191\n"
        "ds_write_b128 v41, v[228:231] offset:24128\n"
        "v_cvt_pk_bf16_f32 v232, v192, v193\n"
        "v_cvt_pk_bf16_f32 v233, v194, v195\n"
        "v_cvt_pk_bf16_f32 v234, v196, v197\n"
        "v_cvt_pk_bf16_f32 v235, v198, v199\n"
        "ds_write_b128 v41, v[232:235] offset:32512\n"
        "v_cvt_pk_bf16_f32 v236, v200, v201\n"
        "v_cvt_pk_bf16_f32 v237, v202, v203\n"
        "v_cvt_pk_bf16_f32 v238, v204, v205\n"
        "v_cvt_pk_bf16_f32 v239, v206, v207\n"
        "ds_write_b128 v41, v[236:239] offset:32576\n"
        "v_cvt_pk_bf16_f32 v240, v208, v209\n"
        "v_cvt_pk_bf16_f32 v241, v210, v211\n"
        "v_cvt_pk_bf16_f32 v242, v212, v213\n"
        "v_cvt_pk_bf16_f32 v243, v214, v215\n"
        "s_mov_b32 exec_lo, 0x00ff00ff\n"
        "s_mov_b32 exec_hi, 0x00ff00ff\n"
        "ds_write_b128 v41, v[240:243] offset:40960\n"
        "v_cvt_pk_bf16_f32 v244, v216, v217\n"
        "v_cvt_pk_bf16_f32 v245, v218, v219\n"
        "v_cvt_pk_bf16_f32 v246, v220, v221\n"
        "v_cvt_pk_bf16_f32 v247, v222, v223\n"
        "ds_write_b128 v41, v[244:247] offset:41024\n"
        "s_mov_b64 exec, -1\n"
        "ds_add_u32 v2, v3 offset:45484\n"
        "ds_read_b128 v[180:183], v6 offset:0\n"
        "ds_read_b128 v[184:187], v6 offset:8192\n"
        "ds_read_b128 v[188:191], v6 offset:16384\n"
        "v_xor_b32 v6, 64, v6\n"
        "ds_read_b128 v[196:199], v6 offset:0\n"
        "ds_read_b128 v[200:203], v6 offset:8192\n"
        "ds_read_b128 v[204:207], v6 offset:16384\n"
        "v_xor_b32 v6, 192, v6\n"
        "ds_read_b128 v[212:215], v6 offset:0\n"
        "ds_read_b128 v[216:219], v6 offset:8192\n"
        "ds_read_b128 v[220:223], v6 offset:16384\n"
        "v_xor_b32 v6, 64, v6\n"
        "ds_read_b128 v[228:231], v6 offset:0\n"
        "ds_read_b128 v[232:235], v6 offset:8192\n"
        "ds_read_b128 v[236:239], v6 offset:16384\n"
        "v_xor_b32 v6, 192, v6\n"
        "ds_read_b128 v[244:247], v6 offset:256\n"
        "ds_read_b128 v[248:251], v6 offset:8448\n"
        "ds_read_b128 v[252:255], v6 offset:16640\n"
        "v_cmp_ne_u32 s[74:75], s66, v172\n"
        "v_cmp_ne_u32 s[76:77], s66, v174\n"
        "v_cmp_ne_u32 s[78:79], s67, v173\n"
        "v_cmp_ne_u32 vcc, s67, v175\n"
        "s_or_b64 s[74:75], s[74:75], s[76:77]\n"
        "s_or_b64 s[78:79], s[78:79], vcc\n"
        "s_or_b64 s[74:75], s[74:75], s[78:79]\n"
        "s_and_b32 s74, s75, 11\n"
        "s_cbranch_scc1 .Lwf\n"
        ".Lwf_e:\n"
        "s_setprio 3\n"
        "buffer_load_dwordx4 v[140:143], v5, s[44:47], s14 offen offset:0 sc1\n"
        "buffer_load_dwordx4 v[144:147], v5, s[44:47], s14 offen offset:1024 sc1\n"
        "buffer_load_dwordx4 v[148:151], v5, s[44:47], s14 offen offset:2048 sc1\n"
        "buffer_load_dwordx4 v[152:155], v5, s[44:47], s14 offen offset:3072 sc1\n"
        "buffer_load_dwordx4 v[156:159], v5, s[44:47], s15 offen offset:0 sc1\n"
        "buffer_load_dwordx4 v[160:163], v5, s[44:47], s15 offen offset:1024 sc1\n"
        "buffer_load_dwordx4 v[164:167], v5, s[44:47], s15 offen offset:2048 sc1\n"
        "buffer_load_dwordx4 v[168:171], v5, s[44:47], s15 offen offset:3072 sc1\n"
        "s_setprio 0\n"
        "s_add_u32 s8, s16, 0\n"
        "s_add_u32 s9, s16, 4096\n"
        "s_add_u32 s10, s16, 8192\n"
        "s_add_u32 s11, s16, 12288\n"
        "s_add_u32 s12, s16, 16384\n"
        "s_add_u32 s13, s16, 20480\n"
        "s_add_u32 s14, s16, 24576\n"
        "s_add_u32 s15, s16, 28672\n"
        "s_cmp_lg_u32 s3, 4\n"
        "s_cbranch_scc1 .Lmove_done\n"
        "s_waitcnt vmcnt(8)\n"
        "v_and_b32 v32, v24, v26\n"
        "v_and_b32 v33, v28, v30\n"
        "v_and_b32 v32, v32, v33\n"
        "v_or_b32 v24, 3, v24\n"
        "v_xor_b32 v24, s66, v24\n"
        "v_xor_b32 v25, s67, v25\n"
        "v_or_b32 v26, 3, v26\n"
        "v_xor_b32 v26, s66, v26\n"
        "v_xor_b32 v27, s67, v27\n"
        "v_or_b32 v28, 3, v28\n"
        "v_xor_b32 v28, s66, v28\n"
        "v_xor_b32 v29, s67, v29\n"
        "v_or_b32 v30, 3, v30\n"
        "v_xor_b32 v30, s66, v30\n"
        "v_xor_b32 v31, s67, v31\n"
        "v_or3_b32 v24, v24, v25, v26\n"
        "v_or3_b32 v27, v27, v28, v29\n"
        "v_or3_b32 v24, v24, v30, v31\n"
        "v_and_b32 v33, 1, v32\n"
        "v_or_b32 v24, v24, v27\n"
        "v_and_b32 v32, 2, v32\n"
        "v_cmp_eq_u32 s[74:75], 0, v33\n"
        "v_cmp_eq_u32 s[76:77], 0, v32\n"
        "v_cmp_ne_u32 vcc, 0, v24\n"
        "s_cmp_eq_u64 s[74:75], 0\n"
        "s_cselect_b32 s62, 1, 0\n"
        "s_cmp_eq_u64 s[76:77], 0\n"
        "s_cselect_b32 s73, 2, 0\n"
        "s_or_b32 s62, s62, s73\n"
        "s_cmp_eq_u64 vcc, 0\n"
        "s_cselect_b32 s73, 4, 0\n"
        "s_or_b32 s62, s62, s73\n"
        "s_bitcmp0_b32 s62, 2\n"
        "s_cbranch_scc1 .Lagain\n"
        ".Lall:\n"
        "s_add_u32 s75, s2, 1\n"
        "s_cmp_eq_u32 s75, s56\n"
        "s_cbranch_scc1 .Lmove\n"
        "s_bitcmp1_b32 s62, 0\n"
        "s_cbranch_scc1 .Ltell\n"
        ".Lmove:\n"
        "s_add_u32 s73, s64, 1\n"
        "s_addc_u32 s74, s65, 0\n"
        "v_mov_b32 v24, s73\n"
        "v_mov_b32 v25, s74\n"
        "s_mov_b64 exec, 1\n"
        "buffer_store_dwordx2 v[24:25], v2, s[44:47], 0 offen sc1\n"
        "s_mov_b64 exec, -1\n"
        ".Ltell:\n"
        "s_cmp_ge_u32 s2, s56\n"
        "s_cbranch_scc1 .Lend_l\n"
        "s_and_b32 s73, s62, 6\n"
        "s_cmp_eq_u32 s73, 6\n"
        "s_cselect_b32 s61, s61, 0x1000\n"
        ".Lmove_done:\n"
        "v_mov_b32 v36, s61\n"
        "ds_add_u32 v2, v36 offset:45452\n"
        "v_xor_b32 v37, 32, v20\n"
        "v_xor_b32 v38, 64, v20\n"
        "v_xor_b32 v39, 96, v20\n"
        "v_and_b32 v8, 15, v1\n"
        "v_mul_u32_u24 v8, 272, v8\n"
        "v_lshrrev_b32 v9, 4, v1\n"
        "v_lshl_add_u32 v33, v9, 4, v8\n"
        "v_lshlrev_b32 v8, 1, v8\n"
        "v_lshl_add_u32 v8, v9, 3, v8\n"
        "s_lshl_b32 s73, s72, 6\n"
        "s_add_u32 s73, s73, 40960\n"
        "v_add_u32 v43, s73, v8\n"
        "v_lshrrev_b32 v8, 3, v1\n"
        "v_and_b32 v9, 1, v1\n"
        "v_lshlrev_b32 v9, 6, v9\n"
        "v_lshl_or_b32 v8, v8, 9, v9\n"
        "v_and_b32 v9, 6, v1\n"
        "v_lshl_or_b32 v42, v9, 3, v8\n"
        "ds_read_b32 v8, v2 offset:45484\n"
        "s_waitcnt lgkmcnt(0)\n"
        "v_readfirstlane_b32 s73, v8\n"
        "s_cmp_ge_u32 s73, 4\n"
        "s_cbranch_scc0 .Lw_35\n"
        ".Le_36:\n"
        "s_setprio 0\n"
        "ds_read_b128 v[176:179], v7 offset:24064\n"
        "ds_read_b128 v[192:195], v7 offset:24128\n"
        "ds_read_b128 v[208:211], v7 offset:24192\n"
        "ds_read_b128 v[224:227], v7 offset:24256\n"
        "ds_read_b128 v[240:243], v7 offset:24320\n"
        "s_waitcnt lgkmcnt(4)\n"
        "v_mfma_f32_16x16x32_bf16 v[8:11], v[180:183], v[176:179], 0\n"
        "v_mfma_f32_16x16x32_bf16 v[12:15], v[184:187], v[176:179], 0\n"
        "v_mfma_f32_16x16x32_bf16 v[16:19], v[188:191], v[176:179], 0\n"
        "v_xor_b32 v6, 64, v6\n"
        "ds_read_b128 v[180:183], v6 offset:256\n"
        "ds_read_b128 v[184:187], v6 offset:8448\n"
        "ds_read_b128 v[188:191], v6 offset:16640\n"
        "ds_read_b128 v[176:179], v7 offset:24384\n"
        "s_waitcnt lgkmcnt(7)\n"
        "v_mfma_f32_16x16x32_bf16 v[8:11], v[196:199], v[192:195], v[8:11]\n"
        "v_mfma_f32_16x16x32_bf16 v[12:15], v[200:203], v[192:195], v[12:15]\n"
        "v_mfma_f32_16x16x32_bf16 v[16:19], v[204:207], v[192:195], v[16:19]\n"
        "v_xor_b32 v6, 192, v6\n"
        "ds_read_b128 v[196:199], v6 offset:256\n"
        "ds_read_b128 v[200:203], v6 offset:8448\n"
        "ds_read_b128 v[204:207], v6 offset:16640\n"
        "ds_read_b128 v[192:195], v7 offset:24448\n"
        "s_waitcnt lgkmcnt(10)\n"
        "v_mfma_f32_16x16x32_bf16 v[8:11], v[212:215], v[208:211], v[8:11]\n"
        "v_mfma_f32_16x16x32_bf16 v[12:15], v[216:219], v[208:211], v[12:15]\n"
        "v_mfma_f32_16x16x32_bf16 v[16:19], v[220:223], v[208:211], v[16:19]\n"
        "v_xor_b32 v6, 64, v6\n"
        "ds_read_b128 v[212:215], v6 offset:256\n"
        "ds_read_b128 v[216:219], v6 offset:8448\n"
        "ds_read_b128 v[220:223], v6 offset:16640\n"
        "ds_read_b128 v[208:211], v7 offset:24512\n"
        "s_waitcnt lgkmcnt(13)\n"
        "v_mfma_f32_16x16x32_bf16 v[8:11], v[228:231], v[224:227], v[8:11]\n"
        "v_mfma_f32_16x16x32_bf16 v[12:15], v[232:235], v[224:227], v[12:15]\n"
        "v_mfma_f32_16x16x32_bf16 v[16:19], v[236:239], v[224:227], v[16:19]\n"
        "ds_read_b64_tr_b16 v[224:225], v20 offset:0\n"
        "ds_read_b64_tr_b16 v[226:227], v37 offset:2048\n"
        "ds_read_b64_tr_b16 v[228:229], v20 offset:8\n"
        "ds_read_b64_tr_b16 v[230:231], v37 offset:2056\n"
        "ds_read_b64_tr_b16 v[232:233], v38 offset:0\n"
        "ds_read_b64_tr_b16 v[234:235], v39 offset:2048\n"
        "ds_read_b64_tr_b16 v[236:237], v38 offset:8\n"
        "ds_read_b64_tr_b16 v[238:239], v39 offset:2056\n"
        "s_waitcnt lgkmcnt(15)\n"
        "v_mfma_f32_16x16x32_bf16 v[8:11], v[244:247], v[240:243], v[8:11]\n"
        "v_mfma_f32_16x16x32_bf16 v[12:15], v[248:251], v[240:243], v[12:15]\n"
        "v_mfma_f32_16x16x32_bf16 v[16:19], v[252:255], v[240:243], v[16:19]\n"
        "ds_read_b64_tr_b16 v[240:241], v20 offset:16384\n"
        "ds_read_b64_tr_b16 v[242:243], v37 offset:18432\n"
        "ds_read_b64_tr_b16 v[244:245], v20 offset:16392\n"
        "ds_read_b64_tr_b16 v[246:247], v37 offset:18440\n"
        "ds_read_b64_tr_b16 v[248:249], v38 offset:16384\n"
        "ds_read_b64_tr_b16 v[250:251], v39 offset:18432\n"
        "ds_read_b64_tr_b16 v[252:253], v38 offset:16392\n"
        "ds_read_b64_tr_b16 v[254:255], v39 offset:18440\n"
        "v_mfma_f32_16x16x32_bf16 v[8:11], v[180:183], v[176:179], v[8:11]\n"
        "v_mfma_f32_16x16x32_bf16 v[12:15], v[184:187], v[176:179], v[12:15]\n"
        "v_mfma_f32_16x16x32_bf16 v[16:19], v[188:191], v[176:179], v[16:19]\n"
        "s_waitcnt lgkmcnt(15)\n"
        "v_mfma_f32_16x16x32_bf16 v[8:11], v[196:199], v[192:195], v[8:11]\n"
        "v_mfma_f32_16x16x32_bf16 v[12:15], v[200:203], v[192:195], v[12:15]\n"
        "v_mfma_f32_16x16x32_bf16 v[16:19], v[204:207], v[192:195], v[16:19]\n"
        "v_mfma_f32_16x16x32_bf16 v[8:11], v[212:215], v[208:211], v[8:11]\n"
        "v_mfma_f32_16x16x32_bf16 v[12:15], v[216:219], v[208:211], v[12:15]\n"
        "v_mfma_f32_16x16x32_bf16 v[16:19], v[220:223], v[208:211], v[16:19]\n"
        "s_mov_b32 exec_lo, 0xf000f0\n"
        "s_mov_b32 exec_hi, 0xf000f0\n"
        "s_nop 4\n"
        "v_mov_b64 v[8:9], v[12:13]\n"
        "v_mov_b64 v[10:11], v[14:15]\n"
        "s_mov_b32 exec_lo, 0xf000f00\n"
        "s_mov_b32 exec_hi, 0xf000f00\n"
        "v_mov_b64 v[8:9], v[16:17]\n"
        "v_mov_b64 v[10:11], v[18:19]\n"
        "s_mov_b32 exec_lo, 0x02220111\n"
        "s_mov_b32 exec_hi, 0x00880044\n"
        "s_and_b32 s73, s64, 7\n"
        "s_lshl_b32 s73, s73, 23\n"
        "s_add_u32 s73, s73, 0x3fb8aa3b\n"
        "v_mul_f32 v8, s73, v8\n"
        "v_mul_f32 v9, s73, v9\n"
        "v_mul_f32 v10, s73, v10\n"
        "v_mul_f32 v11, s73, v11\n"
        "v_exp_f32 v8, v8\n"
        "v_exp_f32 v9, v9\n"
        "v_exp_f32 v10, v10\n"
        "v_exp_f32 v11, v11\n"
        "v_add_f32 v12, v8, v9\n"
        "v_add_f32 v13, v10, v11\n"
        "v_add_f32 v12, v12, v13\n"
        "v_add_f32 v12, 0x3089705f, v12\n"
        "v_rcp_f32 v12, v12\n"
        "s_nop 0\n"
        "v_mul_f32 v8, v8, v12\n"
        "v_mul_f32 v9, v9, v12\n"
        "v_mul_f32 v10, v10, v12\n"
        "v_mul_f32 v11, v11, v12\n"
        "v_cvt_pk_bf16_f32 v8, v8, v9\n"
        "v_cvt_pk_bf16_f32 v9, v10, v11\n"
        "ds_write_b64 v40, v[8:9] offset:21760\n"
        "s_mov_b64 exec, -1\n"
        "ds_add_u32 v2, v3 offset:45532\n"
        "ds_read_b64_tr_b16 v[176:177], v20 offset:32768\n"
        "ds_read_b64_tr_b16 v[178:179], v37 offset:34816\n"
        "ds_read_b64_tr_b16 v[180:181], v20 offset:32776\n"
        "ds_read_b64_tr_b16 v[182:183], v37 offset:34824\n"
        "ds_read_b64_tr_b16 v[184:185], v38 offset:32768\n"
        "ds_read_b64_tr_b16 v[186:187], v39 offset:34816\n"
        "ds_read_b64_tr_b16 v[188:189], v38 offset:32776\n"
        "ds_read_b64_tr_b16 v[190:191], v39 offset:34824\n"
        "ds_read_b64_tr_b16 v[192:193], v20 offset:49152\n"
        "ds_read_b64_tr_b16 v[194:195], v37 offset:51200\n"
        "ds_read_b64_tr_b16 v[196:197], v20 offset:49160\n"
        "ds_read_b64_tr_b16 v[198:199], v37 offset:51208\n"
        "ds_read_b64_tr_b16 v[200:201], v38 offset:49152\n"
        "ds_read_b64_tr_b16 v[202:203], v39 offset:51200\n"
        "ds_read_b64_tr_b16 v[204:205], v38 offset:49160\n"
        "ds_read_b64_tr_b16 v[206:207], v39 offset:51208\n"
        "v_add_u32 v20, 0x10000, v20\n"
        "v_add_u32 v37, 0x10000, v37\n"
        "v_add_u32 v38, 0x10000, v38\n"
        "v_add_u32 v39, 0x10000, v39\n"
        "ds_read_b64_tr_b16 v[208:209], v20 offset:0\n"
        "ds_read_b64_tr_b16 v[210:211], v37 offset:2048\n"
        "ds_read_b64_tr_b16 v[212:213], v20 offset:8\n"
        "ds_read_b64_tr_b16 v[214:215], v37 offset:2056\n"
        "ds_read_b64_tr_b16 v[216:217], v38 offset:0\n"
        "ds_read_b64_tr_b16 v[218:219], v39 offset:2048\n"
        "ds_read_b64_tr_b16 v[220:221], v38 offset:8\n"
        "ds_read_b64_tr_b16 v[222:223], v39 offset:2056\n"
        "ds_read_b32 v32, v2 offset:45532\n"
        "s_waitcnt lgkmcnt(0)\n"
        "v_readfirstlane_b32 s73, v32\n"
        "s_cmp_ge_u32 s73, 4\n"
        "s_cbranch_scc0 .Lw_37\n"
        ".Le_38:\n"
        "ds_read_b128 v[8:11], v21 offset:21760\n"
        "ds_read_b128 v[12:15], v22 offset:21760\n"
        "ds_read_b128 v[16:19], v21 offset:22016\n"
        "ds_read_b128 v[24:27], v22 offset:22016\n"
        "ds_read_b128 v[28:31], v21 offset:22272\n"
        "s_waitcnt lgkmcnt(3)\n"
        "v_mfma_f32_16x16x32_bf16 v[224:227], v[224:227], v[8:11], 0\n"
        "v_mfma_f32_16x16x32_bf16 v[224:227], v[240:243], v[12:15], v[224:227]\n"
        "v_mfma_f32_16x16x32_bf16 v[228:231], v[228:231], v[8:11], 0\n"
        "v_mfma_f32_16x16x32_bf16 v[228:231], v[244:247], v[12:15], v[228:231]\n"
        "v_mfma_f32_16x16x32_bf16 v[232:235], v[232:235], v[8:11], 0\n"
        "v_mfma_f32_16x16x32_bf16 v[232:235], v[248:251], v[12:15], v[232:235]\n"
        "v_mfma_f32_16x16x32_bf16 v[236:239], v[236:239], v[8:11], 0\n"
        "v_mfma_f32_16x16x32_bf16 v[236:239], v[252:255], v[12:15], v[236:239]\n"
        "s_waitcnt lgkmcnt(1)\n"
        "v_mfma_f32_16x16x32_bf16 v[176:179], v[176:179], v[16:19], 0\n"
        "v_mfma_f32_16x16x32_bf16 v[176:179], v[192:195], v[24:27], v[176:179]\n"
        "v_mfma_f32_16x16x32_bf16 v[180:183], v[180:183], v[16:19], 0\n"
        "v_mfma_f32_16x16x32_bf16 v[180:183], v[196:199], v[24:27], v[180:183]\n"
        "v_mfma_f32_16x16x32_bf16 v[184:187], v[184:187], v[16:19], 0\n"
        "v_mfma_f32_16x16x32_bf16 v[184:187], v[200:203], v[24:27], v[184:187]\n"
        "v_mfma_f32_16x16x32_bf16 v[188:191], v[188:191], v[16:19], 0\n"
        "v_mfma_f32_16x16x32_bf16 v[188:191], v[204:207], v[24:27], v[188:191]\n"
        "v_cvt_pk_bf16_f32 v224, v224, v225\n"
        "v_cvt_pk_bf16_f32 v225, v226, v227\n"
        "v_cvt_pk_bf16_f32 v226, v228, v229\n"
        "v_cvt_pk_bf16_f32 v227, v230, v231\n"
        "ds_write_b128 v41, v[224:227] offset:24064\n"
        "v_cvt_pk_bf16_f32 v232, v232, v233\n"
        "v_cvt_pk_bf16_f32 v233, v234, v235\n"
        "v_cvt_pk_bf16_f32 v234, v236, v237\n"
        "v_cvt_pk_bf16_f32 v235, v238, v239\n"
        "ds_write_b128 v41, v[232:235] offset:24128\n"
        "s_waitcnt lgkmcnt(2)\n"
        "v_mfma_f32_16x16x32_bf16 v[208:211], v[208:211], v[28:31], 0\n"
        "v_mfma_f32_16x16x32_bf16 v[212:215], v[212:215], v[28:31], 0\n"
        "v_mfma_f32_16x16x32_bf16 v[216:219], v[216:219], v[28:31], 0\n"
        "v_mfma_f32_16x16x32_bf16 v[220:223], v[220:223], v[28:31], 0\n"
        "v_cvt_pk_bf16_f32 v176, v176, v177\n"
        "v_cvt_pk_bf16_f32 v177, v178, v179\n"
        "v_cvt_pk_bf16_f32 v178, v180, v181\n"
        "v_cvt_pk_bf16_f32 v179, v182, v183\n"
        "ds_write_b128 v41, v[176:179] offset:32512\n"
        "v_cvt_pk_bf16_f32 v184, v184, v185\n"
        "v_cvt_pk_bf16_f32 v185, v186, v187\n"
        "v_cvt_pk_bf16_f32 v186, v188, v189\n"
        "v_cvt_pk_bf16_f32 v187, v190, v191\n"
        "ds_write_b128 v41, v[184:187] offset:32576\n"
        "s_mov_b32 exec_lo, 0x00ff00ff\n"
        "s_mov_b32 exec_hi, 0x00ff00ff\n"
        "v_cvt_pk_bf16_f32 v208, v208, v209\n"
        "v_cvt_pk_bf16_f32 v209, v212, v213\n"
        "v_cvt_pk_bf16_f32 v210, v210, v211\n"
        "v_cvt_pk_bf16_f32 v211, v214, v215\n"
        "ds_write2_b64 v43, v[208:209], v[210:211] offset0:0 offset1:34\n"
        "v_cvt_pk_bf16_f32 v216, v216, v217\n"
        "v_cvt_pk_bf16_f32 v217, v220, v221\n"
        "v_cvt_pk_bf16_f32 v218, v218, v219\n"
        "v_cvt_pk_bf16_f32 v219, v222, v223\n"
        "ds_write2_b64 v43, v[216:217], v[218:219] offset0:4 offset1:38\n"
        "s_mov_b64 exec, -1\n"
        "ds_add_u32 v2, v3 offset:45488\n"
        "s_setprio 0\n"
        "s_mul_i32 s76, s2, 20480\n"
        "s_lshl_b32 s77, s72, 7\n"
        "s_add_u32 s76, s76, s77\n"
        "ds_read_b32 v8, v2 offset:45488\n"
        "s_waitcnt lgkmcnt(0)\n"
        "v_readfirstlane_b32 s73, v8\n"
        "s_cmp_ge_u32 s73, 4\n"
        "s_cbranch_scc0 .Lw_39\n"
        ".Le_40:\n"
        "ds_read_b128 v[224:227], v23 offset:24064\n"
        "ds_read_b128 v[228:231], v23 offset:24128\n"
        "ds_read_b128 v[232:235], v23 offset:32512\n"
        "ds_read_b128 v[236:239], v23 offset:32576\n"
        "ds_read_b128 v[240:243], v33 offset:40960\n"
        "v_mov_b64 v[208:209], 0\n"
        "v_mov_b64 v[210:211], 0\n"
        "v_mov_b64 v[212:213], 0\n"
        "v_mov_b64 v[214:215], 0\n"
        "v_mov_b64 v[216:217], 0\n"
        "v_mov_b64 v[218:219], 0\n"
        "v_mov_b64 v[220:221], 0\n"
        "v_mov_b64 v[222:223], 0\n"
        "ds_read_b128 v[248:251], v23 offset:24192\n"
        "ds_read_b128 v[252:255], v23 offset:24256\n"
        "ds_read_b128 v[8:11], v23 offset:32640\n"
        "ds_read_b128 v[12:15], v23 offset:32704\n"
        "ds_read_b128 v[16:19], v33 offset:41024\n"
        "s_waitcnt lgkmcnt(8)\n"
        "v_mfma_f32_16x16x32_bf16 v[176:179], v[44:47], v[224:227], 0\n"
        "v_mfma_f32_16x16x32_bf16 v[176:179], v[48:51], v[228:231], v[176:179]\n"
        "v_mfma_f32_16x16x32_bf16 v[180:183], v[52:55], v[224:227], 0\n"
        "v_mfma_f32_16x16x32_bf16 v[180:183], v[56:59], v[228:231], v[180:183]\n"
        "v_mfma_f32_16x16x32_bf16 v[184:187], v[60:63], v[224:227], 0\n"
        "v_mfma_f32_16x16x32_bf16 v[184:187], v[64:67], v[228:231], v[184:187]\n"
        "v_mfma_f32_16x16x32_bf16 v[188:191], v[68:71], v[224:227], 0\n"
        "v_mfma_f32_16x16x32_bf16 v[188:191], v[72:75], v[228:231], v[188:191]\n"
        "s_waitcnt lgkmcnt(6)\n"
        "v_mfma_f32_16x16x32_bf16 v[192:195], v[44:47], v[232:235], 0\n"
        "v_mfma_f32_16x16x32_bf16 v[192:195], v[48:51], v[236:239], v[192:195]\n"
        "v_mfma_f32_16x16x32_bf16 v[196:199], v[52:55], v[232:235], 0\n"
        "v_mfma_f32_16x16x32_bf16 v[196:199], v[56:59], v[236:239], v[196:199]\n"
        "v_mfma_f32_16x16x32_bf16 v[200:203], v[60:63], v[232:235], 0\n"
        "v_mfma_f32_16x16x32_bf16 v[200:203], v[64:67], v[236:239], v[200:203]\n"
        "v_mfma_f32_16x16x32_bf16 v[204:207], v[68:71], v[232:235], 0\n"
        "v_mfma_f32_16x16x32_bf16 v[204:207], v[72:75], v[236:239], v[204:207]\n"
        "s_waitcnt lgkmcnt(5)\n"
        "v_smfmac_f32_16x16x64_bf16 v[208:211], v[240:243], v[44:51], v0\n"
        "v_smfmac_f32_16x16x64_bf16 v[212:215], v[240:243], v[52:59], v0\n"
        "v_smfmac_f32_16x16x64_bf16 v[216:219], v[240:243], v[60:67], v0\n"
        "v_smfmac_f32_16x16x64_bf16 v[220:223], v[240:243], v[68:75], v0\n"
        "s_setprio 3\n"
        "buffer_load_dwordx4 v[44:47], v5, s[44:47], s8 offen offset:0 sc1\n"
        "buffer_load_dwordx4 v[48:51], v5, s[44:47], s8 offen offset:1024 sc1\n"
        "buffer_load_dwordx4 v[52:55], v5, s[44:47], s8 offen offset:2048 sc1\n"
        "buffer_load_dwordx4 v[56:59], v5, s[44:47], s8 offen offset:3072 sc1\n"
        "buffer_load_dwordx4 v[60:63], v5, s[44:47], s9 offen offset:0 sc1\n"
        "buffer_load_dwordx4 v[64:67], v5, s[44:47], s9 offen offset:1024 sc1\n"
        "buffer_load_dwordx4 v[68:71], v5, s[44:47], s9 offen offset:2048 sc1\n"
        "buffer_load_dwordx4 v[72:75], v5, s[44:47], s9 offen offset:3072 sc1\n"
        "s_setprio 0\n"
        "ds_read_b128 v[224:227], v23 offset:24320\n"
        "ds_read_b128 v[228:231], v23 offset:24384\n"
        "ds_read_b128 v[232:235], v23 offset:32768\n"
        "ds_read_b128 v[236:239], v23 offset:32832\n"
        "ds_read_b128 v[240:243], v33 offset:41088\n"
        "s_waitcnt lgkmcnt(8)\n"
        "v_mfma_f32_16x16x32_bf16 v[176:179], v[76:79], v[248:251], v[176:179]\n"
        "v_mfma_f32_16x16x32_bf16 v[176:179], v[80:83], v[252:255], v[176:179]\n"
        "v_mfma_f32_16x16x32_bf16 v[180:183], v[84:87], v[248:251], v[180:183]\n"
        "v_mfma_f32_16x16x32_bf16 v[180:183], v[88:91], v[252:255], v[180:183]\n"
        "v_mfma_f32_16x16x32_bf16 v[184:187], v[92:95], v[248:251], v[184:187]\n"
        "v_mfma_f32_16x16x32_bf16 v[184:187], v[96:99], v[252:255], v[184:187]\n"
        "v_mfma_f32_16x16x32_bf16 v[188:191], v[100:103], v[248:251], v[188:191]\n"
        "v_mfma_f32_16x16x32_bf16 v[188:191], v[104:107], v[252:255], v[188:191]\n"
        "s_waitcnt lgkmcnt(6)\n"
        "v_mfma_f32_16x16x32_bf16 v[192:195], v[76:79], v[8:11], v[192:195]\n"
        "v_mfma_f32_16x16x32_bf16 v[192:195], v[80:83], v[12:15], v[192:195]\n"
        "v_mfma_f32_16x16x32_bf16 v[196:199], v[84:87], v[8:11], v[196:199]\n"
        "v_mfma_f32_16x16x32_bf16 v[196:199], v[88:91], v[12:15], v[196:199]\n"
        "v_mfma_f32_16x16x32_bf16 v[200:203], v[92:95], v[8:11], v[200:203]\n"
        "v_mfma_f32_16x16x32_bf16 v[200:203], v[96:99], v[12:15], v[200:203]\n"
        "v_mfma_f32_16x16x32_bf16 v[204:207], v[100:103], v[8:11], v[204:207]\n"
        "v_mfma_f32_16x16x32_bf16 v[204:207], v[104:107], v[12:15], v[204:207]\n"
        "s_waitcnt lgkmcnt(5)\n"
        "v_smfmac_f32_16x16x64_bf16 v[208:211], v[16:19], v[76:83], v0\n"
        "v_smfmac_f32_16x16x64_bf16 v[212:215], v[16:19], v[84:91], v0\n"
        "v_smfmac_f32_16x16x64_bf16 v[216:219], v[16:19], v[92:99], v0\n"
        "v_smfmac_f32_16x16x64_bf16 v[220:223], v[16:19], v[100:107], v0\n"
        "s_setprio 3\n"
        "buffer_load_dwordx4 v[76:79], v5, s[44:47], s10 offen offset:0 sc1\n"
        "buffer_load_dwordx4 v[80:83], v5, s[44:47], s10 offen offset:1024 sc1\n"
        "buffer_load_dwordx4 v[84:87], v5, s[44:47], s10 offen offset:2048 sc1\n"
        "buffer_load_dwordx4 v[88:91], v5, s[44:47], s10 offen offset:3072 sc1\n"
        "buffer_load_dwordx4 v[92:95], v5, s[44:47], s11 offen offset:0 sc1\n"
        "buffer_load_dwordx4 v[96:99], v5, s[44:47], s11 offen offset:1024 sc1\n"
        "buffer_load_dwordx4 v[100:103], v5, s[44:47], s11 offen offset:2048 sc1\n"
        "buffer_load_dwordx4 v[104:107], v5, s[44:47], s11 offen offset:3072 sc1\n"
        "s_setprio 0\n"
        "ds_read_b128 v[248:251], v23 offset:24448\n"
        "ds_read_b128 v[252:255], v23 offset:24512\n"
        "ds_read_b128 v[8:11], v23 offset:32896\n"
        "ds_read_b128 v[12:15], v23 offset:32960\n"
        "ds_read_b128 v[16:19], v33 offset:41152\n"
        "s_waitcnt lgkmcnt(8)\n"
        "v_mfma_f32_16x16x32_bf16 v[176:179], v[108:111], v[224:227], v[176:179]\n"
        "v_mfma_f32_16x16x32_bf16 v[176:179], v[112:115], v[228:231], v[176:179]\n"
        "v_mfma_f32_16x16x32_bf16 v[180:183], v[116:119], v[224:227], v[180:183]\n"
        "v_mfma_f32_16x16x32_bf16 v[180:183], v[120:123], v[228:231], v[180:183]\n"
        "v_mfma_f32_16x16x32_bf16 v[184:187], v[124:127], v[224:227], v[184:187]\n"
        "v_mfma_f32_16x16x32_bf16 v[184:187], v[128:131], v[228:231], v[184:187]\n"
        "v_mfma_f32_16x16x32_bf16 v[188:191], v[132:135], v[224:227], v[188:191]\n"
        "v_mfma_f32_16x16x32_bf16 v[188:191], v[136:139], v[228:231], v[188:191]\n"
        "s_waitcnt lgkmcnt(6)\n"
        "v_mfma_f32_16x16x32_bf16 v[192:195], v[108:111], v[232:235], v[192:195]\n"
        "v_mfma_f32_16x16x32_bf16 v[192:195], v[112:115], v[236:239], v[192:195]\n"
        "v_mfma_f32_16x16x32_bf16 v[196:199], v[116:119], v[232:235], v[196:199]\n"
        "v_mfma_f32_16x16x32_bf16 v[196:199], v[120:123], v[236:239], v[196:199]\n"
        "v_mfma_f32_16x16x32_bf16 v[200:203], v[124:127], v[232:235], v[200:203]\n"
        "v_mfma_f32_16x16x32_bf16 v[200:203], v[128:131], v[236:239], v[200:203]\n"
        "v_mfma_f32_16x16x32_bf16 v[204:207], v[132:135], v[232:235], v[204:207]\n"
        "v_mfma_f32_16x16x32_bf16 v[204:207], v[136:139], v[236:239], v[204:207]\n"
        "s_waitcnt lgkmcnt(5)\n"
        "v_smfmac_f32_16x16x64_bf16 v[208:211], v[240:243], v[108:115], v0\n"
        "v_smfmac_f32_16x16x64_bf16 v[212:215], v[240:243], v[116:123], v0\n"
        "v_smfmac_f32_16x16x64_bf16 v[216:219], v[240:243], v[124:131], v0\n"
        "v_smfmac_f32_16x16x64_bf16 v[220:223], v[240:243], v[132:139], v0\n"
        "s_setprio 3\n"
        "buffer_load_dwordx4 v[108:111], v5, s[44:47], s12 offen offset:0 sc1\n"
        "buffer_load_dwordx4 v[112:115], v5, s[44:47], s12 offen offset:1024 sc1\n"
        "buffer_load_dwordx4 v[116:119], v5, s[44:47], s12 offen offset:2048 sc1\n"
        "buffer_load_dwordx4 v[120:123], v5, s[44:47], s12 offen offset:3072 sc1\n"
        "buffer_load_dwordx4 v[124:127], v5, s[44:47], s13 offen offset:0 sc1\n"
        "buffer_load_dwordx4 v[128:131], v5, s[44:47], s13 offen offset:1024 sc1\n"
        "buffer_load_dwordx4 v[132:135], v5, s[44:47], s13 offen offset:2048 sc1\n"
        "buffer_load_dwordx4 v[136:139], v5, s[44:47], s13 offen offset:3072 sc1\n"
        "s_setprio 0\n"
        "ds_read_b32 v36, v2 offset:45452\n"
        "ds_read_b128 v[224:227], v34 offset:0\n"
        "ds_read_b128 v[228:231], v34 offset:64\n"
        "ds_read_b128 v[232:235], v34 offset:128\n"
        "ds_read_b128 v[236:239], v34 offset:192\n"
        "ds_read_b128 v[240:243], v34 offset:272\n"
        "ds_read_b128 v[244:247], v34 offset:336\n"
        "s_waitcnt vmcnt(24)\n"
        "s_waitcnt lgkmcnt(10)\n"
        "v_mfma_f32_16x16x32_bf16 v[176:179], v[140:143], v[248:251], v[176:179]\n"
        "v_mfma_f32_16x16x32_bf16 v[176:179], v[144:147], v[252:255], v[176:179]\n"
        "v_mfma_f32_16x16x32_bf16 v[180:183], v[148:151], v[248:251], v[180:183]\n"
        "v_mfma_f32_16x16x32_bf16 v[180:183], v[152:155], v[252:255], v[180:183]\n"
        "v_mfma_f32_16x16x32_bf16 v[184:187], v[156:159], v[248:251], v[184:187]\n"
        "v_mfma_f32_16x16x32_bf16 v[184:187], v[160:163], v[252:255], v[184:187]\n"
        "v_mfma_f32_16x16x32_bf16 v[188:191], v[164:167], v[248:251], v[188:191]\n"
        "v_mfma_f32_16x16x32_bf16 v[188:191], v[168:171], v[252:255], v[188:191]\n"
        "s_waitcnt lgkmcnt(8)\n"
        "v_mfma_f32_16x16x32_bf16 v[192:195], v[140:143], v[8:11], v[192:195]\n"
        "v_mfma_f32_16x16x32_bf16 v[192:195], v[144:147], v[12:15], v[192:195]\n"
        "v_mfma_f32_16x16x32_bf16 v[196:199], v[148:151], v[8:11], v[196:199]\n"
        "v_mfma_f32_16x16x32_bf16 v[196:199], v[152:155], v[12:15], v[196:199]\n"
        "v_mfma_f32_16x16x32_bf16 v[200:203], v[156:159], v[8:11], v[200:203]\n"
        "v_mfma_f32_16x16x32_bf16 v[200:203], v[160:163], v[12:15], v[200:203]\n"
        "v_mfma_f32_16x16x32_bf16 v[204:207], v[164:167], v[8:11], v[204:207]\n"
        "v_mfma_f32_16x16x32_bf16 v[204:207], v[168:171], v[12:15], v[204:207]\n"
        "s_waitcnt lgkmcnt(7)\n"
        "v_smfmac_f32_16x16x64_bf16 v[208:211], v[16:19], v[140:147], v0\n"
        "v_smfmac_f32_16x16x64_bf16 v[212:215], v[16:19], v[148:155], v0\n"
        "v_smfmac_f32_16x16x64_bf16 v[216:219], v[16:19], v[156:163], v0\n"
        "v_smfmac_f32_16x16x64_bf16 v[220:223], v[16:19], v[164:171], v0\n"
        "s_setprio 3\n"
        "buffer_load_dwordx4 v[140:143], v5, s[44:47], s14 offen offset:0 sc1\n"
        "buffer_load_dwordx4 v[144:147], v5, s[44:47], s14 offen offset:1024 sc1\n"
        "buffer_load_dwordx4 v[148:151], v5, s[44:47], s14 offen offset:2048 sc1\n"
        "buffer_load_dwordx4 v[152:155], v5, s[44:47], s14 offen offset:3072 sc1\n"
        "buffer_load_dwordx4 v[156:159], v5, s[44:47], s15 offen offset:0 sc1\n"
        "buffer_load_dwordx4 v[160:163], v5, s[44:47], s15 offen offset:1024 sc1\n"
        "buffer_load_dwordx4 v[164:167], v5, s[44:47], s15 offen offset:2048 sc1\n"
        "buffer_load_dwordx4 v[168:171], v5, s[44:47], s15 offen offset:3072 sc1\n"
        "s_setprio 0\n"
        "v_add_u32 v33, 17408, v33\n"
        "ds_read_b128 v[248:251], v34 offset:400\n"
        "ds_read_b128 v[252:255], v34 offset:464\n"
        "s_waitcnt lgkmcnt(8)\n"
        "v_readfirstlane_b32 s73, v36\n"
        "s_cmp_ge_u32 s73, 8\n"
        "s_cbranch_scc1 .Lroll0\n"
        "s_waitcnt lgkmcnt(0)\n"
        "ds_read_b32 v8, v2 offset:45452\n"
        "s_waitcnt lgkmcnt(0)\n"
        "v_readfirstlane_b32 s73, v8\n"
        "s_cmp_ge_u32 s73, 8\n"
        "s_cbranch_scc0 .Lw_41\n"
        ".Le_42:\n"
        "s_cmp_ge_u32 s73, 0x1000\n"
        "s_cbranch_scc1 .Lexit\n"
        "ds_read_b128 v[224:227], v34 offset:0\n"
        "ds_read_b128 v[228:231], v34 offset:64\n"
        "ds_read_b128 v[232:235], v34 offset:128\n"
        "ds_read_b128 v[236:239], v34 offset:192\n"
        "ds_read_b128 v[240:243], v34 offset:272\n"
        "ds_read_b128 v[244:247], v34 offset:336\n"
        "ds_read_b128 v[248:251], v34 offset:400\n"
        "ds_read_b128 v[252:255], v34 offset:464\n"
        "s_waitcnt lgkmcnt(0)\n"
        ".Lroll0:\n"
        "s_cmp_ge_u32 s73, 0x1000\n"
        "s_cbranch_scc1 .Lexit\n"
        "s_waitcnt vmcnt(24)\n"
        "s_waitcnt lgkmcnt(6)\n"
        "v_mfma_f32_16x16x32_bf16 v[176:179], v[44:47], v[224:227], v[176:179]\n"
        "v_mfma_f32_16x16x32_bf16 v[176:179], v[48:51], v[228:231], v[176:179]\n"
        "v_mfma_f32_16x16x32_bf16 v[180:183], v[52:55], v[224:227], v[180:183]\n"
        "v_mfma_f32_16x16x32_bf16 v[180:183], v[56:59], v[228:231], v[180:183]\n"
        "v_mfma_f32_16x16x32_bf16 v[184:187], v[60:63], v[224:227], v[184:187]\n"
        "v_mfma_f32_16x16x32_bf16 v[184:187], v[64:67], v[228:231], v[184:187]\n"
        "v_mfma_f32_16x16x32_bf16 v[188:191], v[68:71], v[224:227], v[188:191]\n"
        "v_mfma_f32_16x16x32_bf16 v[188:191], v[72:75], v[228:231], v[188:191]\n"
        "s_waitcnt vmcnt(16)\n"
        "s_waitcnt lgkmcnt(4)\n"
        "v_mfma_f32_16x16x32_bf16 v[176:179], v[76:79], v[232:235], v[176:179]\n"
        "v_mfma_f32_16x16x32_bf16 v[176:179], v[80:83], v[236:239], v[176:179]\n"
        "v_mfma_f32_16x16x32_bf16 v[180:183], v[84:87], v[232:235], v[180:183]\n"
        "v_mfma_f32_16x16x32_bf16 v[180:183], v[88:91], v[236:239], v[180:183]\n"
        "v_mfma_f32_16x16x32_bf16 v[184:187], v[92:95], v[232:235], v[184:187]\n"
        "v_mfma_f32_16x16x32_bf16 v[184:187], v[96:99], v[236:239], v[184:187]\n"
        "v_mfma_f32_16x16x32_bf16 v[188:191], v[100:103], v[232:235], v[188:191]\n"
        "v_mfma_f32_16x16x32_bf16 v[188:191], v[104:107], v[236:239], v[188:191]\n"
        "s_waitcnt vmcnt(8)\n"
        "s_waitcnt lgkmcnt(2)\n"
        "v_mfma_f32_16x16x32_bf16 v[176:179], v[108:111], v[240:243], v[176:179]\n"
        "v_mfma_f32_16x16x32_bf16 v[176:179], v[112:115], v[244:247], v[176:179]\n"
        "v_mfma_f32_16x16x32_bf16 v[180:183], v[116:119], v[240:243], v[180:183]\n"
        "v_mfma_f32_16x16x32_bf16 v[180:183], v[120:123], v[244:247], v[180:183]\n"
        "v_mfma_f32_16x16x32_bf16 v[184:187], v[124:127], v[240:243], v[184:187]\n"
        "v_mfma_f32_16x16x32_bf16 v[184:187], v[128:131], v[244:247], v[184:187]\n"
        "v_mfma_f32_16x16x32_bf16 v[188:191], v[132:135], v[240:243], v[188:191]\n"
        "v_mfma_f32_16x16x32_bf16 v[188:191], v[136:139], v[244:247], v[188:191]\n"
        "ds_read_b32 v36, v2 offset:45468\n"
        "ds_read_b128 v[224:227], v34 offset:8704\n"
        "ds_read_b128 v[228:231], v34 offset:8768\n"
        "ds_read_b128 v[232:235], v34 offset:8832\n"
        "ds_read_b128 v[236:239], v34 offset:8896\n"
        "ds_read_b128 v[240:243], v34 offset:8976\n"
        "ds_read_b128 v[244:247], v34 offset:9040\n"
        "s_waitcnt vmcnt(0)\n"
        "s_waitcnt lgkmcnt(7)\n"
        "v_mfma_f32_16x16x32_bf16 v[176:179], v[140:143], v[248:251], v[176:179]\n"
        "v_mfma_f32_16x16x32_bf16 v[176:179], v[144:147], v[252:255], v[176:179]\n"
        "v_mfma_f32_16x16x32_bf16 v[180:183], v[148:151], v[248:251], v[180:183]\n"
        "v_mfma_f32_16x16x32_bf16 v[180:183], v[152:155], v[252:255], v[180:183]\n"
        "v_mfma_f32_16x16x32_bf16 v[184:187], v[156:159], v[248:251], v[184:187]\n"
        "v_mfma_f32_16x16x32_bf16 v[184:187], v[160:163], v[252:255], v[184:187]\n"
        "v_mfma_f32_16x16x32_bf16 v[188:191], v[164:167], v[248:251], v[188:191]\n"
        "v_mfma_f32_16x16x32_bf16 v[188:191], v[168:171], v[252:255], v[188:191]\n"
        "ds_read_b128 v[248:251], v34 offset:9104\n"
        "ds_read_b128 v[252:255], v34 offset:9168\n"
        "s_waitcnt lgkmcnt(8)\n"
        "v_readfirstlane_b32 s73, v36\n"
        "s_cmp_ge_u32 s73, 4\n"
        "s_cbranch_scc1 .Lroll1\n"
        "s_waitcnt lgkmcnt(0)\n"
        "ds_read_b32 v8, v2 offset:45468\n"
        "s_waitcnt lgkmcnt(0)\n"
        "v_readfirstlane_b32 s73, v8\n"
        "s_cmp_ge_u32 s73, 4\n"
        "s_cbranch_scc0 .Lw_43\n"
        ".Le_44:\n"
        "ds_read_b128 v[224:227], v34 offset:8704\n"
        "ds_read_b128 v[228:231], v34 offset:8768\n"
        "ds_read_b128 v[232:235], v34 offset:8832\n"
        "ds_read_b128 v[236:239], v34 offset:8896\n"
        "ds_read_b128 v[240:243], v34 offset:8976\n"
        "ds_read_b128 v[244:247], v34 offset:9040\n"
        "ds_read_b128 v[248:251], v34 offset:9104\n"
        "ds_read_b128 v[252:255], v34 offset:9168\n"
        "s_waitcnt lgkmcnt(0)\n"
        ".Lroll1:\n"
        "s_waitcnt lgkmcnt(6)\n"
        "v_mfma_f32_16x16x32_bf16 v[192:195], v[44:47], v[224:227], v[192:195]\n"
        "v_mfma_f32_16x16x32_bf16 v[192:195], v[48:51], v[228:231], v[192:195]\n"
        "v_mfma_f32_16x16x32_bf16 v[196:199], v[52:55], v[224:227], v[196:199]\n"
        "v_mfma_f32_16x16x32_bf16 v[196:199], v[56:59], v[228:231], v[196:199]\n"
        "v_mfma_f32_16x16x32_bf16 v[200:203], v[60:63], v[224:227], v[200:203]\n"
        "v_mfma_f32_16x16x32_bf16 v[200:203], v[64:67], v[228:231], v[200:203]\n"
        "v_mfma_f32_16x16x32_bf16 v[204:207], v[68:71], v[224:227], v[204:207]\n"
        "v_mfma_f32_16x16x32_bf16 v[204:207], v[72:75], v[228:231], v[204:207]\n"
        "v_cvt_pk_bf16_f32 v12, v176, v180\n"
        "v_cvt_pk_bf16_f32 v14, v178, v182\n"
        "v_cvt_pk_bf16_f32 v13, v184, v188\n"
        "v_cvt_pk_bf16_f32 v15, v186, v190\n"
        "v_cvt_pk_bf16_f32 v16, v177, v181\n"
        "v_cvt_pk_bf16_f32 v18, v179, v183\n"
        "v_cvt_pk_bf16_f32 v17, v185, v189\n"
        "v_cvt_pk_bf16_f32 v19, v187, v191\n"
        "v_permlane32_swap_b32 v12, v14\n"
        "v_permlane32_swap_b32 v13, v15\n"
        "v_permlane32_swap_b32 v16, v18\n"
        "v_permlane32_swap_b32 v17, v19\n"
        "buffer_store_dwordx4 v[12:15], v35, s[48:51], s76 offen offset:0 sc1\n"
        "buffer_store_dwordx4 v[16:19], v35, s[48:51], s76 offen offset:64 sc1\n"
        "s_add_u32 s76, s76, 0x2000\n"
        "s_waitcnt lgkmcnt(4)\n"
        "v_mfma_f32_16x16x32_bf16 v[192:195], v[76:79], v[232:235], v[192:195]\n"
        "v_mfma_f32_16x16x32_bf16 v[192:195], v[80:83], v[236:239], v[192:195]\n"
        "v_mfma_f32_16x16x32_bf16 v[196:199], v[84:87], v[232:235], v[196:199]\n"
        "v_mfma_f32_16x16x32_bf16 v[196:199], v[88:91], v[236:239], v[196:199]\n"
        "v_mfma_f32_16x16x32_bf16 v[200:203], v[92:95], v[232:235], v[200:203]\n"
        "v_mfma_f32_16x16x32_bf16 v[200:203], v[96:99], v[236:239], v[200:203]\n"
        "v_mfma_f32_16x16x32_bf16 v[204:207], v[100:103], v[232:235], v[204:207]\n"
        "v_mfma_f32_16x16x32_bf16 v[204:207], v[104:107], v[236:239], v[204:207]\n"
        "s_waitcnt lgkmcnt(2)\n"
        "v_mfma_f32_16x16x32_bf16 v[192:195], v[108:111], v[240:243], v[192:195]\n"
        "v_mfma_f32_16x16x32_bf16 v[192:195], v[112:115], v[244:247], v[192:195]\n"
        "v_mfma_f32_16x16x32_bf16 v[196:199], v[116:119], v[240:243], v[196:199]\n"
        "v_mfma_f32_16x16x32_bf16 v[196:199], v[120:123], v[244:247], v[196:199]\n"
        "v_mfma_f32_16x16x32_bf16 v[200:203], v[124:127], v[240:243], v[200:203]\n"
        "v_mfma_f32_16x16x32_bf16 v[200:203], v[128:131], v[244:247], v[200:203]\n"
        "v_mfma_f32_16x16x32_bf16 v[204:207], v[132:135], v[240:243], v[204:207]\n"
        "v_mfma_f32_16x16x32_bf16 v[204:207], v[136:139], v[244:247], v[204:207]\n"
        "s_waitcnt lgkmcnt(0)\n"
        "v_mfma_f32_16x16x32_bf16 v[192:195], v[140:143], v[248:251], v[192:195]\n"
        "v_mfma_f32_16x16x32_bf16 v[192:195], v[144:147], v[252:255], v[192:195]\n"
        "v_mfma_f32_16x16x32_bf16 v[196:199], v[148:151], v[248:251], v[196:199]\n"
        "v_mfma_f32_16x16x32_bf16 v[196:199], v[152:155], v[252:255], v[196:199]\n"
        "v_mfma_f32_16x16x32_bf16 v[200:203], v[156:159], v[248:251], v[200:203]\n"
        "v_mfma_f32_16x16x32_bf16 v[200:203], v[160:163], v[252:255], v[200:203]\n"
        "v_mfma_f32_16x16x32_bf16 v[204:207], v[164:167], v[248:251], v[204:207]\n"
        "v_mfma_f32_16x16x32_bf16 v[204:207], v[168:171], v[252:255], v[204:207]\n"
        "ds_read_b32 v36, v2 offset:45476\n"
        "ds_read_b128 v[224:227], v33 offset:0\n"
        "ds_read_b128 v[228:231], v33 offset:64\n"
        "ds_read_b128 v[232:235], v33 offset:128\n"
        "ds_read_b128 v[236:239], v33 offset:192\n"
        "s_waitcnt lgkmcnt(4)\n"
        "v_readfirstlane_b32 s73, v36\n"
        "s_cmp_lt_u32 s73, 4\n"
        "s_cbranch_scc1 .Lslow2\n"
        "s_waitcnt lgkmcnt(3)\n"
        "v_smfmac_f32_16x16x64_bf16 v[208:211], v[224:227], v[44:51], v0\n"
        "s_waitcnt lgkmcnt(2)\n"
        "v_smfmac_f32_16x16x64_bf16 v[208:211], v[228:231], v[76:83], v0\n"
        "s_waitcnt lgkmcnt(1)\n"
        "v_smfmac_f32_16x16x64_bf16 v[208:211], v[232:235], v[108:115], v0\n"
        "v_cvt_pk_bf16_f32 v20, v192, v196\n"
        "v_cvt_pk_bf16_f32 v22, v194, v198\n"
        "s_waitcnt lgkmcnt(0)\n"
        "v_smfmac_f32_16x16x64_bf16 v[208:211], v[236:239], v[140:147], v0\n"
        "v_cvt_pk_bf16_f32 v21, v200, v204\n"
        "v_cvt_pk_bf16_f32 v23, v202, v206\n"
        "v_smfmac_f32_16x16x64_bf16 v[212:215], v[224:227], v[52:59], v0\n"
        "v_cvt_pk_bf16_f32 v24, v193, v197\n"
        "v_cvt_pk_bf16_f32 v26, v195, v199\n"
        "v_smfmac_f32_16x16x64_bf16 v[212:215], v[228:231], v[84:91], v0\n"
        "v_cvt_pk_bf16_f32 v25, v201, v205\n"
        "v_cvt_pk_bf16_f32 v27, v203, v207\n"
        "v_smfmac_f32_16x16x64_bf16 v[212:215], v[232:235], v[116:123], v0\n"
        "v_permlane32_swap_b32 v20, v22\n"
        "v_permlane32_swap_b32 v21, v23\n"
        "v_smfmac_f32_16x16x64_bf16 v[212:215], v[236:239], v[148:155], v0\n"
        "v_permlane32_swap_b32 v24, v26\n"
        "v_permlane32_swap_b32 v25, v27\n"
        "v_smfmac_f32_16x16x64_bf16 v[216:219], v[224:227], v[60:67], v0\n"
        "buffer_store_dwordx4 v[20:23], v35, s[48:51], s76 offen offset:0 sc1\n"
        "buffer_store_dwordx4 v[24:27], v35, s[48:51], s76 offen offset:64 sc1\n"
        "v_smfmac_f32_16x16x64_bf16 v[216:219], v[228:231], v[92:99], v0\n"
        "s_add_u32 s76, s76, 0x2000\n"
        "v_smfmac_f32_16x16x64_bf16 v[216:219], v[232:235], v[124:131], v0\n"
        "v_smfmac_f32_16x16x64_bf16 v[216:219], v[236:239], v[156:163], v0\n"
        "v_smfmac_f32_16x16x64_bf16 v[220:223], v[224:227], v[68:75], v0\n"
        "v_smfmac_f32_16x16x64_bf16 v[220:223], v[228:231], v[100:107], v0\n"
        "v_smfmac_f32_16x16x64_bf16 v[220:223], v[232:235], v[132:139], v0\n"
        "v_smfmac_f32_16x16x64_bf16 v[220:223], v[236:239], v[164:171], v0\n"
        ".Lfin:\n"
        "s_nop 3\n"
        "v_add_f32 v208, v208, v209\n"
        "v_add_f32 v212, v212, v213\n"
        "v_add_f32 v216, v216, v217\n"
        "s_nop 0\n"
        "v_add_f32 v220, v220, v221\n"
        "v_add_f32 v210, v210, v211\n"
        "v_add_f32 v214, v214, v215\n"
        "v_add_f32 v218, v218, v219\n"
        "v_add_f32 v222, v222, v223\n"
        "v_cvt_pk_bf16_f32 v12, v208, v212\n"
        "v_cvt_pk_bf16_f32 v13, v216, v220\n"
        "v_cvt_pk_bf16_f32 v16, v208, v212\n"
        "v_cvt_pk_bf16_f32 v17, v216, v220\n"
        "v_cvt_pk_bf16_f32 v14, v210, v214\n"
        "v_cvt_pk_bf16_f32 v15, v218, v222\n"
        "s_nop 0\n"
        "v_mov_b32_dpp v12, v14 row_ror:8 row_mask:0xf bank_mask:0xc\n"
        "v_mov_b32_dpp v13, v15 row_ror:8 row_mask:0xf bank_mask:0xc\n"
        "v_mov_b32_dpp v14, v16 row_ror:8 row_mask:0xf bank_mask:0x3\n"
        "v_mov_b32_dpp v15, v17 row_ror:8 row_mask:0xf bank_mask:0x3\n"
        "buffer_store_dwordx4 v[12:15], v42, s[48:51], s76 offen sc1\n"
        "s_endpgm\n"
        ".Lslow2:\n"
        "v_cvt_pk_bf16_f32 v20, v192, v196\n"
        "v_cvt_pk_bf16_f32 v22, v194, v198\n"
        "v_cvt_pk_bf16_f32 v21, v200, v204\n"
        "v_cvt_pk_bf16_f32 v23, v202, v206\n"
        "v_cvt_pk_bf16_f32 v24, v193, v197\n"
        "v_cvt_pk_bf16_f32 v26, v195, v199\n"
        "v_cvt_pk_bf16_f32 v25, v201, v205\n"
        "v_cvt_pk_bf16_f32 v27, v203, v207\n"
        "v_permlane32_swap_b32 v20, v22\n"
        "v_permlane32_swap_b32 v21, v23\n"
        "v_permlane32_swap_b32 v24, v26\n"
        "v_permlane32_swap_b32 v25, v27\n"
        "buffer_store_dwordx4 v[20:23], v35, s[48:51], s76 offen offset:0 sc1\n"
        "buffer_store_dwordx4 v[24:27], v35, s[48:51], s76 offen offset:64 sc1\n"
        "s_add_u32 s76, s76, 0x2000\n"
        "s_waitcnt lgkmcnt(0)\n"
        "s_mov_b32 s74, 0x100000\n"
        ".Ltight:\n"
        "ds_read_b32 v8, v2 offset:45476\n"
        "ds_read_b128 v[224:227], v33 offset:0\n"
        "ds_read_b128 v[228:231], v33 offset:64\n"
        "s_sub_u32 s74, s74, 1\n"
        "s_cbranch_scc1 .Ltight_e\n"
        "s_waitcnt lgkmcnt(2)\n"
        "v_readfirstlane_b32 s73, v8\n"
        "s_cmp_lt_u32 s73, 4\n"
        "s_cbranch_scc1 .Ltight\n"
        ".Ltight_e:\n"
        "ds_read_b128 v[232:235], v33 offset:128\n"
        "ds_read_b128 v[236:239], v33 offset:192\n"
        "s_waitcnt lgkmcnt(3)\n"
        "v_smfmac_f32_16x16x64_bf16 v[208:211], v[224:227], v[44:51], v0\n"
        "v_smfmac_f32_16x16x64_bf16 v[212:215], v[224:227], v[52:59], v0\n"
        "v_smfmac_f32_16x16x64_bf16 v[216:219], v[224:227], v[60:67], v0\n"
        "v_smfmac_f32_16x16x64_bf16 v[220:223], v[224:227], v[68:75], v0\n"
        "s_waitcnt lgkmcnt(2)\n"
        "v_smfmac_f32_16x16x64_bf16 v[208:211], v[228:231], v[76:83], v0\n"
        "v_smfmac_f32_16x16x64_bf16 v[212:215], v[228:231], v[84:91], v0\n"
        "v_smfmac_f32_16x16x64_bf16 v[216:219], v[228:231], v[92:99], v0\n"
        "v_smfmac_f32_16x16x64_bf16 v[220:223], v[228:231], v[100:107], v0\n"
        "s_waitcnt lgkmcnt(1)\n"
        "v_smfmac_f32_16x16x64_bf16 v[208:211], v[232:235], v[108:115], v0\n"
        "v_smfmac_f32_16x16x64_bf16 v[212:215], v[232:235], v[116:123], v0\n"
        "v_smfmac_f32_16x16x64_bf16 v[216:219], v[232:235], v[124:131], v0\n"
        "v_smfmac_f32_16x16x64_bf16 v[220:223], v[232:235], v[132:139], v0\n"
        "s_waitcnt lgkmcnt(0)\n"
        "v_smfmac_f32_16x16x64_bf16 v[208:211], v[236:239], v[140:147], v0\n"
        "v_smfmac_f32_16x16x64_bf16 v[212:215], v[236:239], v[148:155], v0\n"
        "v_smfmac_f32_16x16x64_bf16 v[216:219], v[236:239], v[156:163], v0\n"
        "v_smfmac_f32_16x16x64_bf16 v[220:223], v[236:239], v[164:171], v0\n"
        "s_branch .Lfin\n"
        ".Lw_3:\n"
        "s_mov_b32 s74, 0x40000\n"
        ".Lw_3_1:\n"
        "ds_read_b32 v15, v2 offset:45500\n"
        "s_waitcnt lgkmcnt(0)\n"
        "v_readfirstlane_b32 s73, v15\n"
        "s_cmp_ge_u32 s73, 4\n"
        "s_cbranch_scc1 .Le_4\n"
        "s_sub_u32 s74, s74, 1\n"
        "s_cbranch_scc1 .Le_4\n"
        "s_sleep 1\n"
        "s_branch .Lw_3_1\n"
        ".Lw_5:\n"
        "s_mov_b32 s74, 0x40000\n"
        ".Lw_5_1:\n"
        "ds_read_b32 v113, v2 offset:45500\n"
        "s_waitcnt lgkmcnt(0)\n"
        "v_readfirstlane_b32 s73, v113\n"
        "s_cmp_ge_u32 s73, 4\n"
        "s_cbranch_scc1 .Le_6\n"
        "s_sub_u32 s74, s74, 1\n"
        "s_cbranch_scc1 .Le_6\n"
        "s_sleep 1\n"
        "s_branch .Lw_5_1\n"
        ".Lw_7:\n"
        "s_mov_b32 s74, 0x40000\n"
        ".Lw_7_1:\n"
        "ds_read_b32 v8, v2 offset:45516\n"
        "s_waitcnt lgkmcnt(0)\n"
        "v_readfirstlane_b32 s73, v8\n"
        "s_cmp_ge_u32 s73, 4\n"
        "s_cbranch_scc1 .Le_8\n"
        "s_sub_u32 s74, s74, 1\n"
        "s_cbranch_scc1 .Le_8\n"
        "s_sleep 1\n"
        "s_branch .Lw_7_1\n"
        ".Lw_9:\n"
        "s_mov_b32 s74, 0x40000\n"
        ".Lw_9_1:\n"
        "ds_read_b32 v86, v2 offset:45512\n"
        "s_waitcnt lgkmcnt(0)\n"
        "v_readfirstlane_b32 s73, v86\n"
        "s_cmp_ge_u32 s73, 4\n"
        "s_cbranch_scc1 .Le_10\n"
        "s_sub_u32 s74, s74, 1\n"
        "s_cbranch_scc1 .Le_10\n"
        "s_sleep 1\n"
        "s_branch .Lw_9_1\n"
        ".Lcn_again:\n"
        "s_sub_u32 s74, s74, 1\n"
        "s_cbranch_scc1 .Lcn_done\n"
        "s_branch .Lcn\n"
        ".Lw_11:\n"
        "s_mov_b32 s74, 0x40000\n"
        ".Lw_11_1:\n"
        "ds_read_b32 v40, v2 offset:45504\n"
        "s_waitcnt lgkmcnt(0)\n"
        "v_readfirstlane_b32 s73, v40\n"
        "s_cmp_ge_u32 s73, 4\n"
        "s_cbranch_scc1 .Le_12\n"
        "s_sub_u32 s74, s74, 1\n"
        "s_cbranch_scc1 .Le_12\n"
        "s_sleep 1\n"
        "s_branch .Lw_11_1\n"
        ".Lw_13:\n"
        "s_mov_b32 s74, 0x40000\n"
        ".Lw_13_1:\n"
        "ds_read_b32 v40, v2 offset:45520\n"
        "s_waitcnt lgkmcnt(0)\n"
        "v_readfirstlane_b32 s73, v40\n"
        "s_cmp_ge_u32 s73, 4\n"
        "s_cbranch_scc1 .Le_14\n"
        "s_sub_u32 s74, s74, 1\n"
        "s_cbranch_scc1 .Le_14\n"
        "s_sleep 1\n"
        "s_branch .Lw_13_1\n"
        ".Lw_15:\n"
        "s_mov_b32 s74, 0x40000\n"
        ".Lw_15_1:\n"
        "ds_read_b32 v28, v2 offset:45516\n"
        "s_waitcnt lgkmcnt(0)\n"
        "v_readfirstlane_b32 s73, v28\n"
        "s_cmp_ge_u32 s73, 4\n"
        "s_cbranch_scc1 .Le_16\n"
        "s_sub_u32 s74, s74, 1\n"
        "s_cbranch_scc1 .Le_16\n"
        "s_sleep 1\n"
        "s_branch .Lw_15_1\n"
        ".Lw_17:\n"
        "s_mov_b32 s74, 0x40000\n"
        ".Lw_17_1:\n"
        "ds_read_b32 v28, v2 offset:45500\n"
        "s_waitcnt lgkmcnt(0)\n"
        "v_readfirstlane_b32 s73, v28\n"
        "s_cmp_ge_u32 s73, 4\n"
        "s_cbranch_scc1 .Le_18\n"
        "s_sub_u32 s74, s74, 1\n"
        "s_cbranch_scc1 .Le_18\n"
        "s_sleep 1\n"
        "s_branch .Lw_17_1\n"
        ".Lw_20:\n"
        "s_mov_b32 s74, 0x40000\n"
        ".Lw_20_1:\n"
        "ds_read_b32 v28, v2 offset:45516\n"
        "s_waitcnt lgkmcnt(0)\n"
        "v_readfirstlane_b32 s73, v28\n"
        "s_cmp_ge_u32 s73, 4\n"
        "s_cbranch_scc1 .Le_21\n"
        "s_sub_u32 s74, s74, 1\n"
        "s_cbranch_scc1 .Le_21\n"
        "s_sleep 1\n"
        "s_branch .Lw_20_1\n"
        ".Lw_23:\n"
        "s_mov_b32 s74, 0x40000\n"
        ".Lw_23_1:\n"
        "ds_read_b32 v28, v2 offset:45516\n"
        "s_waitcnt lgkmcnt(0)\n"
        "v_readfirstlane_b32 s73, v28\n"
        "s_cmp_ge_u32 s73, 4\n"
        "s_cbranch_scc1 .Le_24\n"
        "s_sub_u32 s74, s74, 1\n"
        "s_cbranch_scc1 .Le_24\n"
        "s_sleep 1\n"
        "s_branch .Lw_23_1\n"
        ".Lw_26:\n"
        "s_mov_b32 s74, 0x40000\n"
        ".Lw_26_1:\n"
        "ds_read_b32 v8, v2 offset:45508\n"
        "s_waitcnt lgkmcnt(0)\n"
        "v_readfirstlane_b32 s73, v8\n"
        "s_cmp_ge_u32 s73, 4\n"
        "s_cbranch_scc1 .Le_27\n"
        "s_sub_u32 s74, s74, 1\n"
        "s_cbranch_scc1 .Le_27\n"
        "s_sleep 1\n"
        "s_branch .Lw_26_1\n"
        ".Lw_33:\n"
        "s_mov_b32 s74, 0x40000\n"
        ".Lw_33_1:\n"
        "ds_read_b32 v8, v2 offset:45496\n"
        "s_waitcnt lgkmcnt(0)\n"
        "v_readfirstlane_b32 s73, v8\n"
        "s_cmp_ge_u32 s73, 4\n"
        "s_cbranch_scc1 .Le_34\n"
        "s_sub_u32 s74, s74, 1\n"
        "s_cbranch_scc1 .Le_34\n"
        "s_sleep 1\n"
        "s_branch .Lw_33_1\n"
        ".Lw_35:\n"
        "s_mov_b32 s74, 0x40000\n"
        ".Lw_35_1:\n"
        "ds_read_b32 v8, v2 offset:45484\n"
        "s_waitcnt lgkmcnt(0)\n"
        "v_readfirstlane_b32 s73, v8\n"
        "s_cmp_ge_u32 s73, 4\n"
        "s_cbranch_scc1 .Le_36\n"
        "s_sub_u32 s74, s74, 1\n"
        "s_cbranch_scc1 .Le_36\n"
        "s_sleep 1\n"
        "s_branch .Lw_35_1\n"
        ".Lw_37:\n"
        "s_mov_b32 s74, 0x40000\n"
        ".Lw_37_1:\n"
        "ds_read_b32 v32, v2 offset:45532\n"
        "s_waitcnt lgkmcnt(0)\n"
        "v_readfirstlane_b32 s73, v32\n"
        "s_cmp_ge_u32 s73, 4\n"
        "s_cbranch_scc1 .Le_38\n"
        "s_sub_u32 s74, s74, 1\n"
        "s_cbranch_scc1 .Le_38\n"
        "s_sleep 1\n"
        "s_branch .Lw_37_1\n"
        ".Lw_39:\n"
        "s_mov_b32 s74, 0x40000\n"
        ".Lw_39_1:\n"
        "ds_read_b32 v8, v2 offset:45488\n"
        "s_waitcnt lgkmcnt(0)\n"
        "v_readfirstlane_b32 s73, v8\n"
        "s_cmp_ge_u32 s73, 4\n"
        "s_cbranch_scc1 .Le_40\n"
        "s_sub_u32 s74, s74, 1\n"
        "s_cbranch_scc1 .Le_40\n"
        "s_sleep 1\n"
        "s_branch .Lw_39_1\n"
        ".Lw_41:\n"
        "s_mov_b32 s74, 0x40000\n"
        ".Lw_41_1:\n"
        "ds_read_b32 v8, v2 offset:45452\n"
        "s_waitcnt lgkmcnt(0)\n"
        "v_readfirstlane_b32 s73, v8\n"
        "s_cmp_ge_u32 s73, 8\n"
        "s_cbranch_scc1 .Le_42\n"
        "s_sub_u32 s74, s74, 1\n"
        "s_cbranch_scc1 .Le_42\n"
        "s_sleep 1\n"
        "s_branch .Lw_41_1\n"
        ".Lw_43:\n"
        "s_mov_b32 s74, 0x40000\n"
        ".Lw_43_1:\n"
        "ds_read_b32 v8, v2 offset:45468\n"
        "s_waitcnt lgkmcnt(0)\n"
        "v_readfirstlane_b32 s73, v8\n"
        "s_cmp_ge_u32 s73, 4\n"
        "s_cbranch_scc1 .Le_44\n"
        "s_sub_u32 s74, s74, 1\n"
        "s_cbranch_scc1 .Le_44\n"
        "s_sleep 1\n"
        "s_branch .Lw_43_1\n"
        ".Lno_m:\n"
        "ds_add_u32 v2, v3 offset:45500\n"
        "s_waitcnt lgkmcnt(0)\n"
        "s_add_u32 s73, s64, 1\n"
        "s_addc_u32 s74, s65, 0\n"
        "s_mul_i32 s76, s73, 0x7f4a7c15\n"
        "s_mul_hi_u32 s77, s73, 0x7f4a7c15\n"
        "s_mul_i32 s75, s73, 0x9e3779b9\n"
        "s_add_u32 s77, s77, s75\n"
        "s_mul_i32 s75, s74, 0x7f4a7c15\n"
        "s_add_u32 s77, s77, s75\n"
        "s_xor_b32 s66, s76, 0xd192ed03\n"
        "s_or_b32 s66, s66, 3\n"
        "s_xor_b32 s67, s77, 0xd1b54a32\n"
        "s_xor_b32 s76, s76, 0x4f6cdd1d\n"
        "s_xor_b32 s77, s77, 0x2545f491\n"
        "s_and_b32 s73, s76, 3\n"
        "s_lshr_b32 s74, s76, 2\n"
        "s_lshl_b32 s74, s74, 23\n"
        "s_or_b32 s68, s73, s74\n"
        "s_lshr_b64 s[76:77], s[76:77], 11\n"
        "s_and_b32 s73, s76, 3\n"
        "s_lshr_b32 s74, s76, 2\n"
        "s_lshl_b32 s74, s74, 23\n"
        "s_or_b32 s69, s73, s74\n"
        "s_lshr_b64 s[76:77], s[76:77], 11\n"
        "s_and_b32 s73, s76, 3\n"
        "s_lshr_b32 s74, s76, 2\n"
        "s_lshl_b32 s74, s74, 23\n"
        "s_or_b32 s70, s73, s74\n"
        "s_lshr_b64 s[76:77], s[76:77], 11\n"
        "s_and_b32 s73, s76, 3\n"
        "s_lshr_b32 s74, s76, 2\n"
        "s_lshl_b32 s74, s74, 23\n"
        "s_or_b32 s71, s73, s74\n"
        "ds_add_u32 v2, v3 offset:45480\n"
        "s_cmp_ge_u32 s83, 200\n"
        "s_cbranch_scc1 .Lspecial_done\n"
        "s_waitcnt vmcnt(0)\n"
        "s_mov_b32 s78, 0x05040100\n"
        "s_mov_b32 s79, 0x07060302\n"
        "v_mov_b32 v172, v76\n"
        "v_mov_b32 v173, v77\n"
        "v_mov_b32 v174, v80\n"
        "v_mov_b32 v175, v81\n"
        "v_mov_b32_dpp v76, v78 row_ror:8 row_mask:0xf bank_mask:0xc\n"
        "v_mov_b32_dpp v77, v79 row_ror:8 row_mask:0xf bank_mask:0xc\n"
        "v_mov_b32_dpp v80, v82 row_ror:8 row_mask:0xf bank_mask:0xc\n"
        "v_mov_b32_dpp v81, v83 row_ror:8 row_mask:0xf bank_mask:0xc\n"
        "v_mov_b32_dpp v78, v172 row_ror:8 row_mask:0xf bank_mask:0x3\n"
        "v_mov_b32_dpp v79, v173 row_ror:8 row_mask:0xf bank_mask:0x3\n"
        "v_mov_b32_dpp v82, v174 row_ror:8 row_mask:0xf bank_mask:0x3\n"
        "v_mov_b32_dpp v83, v175 row_ror:8 row_mask:0xf bank_mask:0x3\n"
        "v_perm_b32 v44, v80, v76, s78\n"
        "v_perm_b32 v52, v80, v76, s79\n"
        "v_perm_b32 v60, v81, v77, s78\n"
        "v_perm_b32 v68, v81, v77, s79\n"
        "v_perm_b32 v48, v82, v78, s78\n"
        "v_perm_b32 v56, v82, v78, s79\n"
        "v_perm_b32 v64, v83, v79, s78\n"
        "v_perm_b32 v72, v83, v79, s79\n"
        "v_mov_b32 v172, v84\n"
        "v_mov_b32 v173, v85\n"
        "v_mov_b32 v174, v88\n"
        "v_mov_b32 v175, v89\n"
        "v_mov_b32_dpp v84, v86 row_ror:8 row_mask:0xf bank_mask:0xc\n"
        "v_mov_b32_dpp v85, v87 row_ror:8 row_mask:0xf bank_mask:0xc\n"
        "v_mov_b32_dpp v88, v90 row_ror:8 row_mask:0xf bank_mask:0xc\n"
        "v_mov_b32_dpp v89, v91 row_ror:8 row_mask:0xf bank_mask:0xc\n"
        "v_mov_b32_dpp v86, v172 row_ror:8 row_mask:0xf bank_mask:0x3\n"
        "v_mov_b32_dpp v87, v173 row_ror:8 row_mask:0xf bank_mask:0x3\n"
        "v_mov_b32_dpp v90, v174 row_ror:8 row_mask:0xf bank_mask:0x3\n"
        "v_mov_b32_dpp v91, v175 row_ror:8 row_mask:0xf bank_mask:0x3\n"
        "v_perm_b32 v45, v88, v84, s78\n"
        "v_perm_b32 v53, v88, v84, s79\n"
        "v_perm_b32 v61, v89, v85, s78\n"
        "v_perm_b32 v69, v89, v85, s79\n"
        "v_perm_b32 v49, v90, v86, s78\n"
        "v_perm_b32 v57, v90, v86, s79\n"
        "v_perm_b32 v65, v91, v87, s78\n"
        "v_perm_b32 v73, v91, v87, s79\n"
        "v_mov_b32 v172, v92\n"
        "v_mov_b32 v173, v93\n"
        "v_mov_b32 v174, v96\n"
        "v_mov_b32 v175, v97\n"
        "v_mov_b32_dpp v92, v94 row_ror:8 row_mask:0xf bank_mask:0xc\n"
        "v_mov_b32_dpp v93, v95 row_ror:8 row_mask:0xf bank_mask:0xc\n"
        "v_mov_b32_dpp v96, v98 row_ror:8 row_mask:0xf bank_mask:0xc\n"
        "v_mov_b32_dpp v97, v99 row_ror:8 row_mask:0xf bank_mask:0xc\n"
        "v_mov_b32_dpp v94, v172 row_ror:8 row_mask:0xf bank_mask:0x3\n"
        "v_mov_b32_dpp v95, v173 row_ror:8 row_mask:0xf bank_mask:0x3\n"
        "v_mov_b32_dpp v98, v174 row_ror:8 row_mask:0xf bank_mask:0x3\n"
        "v_mov_b32_dpp v99, v175 row_ror:8 row_mask:0xf bank_mask:0x3\n"
        "v_perm_b32 v46, v96, v92, s78\n"
        "v_perm_b32 v54, v96, v92, s79\n"
        "v_perm_b32 v62, v97, v93, s78\n"
        "v_perm_b32 v70, v97, v93, s79\n"
        "v_perm_b32 v50, v98, v94, s78\n"
        "v_perm_b32 v58, v98, v94, s79\n"
        "v_perm_b32 v66, v99, v95, s78\n"
        "v_perm_b32 v74, v99, v95, s79\n"
        "v_mov_b32 v172, v100\n"
        "v_mov_b32 v173, v101\n"
        "v_mov_b32 v174, v104\n"
        "v_mov_b32 v175, v105\n"
        "v_mov_b32_dpp v100, v102 row_ror:8 row_mask:0xf bank_mask:0xc\n"
        "v_mov_b32_dpp v101, v103 row_ror:8 row_mask:0xf bank_mask:0xc\n"
        "v_mov_b32_dpp v104, v106 row_ror:8 row_mask:0xf bank_mask:0xc\n"
        "v_mov_b32_dpp v105, v107 row_ror:8 row_mask:0xf bank_mask:0xc\n"
        "v_mov_b32_dpp v102, v172 row_ror:8 row_mask:0xf bank_mask:0x3\n"
        "v_mov_b32_dpp v103, v173 row_ror:8 row_mask:0xf bank_mask:0x3\n"
        "v_mov_b32_dpp v106, v174 row_ror:8 row_mask:0xf bank_mask:0x3\n"
        "v_mov_b32_dpp v107, v175 row_ror:8 row_mask:0xf bank_mask:0x3\n"
        "v_perm_b32 v47, v104, v100, s78\n"
        "v_perm_b32 v55, v104, v100, s79\n"
        "v_perm_b32 v63, v105, v101, s78\n"
        "v_perm_b32 v71, v105, v101, s79\n"
        "v_perm_b32 v51, v106, v102, s78\n"
        "v_perm_b32 v59, v106, v102, s79\n"
        "v_perm_b32 v67, v107, v103, s78\n"
        "v_perm_b32 v75, v107, v103, s79\n"
        "s_lshl_b32 s75, s72, 3\n"
        "s_add_u32 s75, s75, s83\n"
        "s_sub_u32 s75, s75, 192\n"
        "s_lshl_b32 s77, s75, 13\n"
        "s_add_u32 s77, s77, 139264\n"
        "s_add_u32 s76, s77, 0x1000\n"
        "s_lshl_b32 s78, s75, 3\n"
        "s_add_u32 s78, s78, 4352\n"
        "buffer_store_dwordx4 v[44:47], v4, s[44:47], s77 offen offset:0 sc1\n"
        "buffer_store_dwordx4 v[48:51], v4, s[44:47], s77 offen offset:1024 sc1\n"
        "buffer_store_dwordx4 v[52:55], v4, s[44:47], s77 offen offset:2048 sc1\n"
        "buffer_store_dwordx4 v[56:59], v4, s[44:47], s77 offen offset:3072 sc1\n"
        "buffer_store_dwordx4 v[60:63], v4, s[44:47], s76 offen offset:0 sc1\n"
        "buffer_store_dwordx4 v[64:67], v4, s[44:47], s76 offen offset:1024 sc1\n"
        "buffer_store_dwordx4 v[68:71], v4, s[44:47], s76 offen offset:2048 sc1\n"
        "buffer_store_dwordx4 v[72:75], v4, s[44:47], s76 offen offset:3072 sc1\n"
        "s_branch .Lflag_out\n"
        ".Len0:\n"
        "s_mov_b64 exec, s[36:37]\n"
        "v_and_or_b32 v16, v72, s17, v7\n"
        "v_lshrrev_b32 v32, 8, v72\n"
        "ds_add_rtn_u32 v16, v16, v46\n"
        "v_and_or_b32 v17, v73, s17, v7\n"
        "v_lshrrev_b32 v33, 8, v73\n"
        "ds_add_rtn_u32 v17, v17, v46\n"
        "v_and_or_b32 v18, v74, s17, v7\n"
        "v_lshrrev_b32 v34, 8, v74\n"
        "ds_add_rtn_u32 v18, v18, v46\n"
        "v_and_or_b32 v19, v75, s17, v7\n"
        "v_lshrrev_b32 v35, 8, v75\n"
        "ds_add_rtn_u32 v19, v19, v46\n"
        "s_mov_b64 exec, s[38:39]\n"
        "v_and_or_b32 v20, v76, s17, v7\n"
        "v_lshrrev_b32 v36, 8, v76\n"
        "ds_add_rtn_u32 v20, v20, v46\n"
        "v_and_or_b32 v21, v77, s17, v7\n"
        "v_lshrrev_b32 v37, 8, v77\n"
        "ds_add_rtn_u32 v21, v21, v46\n"
        "v_and_or_b32 v22, v78, s17, v7\n"
        "v_lshrrev_b32 v38, 8, v78\n"
        "ds_add_rtn_u32 v22, v22, v46\n"
        "v_and_or_b32 v23, v79, s17, v7\n"
        "v_lshrrev_b32 v39, 8, v79\n"
        "ds_add_rtn_u32 v23, v23, v46\n"
        "s_branch .Len0_e\n"
        ".Len1:\n"
        "s_mov_b64 exec, s[36:37]\n"
        "ds_read_u16 v32, v32\n"
        "ds_read_u16 v33, v33\n"
        "ds_read_u16 v34, v34\n"
        "ds_read_u16 v35, v35\n"
        "s_mov_b64 exec, s[38:39]\n"
        "ds_read_u16 v36, v36\n"
        "ds_read_u16 v37, v37\n"
        "ds_read_u16 v38, v38\n"
        "ds_read_u16 v39, v39\n"
        "s_branch .Len1_e\n"
        ".Lbp:\n"
        "ds_bpermute_b32 v100, v72, v83\n"
        "v_and_b32 v72, s73, v72\n"
        "ds_bpermute_b32 v101, v73, v83\n"
        "v_and_b32 v73, s73, v73\n"
        "ds_bpermute_b32 v102, v74, v83\n"
        "v_and_b32 v74, s73, v74\n"
        "ds_bpermute_b32 v103, v75, v83\n"
        "v_and_b32 v75, s73, v75\n"
        "ds_bpermute_b32 v104, v76, v83\n"
        "v_and_b32 v76, s73, v76\n"
        "ds_bpermute_b32 v105, v77, v83\n"
        "v_and_b32 v77, s73, v77\n"
        "ds_bpermute_b32 v106, v78, v83\n"
        "v_and_b32 v78, s73, v78\n"
        "ds_bpermute_b32 v107, v79, v83\n"
        "v_and_b32 v79, s73, v79\n"
        "s_branch .Lbp_e\n"
        ".Len2:\n"
        "s_mov_b64 exec, s[36:37]\n"
        "v_add_u32 v16, v16, v100\n"
        "ds_write2_b32 v16, v72, v32 offset0:3 offset1:113\n"
        "v_add_u32 v17, v17, v101\n"
        "ds_write2_b32 v17, v73, v33 offset0:3 offset1:113\n"
        "v_add_u32 v18, v18, v102\n"
        "ds_write2_b32 v18, v74, v34 offset0:3 offset1:113\n"
        "v_add_u32 v19, v19, v103\n"
        "ds_write2_b32 v19, v75, v35 offset0:3 offset1:113\n"
        "s_mov_b64 exec, s[38:39]\n"
        "v_add_u32 v20, v20, v104\n"
        "ds_write2_b32 v20, v76, v36 offset0:3 offset1:113\n"
        "v_add_u32 v21, v21, v105\n"
        "ds_write2_b32 v21, v77, v37 offset0:3 offset1:113\n"
        "v_add_u32 v22, v22, v106\n"
        "ds_write2_b32 v22, v78, v38 offset0:3 offset1:113\n"
        "v_add_u32 v23, v23, v107\n"
        "ds_write2_b32 v23, v79, v39 offset0:3 offset1:113\n"
        "s_branch .Len2_e\n"
        ".Lhint:\n"
        "s_setprio 3\n"
        "s_lshl_b32 s41, s72, 9\n"
        "s_add_u32 s41, s41, 2304\n"
        "s_mov_b32 exec_hi, 0\n"
        "buffer_load_dwordx4 v[24:27], v4, s[44:47], s41 offen sc1\n"
        "s_mov_b32 exec_hi, -1\n"
        "s_memrealtime s[76:77]\n"
        "s_waitcnt vmcnt(0) lgkmcnt(0)\n"
        "v_cmp_ne_u32 s[74:75], s66, v24\n"
        "v_cmp_ne_u32 vcc, s66, v26\n"
        "s_or_b32 s74, s74, vcc_lo\n"
        "s_cbranch_scc0 .Lhint_m\n"
        "s_sub_u32 s73, s76, s36\n"
        "s_cmp_gt_u32 s73, 20000\n"
        "s_cbranch_scc1 .Lhint_m\n"
        "s_sleep 6\n"
        "s_branch .Lhint\n"
        ".Lhint_m:\n"
        "buffer_load_dwordx4 v[224:227], v4, s[44:47], s8 offen offset:0 sc1\n"
        "buffer_load_dwordx4 v[228:231], v4, s[44:47], s10 offen offset:0 sc1\n"
        "buffer_load_dwordx4 v[232:235], v4, s[44:47], s12 offen offset:0 sc1\n"
        "buffer_load_dwordx4 v[236:239], v4, s[44:47], s14 offen offset:0 sc1\n"
        "buffer_load_dwordx4 v[240:243], v4, s[44:47], s8 offen offset:1024 sc1\n"
        "buffer_load_dwordx4 v[244:247], v4, s[44:47], s10 offen offset:1024 sc1\n"
        "buffer_load_dwordx4 v[248:251], v4, s[44:47], s12 offen offset:1024 sc1\n"
        "buffer_load_dwordx4 v[252:255], v4, s[44:47], s14 offen offset:1024 sc1\n"
        "buffer_load_dwordx4 v[156:159], v4, s[44:47], s8 offen offset:2048 sc1\n"
        "buffer_load_dwordx4 v[160:163], v4, s[44:47], s10 offen offset:2048 sc1\n"
        "buffer_load_dwordx4 v[164:167], v4, s[44:47], s12 offen offset:2048 sc1\n"
        "buffer_load_dwordx4 v[168:171], v4, s[44:47], s14 offen offset:2048 sc1\n"
        "buffer_load_dwordx4 v[140:143], v4, s[44:47], s8 offen offset:3072 sc1\n"
        "buffer_load_dwordx4 v[144:147], v4, s[44:47], s10 offen offset:3072 sc1\n"
        "buffer_load_dwordx4 v[148:151], v4, s[44:47], s12 offen offset:3072 sc1\n"
        "buffer_load_dwordx4 v[152:155], v4, s[44:47], s14 offen offset:3072 sc1\n"
        "s_setprio 1\n"
        "s_waitcnt vmcnt(0)\n"
        "s_branch .Lhint_e\n"
        ".Lpub_more:\n"
        "s_sub_u32 s75, s75, 0\n"
        "ds_read_b32 v14, v10 offset:0\n"
        "ds_read_b128 v[16:19], v10 offset:64\n"
        "s_waitcnt lgkmcnt(0)\n"
        "v_mov_b32_dpp v15, v14 quad_perm:[0,0,0,0] row_mask:0xf bank_mask:0xf\n"
        "s_movk_i32 s76, 0x2000\n"
        "v_cmp_le_u32 vcc, s76, v15\n"
        "s_mov_b64 exec, vcc\n"
        "v_or_b32 v16, s68, v16\n"
        "v_or_b32 v17, s69, v17\n"
        "v_or_b32 v18, s70, v18\n"
        "v_or_b32 v19, s71, v19\n"
        "buffer_store_dwordx4 v[16:19], v11, s[44:47], s75 offen sc1\n"
        "s_mov_b64 exec, -1\n"
        "s_add_u32 s75, s75, 4096\n"
        "ds_read_b32 v14, v10 offset:9216\n"
        "ds_read_b128 v[16:19], v10 offset:9280\n"
        "s_waitcnt lgkmcnt(0)\n"
        "v_mov_b32_dpp v15, v14 quad_perm:[0,0,0,0] row_mask:0xf bank_mask:0xf\n"
        "s_movk_i32 s76, 0x2000\n"
        "v_cmp_le_u32 vcc, s76, v15\n"
        "s_mov_b64 exec, vcc\n"
        "v_or_b32 v16, s68, v16\n"
        "v_or_b32 v17, s69, v17\n"
        "v_or_b32 v18, s70, v18\n"
        "v_or_b32 v19, s71, v19\n"
        "buffer_store_dwordx4 v[16:19], v11, s[44:47], s75 offen sc1\n"
        "s_mov_b64 exec, -1\n"
        "s_add_u32 s75, s75, 4096\n"
        "ds_read_b32 v14, v10 offset:18432\n"
        "ds_read_b128 v[16:19], v10 offset:18496\n"
        "s_waitcnt lgkmcnt(0)\n"
        "v_mov_b32_dpp v15, v14 quad_perm:[0,0,0,0] row_mask:0xf bank_mask:0xf\n"
        "s_movk_i32 s76, 0x2000\n"
        "v_cmp_le_u32 vcc, s76, v15\n"
        "s_mov_b64 exec, vcc\n"
        "v_or_b32 v16, s68, v16\n"
        "v_or_b32 v17, s69, v17\n"
        "v_or_b32 v18, s70, v18\n"
        "v_or_b32 v19, s71, v19\n"
        "buffer_store_dwordx4 v[16:19], v11, s[44:47], s75 offen sc1\n"
        "s_mov_b64 exec, -1\n"
        "s_add_u32 s75, s75, 4096\n"
        "ds_read_b32 v14, v10 offset:27648\n"
        "ds_read_b128 v[16:19], v10 offset:27712\n"
        "s_waitcnt lgkmcnt(0)\n"
        "v_mov_b32_dpp v15, v14 quad_perm:[0,0,0,0] row_mask:0xf bank_mask:0xf\n"
        "s_movk_i32 s76, 0x2000\n"
        "v_cmp_le_u32 vcc, s76, v15\n"
        "s_mov_b64 exec, vcc\n"
        "v_or_b32 v16, s68, v16\n"
        "v_or_b32 v17, s69, v17\n"
        "v_or_b32 v18, s70, v18\n"
        "v_or_b32 v19, s71, v19\n"
        "buffer_store_dwordx4 v[16:19], v11, s[44:47], s75 offen sc1\n"
        "s_mov_b64 exec, -1\n"
        "s_add_u32 s75, s75, 4096\n"
        "s_branch .Lpub_done\n"
        ".Lwf:\n"
        "s_sleep 6\n"
        "s_mov_b32 exec_hi, 15\n"
        "buffer_load_dwordx4 v[172:175], v42, s[44:47], 0 offen sc1\n"
        "s_mov_b64 exec, -1\n"
        "s_memrealtime s[80:81]\n"
        "s_waitcnt vmcnt(0) lgkmcnt(0)\n"
        "v_cmp_ne_u32 s[74:75], s66, v172\n"
        "v_cmp_ne_u32 s[76:77], s66, v174\n"
        "v_cmp_ne_u32 s[78:79], s67, v173\n"
        "v_cmp_ne_u32 vcc, s67, v175\n"
        "s_or_b64 s[74:75], s[74:75], s[76:77]\n"
        "s_or_b64 s[78:79], s[78:79], vcc\n"
        "s_or_b64 s[74:75], s[74:75], s[78:79]\n"
        "s_and_b32 s74, s75, 11\n"
        "s_cbranch_scc0 .Lwf_e\n"
        "s_sub_u32 s73, s80, s36\n"
        "s_cmp_le_u32 s73, 20000\n"
        "s_cbranch_scc1 .Lwf\n"
        "s_movk_i32 s61, 0x1000\n"
        "s_branch .Lwf_e\n"
        ".Lbk_more:\n"
        "buffer_load_dwordx4 v[72:75], v47, s[44:47], s42 offen sc1\n"
        "buffer_load_dwordx4 v[76:79], v47, s[44:47], s43 offen sc1\n"
        "s_waitcnt vmcnt(0)\n"
        "v_xor_b32 v72, s68, v72\n"
        "v_xor_b32 v73, s69, v73\n"
        "v_xor_b32 v74, s70, v74\n"
        "v_xor_b32 v75, s71, v75\n"
        "v_or3_b32 v8, v72, v73, v74\n"
        "v_or_b32 v8, v8, v75\n"
        "v_and_b32 v8, s73, v8\n"
        "v_cmp_ne_u32 s[74:75], 0, v8\n"
        "v_xor_b32 v76, s68, v76\n"
        "v_xor_b32 v77, s69, v77\n"
        "v_xor_b32 v78, s70, v78\n"
        "v_xor_b32 v79, s71, v79\n"
        "v_or3_b32 v9, v76, v77, v78\n"
        "v_or_b32 v9, v9, v79\n"
        "v_and_b32 v9, s73, v9\n"
        "v_cmp_ne_u32 s[76:77], 0, v9\n"
        "s_and_b64 s[74:75], s[74:75], s[36:37]\n"
        "s_and_b64 s[76:77], s[76:77], s[38:39]\n"
        "s_or_b64 s[74:75], s[74:75], s[76:77]\n"
        "s_cbranch_scc0 .Lbk_done\n"
        ".Lbk_again:\n"
        "s_memrealtime s[80:81]\n"
        "s_sleep 6\n"
        "buffer_load_dwordx4 v[64:67], v47, s[44:47], s40 offen sc1\n"
        "buffer_load_dwordx4 v[68:71], v47, s[44:47], s41 offen sc1\n"
        "s_waitcnt vmcnt(0) lgkmcnt(0)\n"
        "v_xor_b32 v64, s68, v64\n"
        "v_xor_b32 v65, s69, v65\n"
        "v_xor_b32 v66, s70, v66\n"
        "v_xor_b32 v67, s71, v67\n"
        "v_or3_b32 v8, v64, v65, v66\n"
        "v_or_b32 v8, v8, v67\n"
        "v_and_b32 v8, s73, v8\n"
        "v_cmp_ne_u32 s[74:75], 0, v8\n"
        "v_xor_b32 v68, s68, v68\n"
        "v_xor_b32 v69, s69, v69\n"
        "v_xor_b32 v70, s70, v70\n"
        "v_xor_b32 v71, s71, v71\n"
        "v_or3_b32 v9, v68, v69, v70\n"
        "v_or_b32 v9, v9, v71\n"
        "v_and_b32 v9, s73, v9\n"
        "v_cmp_ne_u32 s[76:77], 0, v9\n"
        "v_mov_b32_dpp v10, v64 quad_perm:[0,0,0,0] row_mask:0xf bank_mask:0xf\n"
        "v_mov_b32_dpp v11, v68 quad_perm:[0,0,0,0] row_mask:0xf bank_mask:0xf\n"
        "v_cmp_ge_u32 s[32:33], v10, v44\n"
        "v_cmp_le_u32 s[36:37], s82, v10\n"
        "v_cmp_ge_u32 s[34:35], v11, v44\n"
        "v_cmp_le_u32 s[38:39], s82, v11\n"
        "s_or_b64 s[78:79], s[74:75], s[76:77]\n"
        "s_sub_u32 s80, s80, s60\n"
        "s_cmp_le_u32 s80, 40000\n"
        "s_cbranch_scc1 .Lbk\n"
        "ds_add_u32 v2, v3 offset:45492\n"
        "s_mov_b64 s[32:33], 0\n"
        "s_mov_b64 s[34:35], 0\n"
        "s_mov_b64 s[80:81], 0\n"
        "s_branch .Lbk_done\n"
        ".Lfail_l:\n"
        "v_mov_b32 v8, 0x1000\n"
        "ds_add_u32 v2, v8 offset:45452\n"
        "s_endpgm\n"
        ".Lno_tile:\n"
        "s_waitcnt lgkmcnt(0)\n"
        "s_add_u32 s73, s64, 1\n"
        "s_addc_u32 s74, s65, 0\n"
        "s_mul_i32 s76, s73, 0x7f4a7c15\n"
        "s_mul_hi_u32 s77, s73, 0x7f4a7c15\n"
        "s_mul_i32 s75, s73, 0x9e3779b9\n"
        "s_add_u32 s77, s77, s75\n"
        "s_mul_i32 s75, s74, 0x7f4a7c15\n"
        "s_add_u32 s77, s77, s75\n"
        "s_xor_b32 s66, s76, 0xd192ed03\n"
        "s_or_b32 s66, s66, 3\n"
        "s_xor_b32 s67, s77, 0xd1b54a32\n"
        "ds_add_u32 v2, v3 offset:45480\n"
        "s_cmp_lg_u32 s3, 4\n"
        "s_cbranch_scc1 .Lflag_donex\n"
        "s_mov_b32 s74, 0x40000\n"
        ".Lepx:\n"
        "ds_read_b32 v28, v2 offset:45480\n"
        "s_waitcnt lgkmcnt(0)\n"
        "v_readfirstlane_b32 s73, v28\n"
        "s_and_b32 s75, s73, 255\n"
        "s_cmp_ge_u32 s75, 8\n"
        "s_cbranch_scc1 .Lep_donex\n"
        "s_sleep 1\n"
        "s_sub_u32 s74, s74, 1\n"
        "s_cbranch_scc0 .Lepx\n"
        ".Lep_donex:\n"
        "s_xor_b32 s74, s66, 2\n"
        "s_cmp_gt_u32 s73, 255\n"
        "s_cselect_b32 s35, s74, s66\n"
        "s_lshl_b32 s73, s2, 3\n"
        "s_add_u32 s73, s73, 256\n"
        "v_mov_b32 v28, s35\n"
        "v_mov_b32 v29, s67\n"
        "s_mov_b64 exec, 1\n"
        "buffer_store_dwordx2 v[28:29], v2, s[44:47], s73 offen sc1\n"
        "s_mov_b64 exec, -1\n"
        ".Lflag_donex:\n"
        "s_cmp_lg_u32 s3, 4\n"
        "s_cbranch_scc1 .Lend_l\n"
        "v_mov_b32 v5, v4\n"
        "s_movk_i32 s43, 256\n"
        "s_sleep 127\n"
        "s_sleep 60\n"
        ".Lagain:\n"
        "s_memrealtime s[80:81]\n"
        "s_mov_b32 s63, 0\n"
        ".Lagain2:\n"
        "s_sleep 8\n"
        "buffer_load_dwordx4 v[24:27], v5, s[44:47], s43 offen sc1\n"
        "buffer_load_dwordx4 v[28:31], v5, s[44:47], s43 offen offset:1024 sc1\n"
        "s_memrealtime s[82:83]\n"
        "s_waitcnt vmcnt(0) lgkmcnt(0)\n"
        "v_and_b32 v32, v24, v26\n"
        "v_and_b32 v33, v28, v30\n"
        "v_and_b32 v32, v32, v33\n"
        "v_or_b32 v24, 3, v24\n"
        "v_xor_b32 v24, s66, v24\n"
        "v_xor_b32 v25, s67, v25\n"
        "v_or_b32 v26, 3, v26\n"
        "v_xor_b32 v26, s66, v26\n"
        "v_xor_b32 v27, s67, v27\n"
        "v_or_b32 v28, 3, v28\n"
        "v_xor_b32 v28, s66, v28\n"
        "v_xor_b32 v29, s67, v29\n"
        "v_or_b32 v30, 3, v30\n"
        "v_xor_b32 v30, s66, v30\n"
        "v_xor_b32 v31, s67, v31\n"
        "v_or3_b32 v24, v24, v25, v26\n"
        "v_or3_b32 v27, v27, v28, v29\n"
        "v_or3_b32 v24, v24, v30, v31\n"
        "v_and_b32 v33, 1, v32\n"
        "v_or_b32 v24, v24, v27\n"
        "v_and_b32 v32, 2, v32\n"
        "v_cmp_eq_u32 s[74:75], 0, v33\n"
        "v_cmp_eq_u32 s[76:77], 0, v32\n"
        "v_cmp_ne_u32 vcc, 0, v24\n"
        "s_cmp_eq_u64 s[74:75], 0\n"
        "s_cselect_b32 s62, 1, 0\n"
        "s_cmp_eq_u64 s[76:77], 0\n"
        "s_cselect_b32 s73, 2, 0\n"
        "s_or_b32 s62, s62, s73\n"
        "s_cmp_eq_u64 vcc, 0\n"
        "s_cselect_b32 s73, 4, 0\n"
        "s_or_b32 s62, s62, s73\n"
        "s_bitcmp1_b32 s62, 2\n"
        "s_cbranch_scc1 .Lall\n"
        "s_cmp_lg_u32 s63, 0\n"
        "s_cbranch_scc1 .Ltell\n"
        "s_sub_u32 s73, s82, s80\n"
        "s_cmp_le_u32 s73, 20000\n"
        "s_cbranch_scc1 .Lagain2\n"
        "s_add_u32 s75, s2, 1\n"
        "s_cmp_lg_u32 s75, s56\n"
        "s_cbranch_scc1 .Ltell\n"
        "s_xor_b32 s73, s35, 1\n"
        "s_lshl_b32 s74, s2, 3\n"
        "s_add_u32 s74, s74, 256\n"
        "v_mov_b32 v24, s73\n"
        "v_mov_b32 v25, s67\n"
        "s_mov_b64 exec, 1\n"
        "buffer_store_dwordx2 v[24:25], v2, s[44:47], s74 offen sc1\n"
        "s_mov_b64 exec, -1\n"
        "s_mov_b32 s63, 1\n"
        "s_waitcnt vmcnt(0)\n"
        "s_branch .Lagain2\n"
        ".Lcode_end:\n"
        ".Lqm_again:\n"
        "s_sleep 6\n"
        "s_memrealtime s[80:81]\n"
        "s_waitcnt lgkmcnt(0)\n"
        "s_sub_u32 s73, s80, s36\n"
        "s_cmp_gt_u32 s73, 20000\n"
        "s_cselect_b32 s61, 0x1000, s61\n"
        "s_cbranch_scc1 .Lqm_done\n"
        "s_setprio 3\n"
        "s_mov_b32 exec_hi, 15\n"
        "buffer_load_dwordx4 v[172:175], v42, s[44:47], 0 offen sc1\n"
        "s_mov_b64 exec, -1\n"
        "buffer_load_dwordx4 v[224:227], v4, s[44:47], s8 offen offset:0 sc1\n"
        "buffer_load_dwordx4 v[228:231], v4, s[44:47], s10 offen offset:0 sc1\n"
        "buffer_load_dwordx4 v[232:235], v4, s[44:47], s12 offen offset:0 sc1\n"
        "buffer_load_dwordx4 v[236:239], v4, s[44:47], s14 offen offset:0 sc1\n"
        "buffer_load_dwordx4 v[240:243], v4, s[44:47], s8 offen offset:1024 sc1\n"
        "buffer_load_dwordx4 v[244:247], v4, s[44:47], s10 offen offset:1024 sc1\n"
        "buffer_load_dwordx4 v[248:251], v4, s[44:47], s12 offen offset:1024 sc1\n"
        "buffer_load_dwordx4 v[252:255], v4, s[44:47], s14 offen offset:1024 sc1\n"
        "buffer_load_dwordx4 v[156:159], v4, s[44:47], s8 offen offset:2048 sc1\n"
        "buffer_load_dwordx4 v[160:163], v4, s[44:47], s10 offen offset:2048 sc1\n"
        "buffer_load_dwordx4 v[164:167], v4, s[44:47], s12 offen offset:2048 sc1\n"
        "buffer_load_dwordx4 v[168:171], v4, s[44:47], s14 offen offset:2048 sc1\n"
        "buffer_load_dwordx4 v[140:143], v4, s[44:47], s8 offen offset:3072 sc1\n"
        "buffer_load_dwordx4 v[144:147], v4, s[44:47], s10 offen offset:3072 sc1\n"
        "buffer_load_dwordx4 v[148:151], v4, s[44:47], s12 offen offset:3072 sc1\n"
        "buffer_load_dwordx4 v[152:155], v4, s[44:47], s14 offen offset:3072 sc1\n"
        "ds_read_b128 v[8:11], v36 offset:0\n"
        "ds_read_b128 v[12:15], v36 offset:8192\n"
        "ds_read_b128 v[16:19], v36 offset:16384\n"
        "s_waitcnt vmcnt(12)\n"
        "s_waitcnt lgkmcnt(2)\n"
        "v_mfma_f32_16x16x32_bf16 v[176:179], v[224:227], v[8:11], 0\n"
        "v_add_u32 v43, v224, v225\n"
        "v_mfma_f32_16x16x32_bf16 v[180:183], v[228:231], v[8:11], 0\n"
        "v_add3_u32 v43, v43, v226, v227\n"
        "v_mfma_f32_16x16x32_bf16 v[184:187], v[232:235], v[8:11], 0\n"
        "v_add3_u32 v43, v43, v228, v229\n"
        "v_mfma_f32_16x16x32_bf16 v[188:191], v[236:239], v[8:11], 0\n"
        "v_add3_u32 v43, v43, v230, v231\n"
        "ds_read_b128 v[8:11], v37 offset:0\n"
        "s_waitcnt lgkmcnt(2)\n"
        "v_mfma_f32_16x16x32_bf16 v[192:195], v[224:227], v[12:15], 0\n"
        "v_add3_u32 v43, v43, v232, v233\n"
        "v_mfma_f32_16x16x32_bf16 v[196:199], v[228:231], v[12:15], 0\n"
        "v_add3_u32 v43, v43, v234, v235\n"
        "v_mfma_f32_16x16x32_bf16 v[200:203], v[232:235], v[12:15], 0\n"
        "v_add3_u32 v43, v43, v236, v237\n"
        "v_mfma_f32_16x16x32_bf16 v[204:207], v[236:239], v[12:15], 0\n"
        "v_add3_u32 v43, v43, v238, v239\n"
        "ds_read_b128 v[12:15], v37 offset:8192\n"
        "s_waitcnt lgkmcnt(2)\n"
        "v_mfma_f32_16x16x32_bf16 v[208:211], v[224:227], v[16:19], 0\n"
        "v_mfma_f32_16x16x32_bf16 v[212:215], v[228:231], v[16:19], 0\n"
        "v_mfma_f32_16x16x32_bf16 v[216:219], v[232:235], v[16:19], 0\n"
        "v_mfma_f32_16x16x32_bf16 v[220:223], v[236:239], v[16:19], 0\n"
        "ds_read_b128 v[16:19], v37 offset:16384\n"
        "buffer_load_dwordx4 v[224:227], v4, s[44:47], s9 offen offset:0 sc1\n"
        "buffer_load_dwordx4 v[228:231], v4, s[44:47], s11 offen offset:0 sc1\n"
        "buffer_load_dwordx4 v[232:235], v4, s[44:47], s13 offen offset:0 sc1\n"
        "buffer_load_dwordx4 v[236:239], v4, s[44:47], s15 offen offset:0 sc1\n"
        "s_waitcnt vmcnt(12)\n"
        "s_waitcnt lgkmcnt(2)\n"
        "v_mfma_f32_16x16x32_bf16 v[176:179], v[240:243], v[8:11], v[176:179]\n"
        "v_add3_u32 v43, v43, v240, v241\n"
        "v_mfma_f32_16x16x32_bf16 v[180:183], v[244:247], v[8:11], v[180:183]\n"
        "v_add3_u32 v43, v43, v242, v243\n"
        "v_mfma_f32_16x16x32_bf16 v[184:187], v[248:251], v[8:11], v[184:187]\n"
        "v_add3_u32 v43, v43, v244, v245\n"
        "v_mfma_f32_16x16x32_bf16 v[188:191], v[252:255], v[8:11], v[188:191]\n"
        "v_add3_u32 v43, v43, v246, v247\n"
        "ds_read_b128 v[8:11], v38 offset:0\n"
        "s_waitcnt lgkmcnt(2)\n"
        "v_mfma_f32_16x16x32_bf16 v[192:195], v[240:243], v[12:15], v[192:195]\n"
        "v_add3_u32 v43, v43, v248, v249\n"
        "v_mfma_f32_16x16x32_bf16 v[196:199], v[244:247], v[12:15], v[196:199]\n"
        "v_add3_u32 v43, v43, v250, v251\n"
        "v_mfma_f32_16x16x32_bf16 v[200:203], v[248:251], v[12:15], v[200:203]\n"
        "v_add3_u32 v43, v43, v252, v253\n"
        "v_mfma_f32_16x16x32_bf16 v[204:207], v[252:255], v[12:15], v[204:207]\n"
        "v_add3_u32 v43, v43, v254, v255\n"
        "ds_read_b128 v[12:15], v38 offset:8192\n"
        "s_waitcnt lgkmcnt(2)\n"
        "v_mfma_f32_16x16x32_bf16 v[208:211], v[240:243], v[16:19], v[208:211]\n"
        "v_mfma_f32_16x16x32_bf16 v[212:215], v[244:247], v[16:19], v[212:215]\n"
        "v_mfma_f32_16x16x32_bf16 v[216:219], v[248:251], v[16:19], v[216:219]\n"
        "v_mfma_f32_16x16x32_bf16 v[220:223], v[252:255], v[16:19], v[220:223]\n"
        "ds_read_b128 v[16:19], v38 offset:16384\n"
        "buffer_load_dwordx4 v[240:243], v4, s[44:47], s9 offen offset:1024 sc1\n"
        "buffer_load_dwordx4 v[244:247], v4, s[44:47], s11 offen offset:1024 sc1\n"
        "buffer_load_dwordx4 v[248:251], v4, s[44:47], s13 offen offset:1024 sc1\n"
        "buffer_load_dwordx4 v[252:255], v4, s[44:47], s15 offen offset:1024 sc1\n"
        "s_waitcnt vmcnt(12)\n"
        "s_waitcnt lgkmcnt(2)\n"
        "v_mfma_f32_16x16x32_bf16 v[176:179], v[156:159], v[8:11], v[176:179]\n"
        "v_add3_u32 v43, v43, v156, v157\n"
        "v_mfma_f32_16x16x32_bf16 v[180:183], v[160:163], v[8:11], v[180:183]\n"
        "v_add3_u32 v43, v43, v158, v159\n"
        "v_mfma_f32_16x16x32_bf16 v[184:187], v[164:167], v[8:11], v[184:187]\n"
        "v_add3_u32 v43, v43, v160, v161\n"
        "v_mfma_f32_16x16x32_bf16 v[188:191], v[168:171], v[8:11], v[188:191]\n"
        "v_add3_u32 v43, v43, v162, v163\n"
        "ds_read_b128 v[8:11], v39 offset:0\n"
        "s_waitcnt lgkmcnt(2)\n"
        "v_mfma_f32_16x16x32_bf16 v[192:195], v[156:159], v[12:15], v[192:195]\n"
        "v_add3_u32 v43, v43, v164, v165\n"
        "v_mfma_f32_16x16x32_bf16 v[196:199], v[160:163], v[12:15], v[196:199]\n"
        "v_add3_u32 v43, v43, v166, v167\n"
        "v_mfma_f32_16x16x32_bf16 v[200:203], v[164:167], v[12:15], v[200:203]\n"
        "v_add3_u32 v43, v43, v168, v169\n"
        "v_mfma_f32_16x16x32_bf16 v[204:207], v[168:171], v[12:15], v[204:207]\n"
        "v_add3_u32 v43, v43, v170, v171\n"
        "ds_read_b128 v[12:15], v39 offset:8192\n"
        "s_waitcnt lgkmcnt(2)\n"
        "v_mfma_f32_16x16x32_bf16 v[208:211], v[156:159], v[16:19], v[208:211]\n"
        "v_mfma_f32_16x16x32_bf16 v[212:215], v[160:163], v[16:19], v[212:215]\n"
        "v_mfma_f32_16x16x32_bf16 v[216:219], v[164:167], v[16:19], v[216:219]\n"
        "v_mfma_f32_16x16x32_bf16 v[220:223], v[168:171], v[16:19], v[220:223]\n"
        "ds_read_b128 v[16:19], v39 offset:16384\n"
        "buffer_load_dwordx4 v[156:159], v4, s[44:47], s9 offen offset:2048 sc1\n"
        "buffer_load_dwordx4 v[160:163], v4, s[44:47], s11 offen offset:2048 sc1\n"
        "buffer_load_dwordx4 v[164:167], v4, s[44:47], s13 offen offset:2048 sc1\n"
        "buffer_load_dwordx4 v[168:171], v4, s[44:47], s15 offen offset:2048 sc1\n"
        "s_waitcnt vmcnt(12)\n"
        "s_waitcnt lgkmcnt(2)\n"
        "v_mfma_f32_16x16x32_bf16 v[176:179], v[140:143], v[8:11], v[176:179]\n"
        "v_add3_u32 v43, v43, v140, v141\n"
        "v_mfma_f32_16x16x32_bf16 v[180:183], v[144:147], v[8:11], v[180:183]\n"
        "v_add3_u32 v43, v43, v142, v143\n"
        "v_mfma_f32_16x16x32_bf16 v[184:187], v[148:151], v[8:11], v[184:187]\n"
        "v_add3_u32 v43, v43, v144, v145\n"
        "v_mfma_f32_16x16x32_bf16 v[188:191], v[152:155], v[8:11], v[188:191]\n"
        "v_add3_u32 v43, v43, v146, v147\n"
        "ds_read_b128 v[8:11], v36 offset:256\n"
        "s_waitcnt lgkmcnt(2)\n"
        "v_mfma_f32_16x16x32_bf16 v[192:195], v[140:143], v[12:15], v[192:195]\n"
        "v_add3_u32 v43, v43, v148, v149\n"
        "v_mfma_f32_16x16x32_bf16 v[196:199], v[144:147], v[12:15], v[196:199]\n"
        "v_add3_u32 v43, v43, v150, v151\n"
        "v_mfma_f32_16x16x32_bf16 v[200:203], v[148:151], v[12:15], v[200:203]\n"
        "v_add3_u32 v43, v43, v152, v153\n"
        "v_mfma_f32_16x16x32_bf16 v[204:207], v[152:155], v[12:15], v[204:207]\n"
        "v_add3_u32 v43, v43, v154, v155\n"
        "ds_read_b128 v[12:15], v36 offset:8448\n"
        "s_waitcnt lgkmcnt(2)\n"
        "v_mfma_f32_16x16x32_bf16 v[208:211], v[140:143], v[16:19], v[208:211]\n"
        "v_mfma_f32_16x16x32_bf16 v[212:215], v[144:147], v[16:19], v[212:215]\n"
        "v_mfma_f32_16x16x32_bf16 v[216:219], v[148:151], v[16:19], v[216:219]\n"
        "v_mfma_f32_16x16x32_bf16 v[220:223], v[152:155], v[16:19], v[220:223]\n"
        "ds_read_b128 v[16:19], v36 offset:16640\n"
        "buffer_load_dwordx4 v[140:143], v4, s[44:47], s9 offen offset:3072 sc1\n"
        "buffer_load_dwordx4 v[144:147], v4, s[44:47], s11 offen offset:3072 sc1\n"
        "buffer_load_dwordx4 v[148:151], v4, s[44:47], s13 offen offset:3072 sc1\n"
        "buffer_load_dwordx4 v[152:155], v4, s[44:47], s15 offen offset:3072 sc1\n"
        "s_waitcnt vmcnt(12)\n"
        "s_waitcnt lgkmcnt(2)\n"
        "v_mfma_f32_16x16x32_bf16 v[176:179], v[224:227], v[8:11], v[176:179]\n"
        "v_add3_u32 v43, v43, v224, v225\n"
        "v_mfma_f32_16x16x32_bf16 v[180:183], v[228:231], v[8:11], v[180:183]\n"
        "v_add3_u32 v43, v43, v226, v227\n"
        "v_mfma_f32_16x16x32_bf16 v[184:187], v[232:235], v[8:11], v[184:187]\n"
        "v_add3_u32 v43, v43, v228, v229\n"
        "v_mfma_f32_16x16x32_bf16 v[188:191], v[236:239], v[8:11], v[188:191]\n"
        "v_add3_u32 v43, v43, v230, v231\n"
        "ds_read_b128 v[8:11], v37 offset:256\n"
        "s_waitcnt lgkmcnt(2)\n"
        "v_mfma_f32_16x16x32_bf16 v[192:195], v[224:227], v[12:15], v[192:195]\n"
        "v_add3_u32 v43, v43, v232, v233\n"
        "v_mfma_f32_16x16x32_bf16 v[196:199], v[228:231], v[12:15], v[196:199]\n"
        "v_add3_u32 v43, v43, v234, v235\n"
        "v_mfma_f32_16x16x32_bf16 v[200:203], v[232:235], v[12:15], v[200:203]\n"
        "v_add3_u32 v43, v43, v236, v237\n"
        "v_mfma_f32_16x16x32_bf16 v[204:207], v[236:239], v[12:15], v[204:207]\n"
        "v_add3_u32 v43, v43, v238, v239\n"
        "ds_read_b128 v[12:15], v37 offset:8448\n"
        "s_waitcnt lgkmcnt(2)\n"
        "v_mfma_f32_16x16x32_bf16 v[208:211], v[224:227], v[16:19], v[208:211]\n"
        "v_mfma_f32_16x16x32_bf16 v[212:215], v[228:231], v[16:19], v[212:215]\n"
        "v_mfma_f32_16x16x32_bf16 v[216:219], v[232:235], v[16:19], v[216:219]\n"
        "v_mfma_f32_16x16x32_bf16 v[220:223], v[236:239], v[16:19], v[220:223]\n"
        "ds_read_b128 v[16:19], v37 offset:16640\n"
        "s_waitcnt vmcnt(8)\n"
        "s_waitcnt lgkmcnt(2)\n"
        "v_mfma_f32_16x16x32_bf16 v[176:179], v[240:243], v[8:11], v[176:179]\n"
        "v_add3_u32 v43, v43, v240, v241\n"
        "v_mfma_f32_16x16x32_bf16 v[180:183], v[244:247], v[8:11], v[180:183]\n"
        "v_add3_u32 v43, v43, v242, v243\n"
        "v_mfma_f32_16x16x32_bf16 v[184:187], v[248:251], v[8:11], v[184:187]\n"
        "v_add3_u32 v43, v43, v244, v245\n"
        "v_mfma_f32_16x16x32_bf16 v[188:191], v[252:255], v[8:11], v[188:191]\n"
        "v_add3_u32 v43, v43, v246, v247\n"
        "ds_read_b128 v[8:11], v38 offset:256\n"
        "s_waitcnt lgkmcnt(2)\n"
        "v_mfma_f32_16x16x32_bf16 v[192:195], v[240:243], v[12:15], v[192:195]\n"
        "v_add3_u32 v43, v43, v248, v249\n"
        "v_mfma_f32_16x16x32_bf16 v[196:199], v[244:247], v[12:15], v[196:199]\n"
        "v_add3_u32 v43, v43, v250, v251\n"
        "v_mfma_f32_16x16x32_bf16 v[200:203], v[248:251], v[12:15], v[200:203]\n"
        "v_add3_u32 v43, v43, v252, v253\n"
        "v_mfma_f32_16x16x32_bf16 v[204:207], v[252:255], v[12:15], v[204:207]\n"
        "v_add3_u32 v43, v43, v254, v255\n"
        "ds_read_b128 v[12:15], v38 offset:8448\n"
        "s_waitcnt lgkmcnt(2)\n"
        "v_mfma_f32_16x16x32_bf16 v[208:211], v[240:243], v[16:19], v[208:211]\n"
        "v_mfma_f32_16x16x32_bf16 v[212:215], v[244:247], v[16:19], v[212:215]\n"
        "v_mfma_f32_16x16x32_bf16 v[216:219], v[248:251], v[16:19], v[216:219]\n"
        "v_mfma_f32_16x16x32_bf16 v[220:223], v[252:255], v[16:19], v[220:223]\n"
        "ds_read_b128 v[16:19], v38 offset:16640\n"
        "s_waitcnt vmcnt(4)\n"
        "s_waitcnt lgkmcnt(2)\n"
        "v_mfma_f32_16x16x32_bf16 v[176:179], v[156:159], v[8:11], v[176:179]\n"
        "v_add3_u32 v43, v43, v156, v157\n"
        "v_mfma_f32_16x16x32_bf16 v[180:183], v[160:163], v[8:11], v[180:183]\n"
        "v_add3_u32 v43, v43, v158, v159\n"
        "v_mfma_f32_16x16x32_bf16 v[184:187], v[164:167], v[8:11], v[184:187]\n"
        "v_add3_u32 v43, v43, v160, v161\n"
        "v_mfma_f32_16x16x32_bf16 v[188:191], v[168:171], v[8:11], v[188:191]\n"
        "v_add3_u32 v43, v43, v162, v163\n"
        "ds_read_b128 v[8:11], v39 offset:256\n"
        "s_waitcnt lgkmcnt(2)\n"
        "v_mfma_f32_16x16x32_bf16 v[192:195], v[156:159], v[12:15], v[192:195]\n"
        "v_add3_u32 v43, v43, v164, v165\n"
        "v_mfma_f32_16x16x32_bf16 v[196:199], v[160:163], v[12:15], v[196:199]\n"
        "v_add3_u32 v43, v43, v166, v167\n"
        "v_mfma_f32_16x16x32_bf16 v[200:203], v[164:167], v[12:15], v[200:203]\n"
        "v_add3_u32 v43, v43, v168, v169\n"
        "v_mfma_f32_16x16x32_bf16 v[204:207], v[168:171], v[12:15], v[204:207]\n"
        "v_add3_u32 v43, v43, v170, v171\n"
        "ds_read_b128 v[12:15], v39 offset:8448\n"
        "s_waitcnt lgkmcnt(2)\n"
        "v_mfma_f32_16x16x32_bf16 v[208:211], v[156:159], v[16:19], v[208:211]\n"
        "v_mfma_f32_16x16x32_bf16 v[212:215], v[160:163], v[16:19], v[212:215]\n"
        "v_mfma_f32_16x16x32_bf16 v[216:219], v[164:167], v[16:19], v[216:219]\n"
        "v_mfma_f32_16x16x32_bf16 v[220:223], v[168:171], v[16:19], v[220:223]\n"
        "ds_read_b128 v[16:19], v39 offset:16640\n"
        "s_waitcnt vmcnt(0)\n"
        "s_waitcnt lgkmcnt(2)\n"
        "v_mfma_f32_16x16x32_bf16 v[176:179], v[140:143], v[8:11], v[176:179]\n"
        "v_add3_u32 v43, v43, v140, v141\n"
        "v_add3_u32 v43, v43, v142, v143\n"
        "v_mfma_f32_16x16x32_bf16 v[180:183], v[144:147], v[8:11], v[180:183]\n"
        "v_add3_u32 v43, v43, v144, v145\n"
        "v_add3_u32 v43, v43, v146, v147\n"
        "v_mfma_f32_16x16x32_bf16 v[184:187], v[148:151], v[8:11], v[184:187]\n"
        "v_add3_u32 v43, v43, v148, v149\n"
        "v_add3_u32 v43, v43, v150, v151\n"
        "v_mfma_f32_16x16x32_bf16 v[188:191], v[152:155], v[8:11], v[188:191]\n"
        "v_add3_u32 v43, v43, v152, v153\n"
        "v_add3_u32 v43, v43, v154, v155\n"
        "s_waitcnt lgkmcnt(1)\n"
        "v_mfma_f32_16x16x32_bf16 v[192:195], v[140:143], v[12:15], v[192:195]\n"
        "s_mov_b32 exec_hi, 0\n"
        "v_sub_u32 v43, v43, v173\n"
        "v_cmp_ne_u32 s[74:75], s66, v172\n"
        "v_sub_u32 v43, v43, v175\n"
        "v_cmp_ne_u32 s[76:77], s66, v174\n"
        "s_mov_b32 exec_hi, -1\n"
        "v_add_u32_dpp v43, v43, v43 row_shr:1 row_mask:0xf bank_mask:0xf bound_ctrl:0\n"
        "v_mfma_f32_16x16x32_bf16 v[196:199], v[144:147], v[12:15], v[196:199]\n"
        "s_nop 0\n"
        "v_add_u32_dpp v43, v43, v43 row_shr:2 row_mask:0xf bank_mask:0xf bound_ctrl:0\n"
        "s_nop 1\n"
        "v_add_u32_dpp v43, v43, v43 row_shr:4 row_mask:0xf bank_mask:0xf bound_ctrl:0\n"
        "v_mfma_f32_16x16x32_bf16 v[200:203], v[148:151], v[12:15], v[200:203]\n"
        "s_nop 0\n"
        "v_add_u32_dpp v43, v43, v43 row_shr:8 row_mask:0xf bank_mask:0xf bound_ctrl:0\n"
        "s_nop 1\n"
        "v_add_u32_dpp v43, v43, v43 row_bcast:15 row_mask:0xa bank_mask:0xf\n"
        "v_mfma_f32_16x16x32_bf16 v[204:207], v[152:155], v[12:15], v[204:207]\n"
        "s_nop 0\n"
        "v_add_u32_dpp v43, v43, v43 row_bcast:31 row_mask:0xc bank_mask:0xf\n"
        "s_lshl_b32 s75, s67, 6\n"
        "s_or_b32 s74, s74, s76\n"
        "s_waitcnt lgkmcnt(0)\n"
        "v_mfma_f32_16x16x32_bf16 v[208:211], v[140:143], v[16:19], v[208:211]\n"
        "v_readlane_b32 s73, v43, 63\n"
        "s_add_u32 s73, s73, s75\n"
        "s_or_b32 s73, s73, s74\n"
        "v_mfma_f32_16x16x32_bf16 v[212:215], v[144:147], v[16:19], v[212:215]\n"
        "v_mfma_f32_16x16x32_bf16 v[216:219], v[148:151], v[16:19], v[216:219]\n"
        "v_mfma_f32_16x16x32_bf16 v[220:223], v[152:155], v[16:19], v[220:223]\n"
        "s_setprio 1\n"
        "s_cbranch_scc1 .Lqm_again\n"
        "s_branch .Lqm_done\n"
        ".Lexit:\n"
        "s_mov_b64 exec, -1\n"
        "s_setprio 0\n"
        "s_waitcnt vmcnt(0) lgkmcnt(0)\n"
        : "={s3}"(wave), "+{v0}"(tx), "+{s[0:1]}"(kp), "+{s2}"(tile)
        :
        :
          "v1", "v2", "v3", "v4", "v5", "v6", "v7", "v8", "v9", "v10", "v11", "v12", "v13", "v14", "v15", "v16",
          "v17", "v18", "v19", "v20", "v21", "v22", "v23", "v24", "v25", "v26", "v27", "v28", "v29", "v30", "v31", "v32",
          "v33", "v34", "v35", "v36", "v37", "v38", "v39", "v40", "v41", "v42", "v43", "v44", "v45", "v46", "v47", "v48",
          "v49", "v50", "v51", "v52", "v53", "v54", "v55", "v56", "v57", "v58", "v59", "v60", "v61", "v62", "v63", "v64",
          "v65", "v66", "v67", "v68", "v69", "v70", "v71", "v72", "v73", "v74", "v75", "v76", "v77", "v78", "v79", "v80",
          "v81", "v82", "v83", "v84", "v85", "v86", "v87", "v88", "v89", "v90", "v91", "v92", "v93", "v94", "v95", "v96",
          "v97", "v98", "v99", "v100", "v101", "v102", "v103", "v104", "v105", "v106", "v107", "v108", "v109", "v110", "v111", "v112",
          "v113", "v114", "v115", "v116", "v117", "v118", "v119", "v120", "v121", "v122", "v123", "v124", "v125", "v126", "v127", "v128",
          "v129", "v130", "v131", "v132", "v133", "v134", "v135", "v136", "v137", "v138", "v139", "v140", "v141", "v142", "v143", "v144",
          "v145", "v146", "v147", "v148", "v149", "v150", "v151", "v152", "v153", "v154", "v155", "v156", "v157", "v158", "v159", "v160",
          "v161", "v162", "v163", "v164", "v165", "v166", "v167", "v168", "v169", "v170", "v171", "v172", "v173", "v174", "v175", "v176",
          "v177", "v178", "v179", "v180", "v181", "v182", "v183", "v184", "v185", "v186", "v187", "v188", "v189", "v190", "v191", "v192",
          "v193", "v194", "v195", "v196", "v197", "v198", "v199", "v200", "v201", "v202", "v203", "v204", "v205", "v206", "v207", "v208",
          "v209", "v210", "v211", "v212", "v213", "v214", "v215", "v216", "v217", "v218", "v219", "v220", "v221", "v222", "v223", "v224",
          "v225", "v226", "v227", "v228", "v229", "v230", "v231", "v232", "v233", "v234", "v235", "v236", "v237", "v238", "v239", "v240",
          "v241", "v242", "v243", "v244", "v245", "v246", "v247", "v248", "v249", "v250", "v251", "v252", "v253", "v254", "v255", "s4",
          "s5", "s6", "s7", "s8", "s9", "s10", "s11", "s12", "s13", "s14", "s15", "s16", "s17", "s18", "s19", "s20",
          "s21", "s22", "s23", "s24", "s25", "s26", "s27", "s28", "s29", "s30", "s31", "s33", "s34", "s35", "s36", "s37",
          "s38", "s39", "s40", "s41", "s42", "s43", "s44", "s45", "s46", "s47", "s48", "s49", "s50", "s51", "s52", "s53",
          "s54", "s55", "s56", "s57", "s58", "s59", "s60", "s61", "s62", "s63", "s64", "s65", "s66", "s67", "s68", "s69",
          "s70", "s71", "s72", "s73", "s74", "s75", "s76", "s77", "s78", "s79", "s80", "s81", "s82", "s83", "s84", "s85",
          "s86", "s87", "s88", "s89", "s90", "s91", "s92", "s93", "s94", "s95", "s96", "s97", "s98", "s99", "vcc", "scc",
          "memory"
    );
    const uint16_t *x = kp->x, *dis = kp->dis, *wl = kp->wl, *wv = kp->wv, *wq = kp->wq, *wk = kp->wk;
    const int *src = kp->src, *dst = kp->dst, *esrc = kp->esrc, *edst = kp->edst;
    uint16_t* out = kp->out;
    const int N = kp->N, E = kp->E, Ee = kp->Ee;
    const int lane = __builtin_amdgcn_mbcnt_hi(-1, __builtin_amdgcn_mbcnt_lo(-1, 0));
    wave -= 4;
    const int tid = wave * 64 + lane;
    uint16_t* xs = reinterpret_cast<uint16_t*>(smem + 46080);
    uint16_t* qs = xs + TN * D;
    uint16_t* ag = qs + TN * D;
    uint32_t* lst = reinterpret_cast<uint32_t*>(smem + 24064);
    uint32_t* lc = lst + 1024;
    __syncthreads();
    for (int i = tid; i < TN * D; i += 256) xs[i] = tile * TN + i / D < N ? x[(size_t)tile * TN * D + i] : (uint16_t)0;
    __syncthreads();
    const int col = tid;
#pragma unroll 1
    for (int r = 0; r < TN; r += 4) {
        f32x4 a = {0.f, 0.f, 0.f, 0.f};
#pragma unroll 2
        for (int k = 0; k < D; ++k) {
            const float w = bf2f(wq[k * D + col]);
#pragma unroll
            for (int i = 0; i < 4; ++i) a[i] += w * bf2f(xs[(r + i) * D + k]);
        }
#pragma unroll
        for (int i = 0; i < 4; ++i) qs[(r + i) * D + col] = f2bf(a[i]);
    }
    __syncthreads();
#pragma unroll 1
    for (int r = 0; r < TN; r += 4) {
        f32x4 a = {0.f, 0.f, 0.f, 0.f};
#pragma unroll 2
        for (int k = 0; k < D; ++k) {
            const float w = bf2f(wk[col * D + k]);
#pragma unroll
            for (int i = 0; i < 4; ++i) a[i] += w * bf2f(qs[(r + i) * D + k]);
        }
#pragma unroll
        for (int i = 0; i < 4; ++i) xs[(r + i) * D + col] = f2bf(a[i] * 0.0625f);
    }
#pragma unroll 1
    for (int pass = 0; pass < 2; ++pass) {
        f32x4 ac[10];
        float den[10];
#pragma unroll
        for (int k = 0; k < 10; ++k) {
            ac[k] = f32x4{0.f, 0.f, 0.f, 0.f};
            den[k] = 0.f;
        }
        const int* S = pass ? esrc : src;
        const int* Dd = pass ? edst : dst;
        const int ne = pass ? Ee : E;
        for (int e0 = 0; e0 < ne; e0 += 256) {
            const int e = e0 + tid;
            const uint32_t nd = e < ne ? (uint32_t)(Dd[e] - tile * TN) : ~0u;
            const uint64_t m = __ballot(nd < (uint32_t)TN);
            __syncthreads();
            if (lane == 0) lc[wave] = (uint32_t)__popcll(m);
            __syncthreads();
            uint32_t p = (uint32_t)__popcll(m & ((1ull << lane) - 1ull)), cn = 0;
            for (int w = 0; w < 4; ++w) {
                p += w < wave ? lc[w] : 0u;
                cn += lc[w];
            }
            if (nd < (uint32_t)TN) {
                lst[2 * p] = (uint32_t)S[e];
                lst[2 * p + 1] = nd;
            }
            __syncthreads();
            for (uint32_t i = 0; i < cn; ++i) {
                const int k = __builtin_amdgcn_readfirstlane((int)lst[2 * i + 1]) - 10 * wave;
                if (k < 0 || k >= 10) continue;
                const uint32_t s = lst[2 * i];
                const f32x4 rv = wide(*reinterpret_cast<const u32x2*>(x + (size_t)s * D + lane * 4));
                float wgt;
                if (!pass) {
                    wgt = bf2f(dis[s]);
                } else {
                    const f32x4 t = rv * wide(*reinterpret_cast<const u32x2*>(xs + (10 * wave + k) * D + lane * 4));
                    float lg = (t[0] + t[1]) + (t[2] + t[3]);
                    for (int o = 1; o < 64; o <<= 1) lg += __shfl_xor(lg, o);
                    wgt = __expf(lg);
                }
#pragma unroll
                for (int kk = 0; kk < 10; ++kk) {
                    if (kk == k) {
                        ac[kk] += rv * wgt;
                        den[kk] += wgt;
                    }
                }
            }
        }
#pragma unroll
        for (int k = 0; k < 10; ++k) {
            const f32x4 o = ac[k] * (pass ? 1.f / (den[k] + 1e-9f) : bf2f(dis[min(tile * TN + 10 * wave + k, N - 1)]));
            *reinterpret_cast<u32x2*>(ag + (pass * TN + 10 * wave + k) * D + lane * 4) = u32x2{pack2(o[0], o[1]), pack2(o[2], o[3])};
        }
    }
    __syncthreads();
#pragma unroll 1
    for (int r = 0; r < TN; r += 4) {
        f32x4 a = {0.f, 0.f, 0.f, 0.f};
#pragma unroll 2
        for (int k = 0; k < 2 * D; ++k) {
            const float w = bf2f(k < D ? wl[k * D + col] : wv[(k - D) * D + col]);
#pragma unroll
            for (int i = 0; i < 4; ++i) a[i] += w * bf2f(ag[((k >> 8) * TN + r + i) * D + (k & 255)]);
        }
#pragma unroll
        for (int i = 0; i < 4; ++i)
            if (tile * TN + r + i < N) out[(size_t)(tile * TN + r + i) * D + col] = f2bf(a[i]);
    }
}

extern "C" void kernel_launch(void* const* d_in, const int* in_sizes, int n_in,
                              void* d_out, int out_size, void* d_ws, size_t ws_size,
                              hipStream_t stream) {
    const int N = in_sizes[0] / D;
    const int E = in_sizes[1];
    const int Ee = in_sizes[3];
    const int T = (N + TN - 1) / TN;
    if (T == 0) return;
    const size_t need = WS_BK + 4194304;
    int fast = T >= 192 && T <= 250 && (N & 1) == 0 && (E & 3) == 0 && E <= 262144 && Ee < (1 << 26) && ws_size >= need;
    if (fast && Ee == 4 * N) fast = 3;
    const uint32_t F = 0x00020000u, W = 2 * D * D;
    const uint32_t on = fast ? ~0u : 0u;
    void* stamps = d_out;
    (void)hipFuncSetAttribute((const void*)main_kernel, hipFuncAttributeMaxDynamicSharedMemorySize, L_TOT);
    main_kernel<<<fast ? 256 : T, 512, L_TOT, stream>>>(
        (const uint16_t*)d_in[0], ((uint32_t)N * 512u) & on, F, (const uint16_t*)d_in[5], ((uint32_t)N * 2u) & on, F,
        (const uint16_t*)d_in[6], W, F, (const uint16_t*)d_in[9], W, F, (const uint16_t*)d_in[7], W, F, (const uint16_t*)d_in[8], W, F,
        (const int*)d_in[1], ((uint32_t)E * 4u) & on, F, (const int*)d_in[2], ((uint32_t)E * 4u) & on, F,
        (const int*)d_in[3], ((uint32_t)Ee * 4u) & on, F, (const int*)d_in[4], ((uint32_t)Ee * 4u) & on, F,
        (char*)(fast ? d_ws : d_in[6]), (uint32_t)need & on, F, (uint16_t*)d_out, ((uint32_t)N * 512u) & on, F,
        N, E, Ee, fast, T, 0, stamps);
}
```
